```python
import torch

def _softmax(x, axis=-1):
    return torch.softmax(x, dim=axis)
def _gelu(x):
    return torch.nn.functional.gelu(x, approximate="tanh")
def _silu(x):
    return torch.nn.functional.silu(x)
def _sigmoid(x):
    return torch.sigmoid(x)
def _layernorm(x, axis=-1, eps=1e-5):
    m = x.mean(dim=axis, keepdim=True)
    v = x.var(dim=axis, keepdim=True, unbiased=False)
    return (x - m) / torch.sqrt(v + eps)
def _scatter_add(tgt, idx, src, axis=0):
    return tgt.clone().index_add_(axis, idx, src)
def _segment_sum(src, seg, n):
    out = torch.zeros((n,) + src.shape[1:], dtype=src.dtype)
    return out.index_add_(0, seg, src)
def _pad(x, pw):
    flat = tuple(v for pair in reversed(pw) for v in pair)
    return torch.nn.functional.pad(x, flat)

kernel_name = 'sequential_preLN__linear_attn_block__glu_gated_geglu__prompt'

def setup_inputs():

    _w = (lambda a: (lambda t: t.to(torch.bfloat16) if t.is_floating_point() else t)(torch.as_tensor(a)))
    def _A_setup():
        B, S, D = 1, 2048, 1024
        return {
            "x": _w((lambda *s: torch.randn(*s, dtype=torch.bfloat16) * 0.05)(B, S, D)),
            "wqkv": _w((lambda *s: torch.randn(*s, dtype=torch.bfloat16) * 0.05)(D, 3 * D)),
            "wo": _w((lambda *s: torch.randn(*s, dtype=torch.bfloat16) * 0.05)(D, D)),
        }
    _A_in = _A_setup()

    def _B_setup():
        B, S, Dm, Df = 1, 2048, 1024, 3584
        return {
            "x": _w((lambda *s: torch.randn(*s, dtype=torch.bfloat16) * 0.05)(B, S, Dm)),
            "w_gate": _w((lambda *s: torch.randn(*s, dtype=torch.bfloat16) * 0.4)(Dm, Df) * (Dm ** -0.5)),
            "w_up": _w((lambda *s: torch.randn(*s, dtype=torch.bfloat16) * 0.4)(Dm, Df) * (Dm ** -0.5)),
            "w_down": _w((lambda *s: torch.randn(*s, dtype=torch.bfloat16) * 0.4)(Df, Dm) * (Df ** -0.5)),
        }
    _B_in = _B_setup()

    return {
        "A_x": _A_in["x"],
        "A_wqkv": _A_in["wqkv"],
        "A_wo": _A_in["wo"],
        "B_x": _B_in["x"],
        "B_w_gate": _B_in["w_gate"],
        "B_w_up": _B_in["w_up"],
        "B_w_down": _B_in["w_down"],
    }

def reference(A_x, A_wqkv, A_wo, B_x, B_w_gate, B_w_up, B_w_down):
    def _A_ref(x, wqkv, wo):
        B, S, D = x.shape
        H = 32; Dh = D // H
        qkv = (lambda x, *d: x.permute(*d))((x @ wqkv).reshape(B, S, 3, H, Dh), 2, 0, 3, 1, 4)
        q, k, v = qkv[0], qkv[1], qkv[2]
        phi = lambda t: torch.where(t > 0, t + 1, torch.exp(t)) + 1e-6
        pq, pk = phi(q), phi(k)
        kv = torch.einsum("bhsd,bhse->bhde", pk, v)
        z = torch.einsum("bhsd->bhd", pk)
        num = torch.einsum("bhqd,bhde->bhqe", pq, kv)
        den = torch.einsum("bhqd,bhd->bhq", pq, z)[..., None] + 1e-6
        o = num / den
        return (lambda x, *d: x.permute(*d))(o, 0, 2, 1, 3).reshape(B, S, D) @ wo

    def _B_ref(x, w_gate, w_up, w_down):
        g = torch.einsum("bsd,df->bsf", x, w_gate)
        u = torch.einsum("bsd,df->bsf", x, w_up)
        h = _gelu(g) * u
        return torch.einsum("bsf,fd->bsd", h, w_down)

    x = A_x
    h = x + _A_ref(_layernorm(x, axis=-1), A_wqkv, A_wo)
    h2 = h + _B_ref(_layernorm(h, axis=-1), B_w_gate, B_w_up, B_w_down)
    return h2
```

```cpp
#include <hip/hip_runtime.h>
#include <hip/hip_bf16.h>
typedef unsigned short u16;
__device__ __forceinline__ float bf2f(u16 v){ return __uint_as_float(((unsigned)v)<<16); }
__device__ __forceinline__ u16 f2bf(float f){ unsigned u=__float_as_uint(f); u += 0x7FFFu + ((u>>16)&1u); return (u16)(u>>16); }
constexpr int S=2048, D=1024, H=32, DH=32, DF=3584;

__global__ void ln_k(const u16* x, float* xn){
  __shared__ float red[256];
  int r=blockIdx.x, t=threadIdx.x; float s=0;
  for(int i=t;i<D;i+=256) s+=bf2f(x[r*D+i]);
  red[t]=s; __syncthreads(); for(int o=128;o>0;o>>=1){ if(t<o) red[t]+=red[t+o]; __syncthreads(); }
  float m=red[0]/D; __syncthreads(); float q=0;
  for(int i=t;i<D;i+=256){ float d=bf2f(x[r*D+i])-m; q+=d*d; }
  red[t]=q; __syncthreads(); for(int o=128;o>0;o>>=1){ if(t<o) red[t]+=red[t+o]; __syncthreads(); }
  float rs=rsqrtf(red[0]/D+1e-5f);
  for(int i=t;i<D;i+=256) xn[r*D+i]=(bf2f(x[r*D+i])-m)*rs;
}
__global__ void gemm_k(const float* A, const u16* B, float* C, int M,int N,int K){
  __shared__ float sa[16][17], sb[16][17];
  int tx=threadIdx.x, ty=threadIdx.y; int row=blockIdx.y*16+ty, col=blockIdx.x*16+tx; float acc=0;
  for(int k0=0;k0<K;k0+=16){ sa[ty][tx]=A[(size_t)row*K+k0+tx]; sb[ty][tx]=bf2f(B[(size_t)(k0+ty)*N+col]); __syncthreads();
    for(int k=0;k<16;++k) acc+=sa[ty][k]*sb[k][tx]; __syncthreads(); }
  C[(size_t)row*N+col]=acc;
}
__device__ float phi(float t){ return (t>0? t+1.f : __expf(t)) + 1e-6f; }
__global__ void kv_k(const float* qkv, float* kv){
  int h=blockIdx.x; int t=threadIdx.x;
  int d=t>>5, e=t&31; float acc=0, z=0;
  for(int s=0;s<S;++s){ float pk=phi(qkv[(size_t)s*3*D + D + h*DH+d]); float v=qkv[(size_t)s*3*D+2*D+h*DH+e]; acc+=pk*v; z+=pk; }
  kv[h*1056+t]=acc; if(e==0) kv[h*1056+1024+d]=z;
}
__global__ void o_k(const float* qkv, const float* kv, float* o){
  int s=blockIdx.x; int t=threadIdx.x;
  int h=t>>5, e=t&31; float num=0, den=0;
  for(int d=0;d<32;++d){ float pq=phi(qkv[(size_t)s*3*D+h*DH+d]); num+=pq*kv[h*1056+d*32+e]; den+=pq*kv[h*1056+1024+d]; }
  o[(size_t)s*D+t]=num/(den+1e-6f);
}
__global__ void addres_k(const u16* x, const float* a, u16* h){ int i=blockIdx.x*256+threadIdx.x; h[i]=f2bf(bf2f(x[i])+a[i]); }
__global__ void geglu_k(const float* g, const float* u, float* a){ int i=blockIdx.x*256+threadIdx.x; float x=g[i]; float t=tanhf(0.7978845608028654f*(x+0.044715f*x*x*x)); a[i]=0.5f*x*(1.f+t)*u[i]; }

extern "C" void kernel_launch(void* const* d_in, const int* in_sizes, int n_in, void* d_out, int out_size, void* d_ws, size_t ws_size, hipStream_t stream){
  const u16* x=(const u16*)d_in[0]; const u16* wqkv=(const u16*)d_in[1]; const u16* wo=(const u16*)d_in[2];
  const u16* wg=(const u16*)d_in[4]; const u16* wu=(const u16*)d_in[5]; const u16* wd=(const u16*)d_in[6];
  u16* out=(u16*)d_out; char* ws=(char*)d_ws;
  float* xn=(float*)ws; ws+= (size_t)S*D*4;
  float* qkv=(float*)ws; ws+= (size_t)S*3*D*4;
  float* kv=(float*)ws; ws+= 1<<20;
  float* o=(float*)ws; ws+= (size_t)S*D*4;
  float* att=(float*)ws; ws+= (size_t)S*D*4;
  u16* h=(u16*)ws; ws+= (size_t)S*D*2;
  float* g=(float*)ws; ws+= (size_t)S*DF*4;
  float* u=(float*)ws; ws+= (size_t)S*DF*4;
  float* a=(float*)ws; ws+= (size_t)S*DF*4;
  ln_k<<<S,256,0,stream>>>(x,xn);
  gemm_k<<<dim3(3*D/16,S/16),dim3(16,16),0,stream>>>(xn,wqkv,qkv,S,3*D,D);
  kv_k<<<H,1024,0,stream>>>(qkv,kv);
  o_k<<<S,1024,0,stream>>>(qkv,kv,o);
  gemm_k<<<dim3(D/16,S/16),dim3(16,16),0,stream>>>(o,wo,att,S,D,D);
  addres_k<<<S*D/256,256,0,stream>>>(x,att,h);
  ln_k<<<S,256,0,stream>>>(h,xn);
  gemm_k<<<dim3(DF/16,S/16),dim3(16,16),0,stream>>>(xn,wg,g,S,DF,D);
  gemm_k<<<dim3(DF/16,S/16),dim3(16,16),0,stream>>>(xn,wu,u,S,DF,D);
  geglu_k<<<S*DF/256,256,0,stream>>>(g,u,a);
  gemm_k<<<dim3(D/16,S/16),dim3(16,16),0,stream>>>(a,wd,att,S,D,DF);
  addres_k<<<S*D/256,256,0,stream>>>(h,att,out);
}
```

```cpp
#include <hip/hip_runtime.h>
#include <hip/hip_bf16.h>
#include <stdint.h>

typedef unsigned short u16;
typedef short bf16x8 __attribute__((ext_vector_type(8)));
typedef short s16x4 __attribute__((ext_vector_type(4)));
typedef float f32x4 __attribute__((ext_vector_type(4)));
typedef float f32x2 __attribute__((ext_vector_type(2)));
typedef unsigned u32x4 __attribute__((ext_vector_type(4)));
typedef unsigned u32x2 __attribute__((ext_vector_type(2)));
typedef __bf16 bf16x2_t __attribute__((ext_vector_type(2)));
#define LAS __attribute__((address_space(3)))
#define DEV __device__ __forceinline__

constexpr int S_ = 2048, D_ = 1024, DF_ = 3584, NH_ = 32;
constexpr int KVP = 1056;

DEV float bf2f(u16 v) { return __uint_as_float(((unsigned)v) << 16); }
DEV float bflo(unsigned v) { return __uint_as_float(v << 16); }
DEV float bfhi(unsigned v) { return __uint_as_float(v & 0xffff0000u); }
DEV unsigned pk2(float lo, float hi) {
  bf16x2_t r; r.x = (__bf16)lo; r.y = (__bf16)hi; return __builtin_bit_cast(unsigned, r);
}
DEV float dot2(unsigned a, unsigned b, float c) {
  return __builtin_amdgcn_fdot2_f32_bf16(__builtin_bit_cast(bf16x2_t, a), __builtin_bit_cast(bf16x2_t, b), c, false);
}
template <int N> DEV void wait_vm() { asm volatile("s_waitcnt vmcnt(%0)" ::"n"(N) : "memory"); }
DEV void wait_lgkm0() { __builtin_amdgcn_s_waitcnt(0xC07F); }
DEV void bar() { __builtin_amdgcn_s_barrier(); }
DEV void sched_fence() { __builtin_amdgcn_sched_barrier(0); }
DEV void glds_v(const void* g, unsigned lds_addr) {
  asm volatile("s_mov_b32 m0, %1\n\ts_nop 0\n\tglobal_load_lds_dwordx4 %0, off" ::"v"(g), "s"(lds_addr) : "memory");
}
DEV void glds_vm(const void* g, unsigned lds_addr, unsigned long long mask) {
  asm volatile("s_mov_b32 m0, %1\n\ts_mov_b64 exec, %2\n\tglobal_load_lds_dwordx4 %0, off\n\ts_mov_b64 exec, -1"
               ::"v"(g), "s"(lds_addr), "s"(mask) : "memory");
}
DEV void glds_s(unsigned voff, const void* sbase, unsigned lds_addr) {
  asm volatile("s_mov_b32 m0, %2\n\ts_nop 0\n\tglobal_load_lds_dwordx4 %0, %1" ::"v"(voff), "s"(sbase), "s"(lds_addr) : "memory");
}

#define STAMP(kid, i)
template <int BM_, int NC_, int WM_, int WN_, int MT_, int NT_, int RA_, int RB_, int NS_, int P_, bool GU_,
          unsigned TRANS_, bool RSTAT_, int CSKIND_>
struct Cfg {
  static constexpr int BM = BM_, NC = NC_, WM = WM_, WN = WN_, MT = MT_, NT = NT_, RA = RA_, RB = RB_, NS = NS_, P = P_;
  static constexpr bool GU = GU_;
  static constexpr unsigned TRANS = TRANS_;
  static constexpr bool RSTAT = RSTAT_;
  static constexpr int CSKIND = CSKIND_;
  static constexpr unsigned CSMASK = CSKIND == 0 ? 0u : CSKIND == 1 ? 0x55555555u & ((1u << NT) - 1) : 0x3u;
  static constexpr int NW = WM * WN, NTHR = NW * 64;
  static constexpr int ATILE = BM * 128, BSLOT = 32 * NC * 16;
  static constexpr int NA = BM * 8 / NTHR;
  static constexpr int CPW = 32 * NC / NW;
  static constexpr int NB = (CPW + 63) / 64;
  static constexpr int LDS_B0 = RA * ATILE, LDS_BYTES = RA * ATILE + RB * BSLOT;
  static constexpr int NTA = NS / 2;
  static constexpr int MR = RSTAT ? MT / WN : 1;
  static constexpr int RQ1 = (MT * NT * 3) / 4, GQ1 = (MT * NT) / 2, SQ0 = 0;
  static_assert(BM * 8 % NTHR == 0, "A tile split");
  static_assert((32 * NC) % NW == 0, "B slot split");
  static_assert(P % 2 == 0 && P % RB == 0 && (P / 2) % RA == 0, "period");
  static_assert(BM == WM * MT * 16, "M tiling");
  static_assert(NC * 8 == WN * NT * 16, "N tiling");
  DEV static int mmap(int m, int wn) { return RSTAT ? (m + wn * (MT / WN)) % MT : m; }
  DEV static int nmap(int j, int wm) { return CSKIND == 1 ? (j ^ (wm & 1)) : CSKIND == 2 ? (j + 2 * wm) % NT : j; }
  DEV static constexpr int bswz(int k) {
    return NC == 28 ? ((k >> 3) & 1)
         : (NC % 16 == 8) ? 2 * (((k >> 1) & 1) | (((k >> 3) & 1) << 1))
                          : 2 * ((k & 3) | (((k >> 3) & 1) << 2));
  }
  static constexpr int vm_at(int s) {
    int posA[64] = {}, posB[128] = {};
    int tot = 0;
    for (int T = 0; T < 64; ++T) {
      if (T < RA && T < NTA) { tot += NA; posA[T] = tot; }
      if (2 * T < RB && 2 * T < NS) { tot += NB; posB[2 * T] = tot; }
      if (2 * T + 1 < RB && 2 * T + 1 < NS) { tot += NB; posB[2 * T + 1] = tot; }
    }
    for (int t = 0; t < s; ++t) {
      if (t + RB < NS) { tot += NB; posB[t + RB] = tot; }
      if ((t & 1) && ((t - 1) / 2 + RA < NTA)) { tot += NA; posA[(t - 1) / 2 + RA] = tot; }
    }
    int nb = s + 1;
    if (nb >= NS) return 63;
    int need = posB[nb] > posA[nb / 2] ? posB[nb] : posA[nb / 2];
    int v = tot - need;
    return v > 63 ? 63 : v;
  }
  static constexpr int nmain() {
    int lim = NS - RB < NS - 2 * RA + 1 ? NS - RB : NS - 2 * RA + 1;
    return (lim / P) * P;
  }
  static constexpr int NMAIN = nmain();
  static constexpr int vm_main(int u) {
    int v = 63;
    for (int s = u; s < NMAIN; s += P) { int x = vm_at(s); v = x < v ? x : v; }
    return v;
  }
};

template <class C> struct Loop {
  unsigned lbase;
  const char* abase;
  unsigned ga[C::NA];
  const char* gb[C::NB];
  long bstep;
  int wid, lane, wm, wn;
  unsigned ra[2][C::RSTAT ? C::MT : 1];
  unsigned rb[C::NT];
  bf16x8 FA[2][C::MT];
  s16x4 FBh[2][C::NT][2];
  f32x4 acc[C::MT][C::NT];
  float rs[C::MR], rq[C::MR], cs[C::NT];

  DEV void issueA(int slot) {
#pragma unroll
    for (int i = 0; i < C::NA; ++i) glds_s(ga[i], abase, lbase + slot * C::ATILE + (wid * C::NA + i) * 1024);
    abase += 128;
  }
  DEV void issueB(int slot) {
#pragma unroll
    for (int i = 0; i < C::NB; ++i) {
      const int rem = C::CPW - 64 * i;
      const unsigned dst = lbase + C::LDS_B0 + slot * C::BSLOT + (wid * C::CPW + i * 64) * 16;
      if (rem >= 64) glds_v(gb[i], dst);
      else glds_vm(gb[i], dst, (1ull << (rem & 63)) - 1ull);
      gb[i] += bstep;
    }
  }
  template <int SET, int I> DEV void readOp(int aslot, int ks, int bslot) {
    if constexpr (I < C::MT) {
      unsigned a = C::RSTAT ? ra[ks][C::RSTAT ? I : 0] : ra[ks][0] + I * 2048;
      FA[SET][I] = *(const LAS bf16x8*)(size_t)(a + aslot * C::ATILE);
    } else {
      constexpr int j = (I - C::MT) / 2, hf = (I - C::MT) & 1;
      unsigned a = rb[j] + bslot * C::BSLOT + hf * 4 * C::NC * 16;
      FBh[SET][j][hf] = __builtin_amdgcn_ds_read_tr16_b64_v4i16((LAS s16x4*)(size_t)a);
    }
  }
  template <int SET, int I> DEV void readAll(int aslot, int ks, int bslot) {
    if constexpr (I < C::MT + 2 * C::NT) { readOp<SET, I>(aslot, ks, bslot); readAll<SET, I + 1>(aslot, ks, bslot); }
  }
  template <int SET> DEV void readFrags(int aslot, int ks, int bslot) { readAll<SET, 0>(aslot, ks, bslot); }
  template <int SET> DEV bf16x8 FB(int j) const { return __builtin_shufflevector(FBh[SET][j][0], FBh[SET][j][1], 0, 1, 2, 3, 4, 5, 6, 7); }
  template <int SET, int Q> DEV void mmaOp() {
    constexpr int m = Q / C::NT, j = Q % C::NT;
    if constexpr ((C::TRANS >> j) & 1) acc[m][j] = __builtin_amdgcn_mfma_f32_16x16x32_bf16(FB<SET>(j), FA[SET][m], acc[m][j], 0, 0, 0);
    else acc[m][j] = __builtin_amdgcn_mfma_f32_16x16x32_bf16(FA[SET][m], FB<SET>(j), acc[m][j], 0, 0, 0);
  }
  static constexpr int NRS = C::RSTAT ? 4 * C::MR : 0;
  static constexpr int NCS = 2 * __builtin_popcount(C::CSMASK);
  static constexpr int csTile(int n) { int c = 0; for (int j = 0; j < C::NT; ++j) if ((C::CSMASK >> j) & 1) { if (c == n) return j; ++c; } return 0; }
  template <int SET, int I> DEV void statOp() {
    if constexpr (I < NRS) {
      constexpr int m = I / 4, i = I % 4;
      u32x4 v = __builtin_bit_cast(u32x4, FA[SET][m]);
      rs[m] = dot2(v[i], 0x3F803F80u, rs[m]); rq[m] = dot2(v[i], v[i], rq[m]);
    } else {
      constexpr int n = (I - NRS) / 2, hf = (I - NRS) & 1, j = csTile(n);
      u32x2 v = __builtin_bit_cast(u32x2, FBh[SET][j][hf]);
      cs[j] = dot2(v[0], 0x3F803F80u, cs[j]); cs[j] = dot2(v[1], 0x3F803F80u, cs[j]);
    }
  }
  template <int I> DEV void gldsOp(int bslot, int aslot) {
    if constexpr (I < C::NB) {
      constexpr int rem = C::CPW - 64 * I;
      const unsigned dst = lbase + C::LDS_B0 + bslot * C::BSLOT + (wid * C::CPW + I * 64) * 16;
      if constexpr (rem >= 64) glds_v(gb[I], dst);
      else glds_vm(gb[I], dst, (1ull << (rem & 63)) - 1ull);
      gb[I] += bstep;
    } else {
      constexpr int i = I - C::NB;
      glds_s(ga[i], abase, lbase + aslot * C::ATILE + (wid * C::NA + i) * 1024);
      if constexpr (i == C::NA - 1) abase += 128;
    }
  }
  static constexpr int NM = C::MT * C::NT, NR = C::MT + 2 * C::NT;
  static constexpr int spread(int N, int Q0, int Q1, int Q) { return Q <= Q0 ? 0 : Q >= Q1 ? N : (N * (Q - Q0) + (Q1 - Q0) - 1) / (Q1 - Q0); }
  template <int SET, int LO, int HI> DEV void readRange(int aslot, int ks, int bslot) {
    if constexpr (LO < HI) { readOp<SET, LO>(aslot, ks, bslot); readRange<SET, LO + 1, HI>(aslot, ks, bslot); }
  }
  template <int SET, int LO, int HI> DEV void statRange() {
    if constexpr (LO < HI) { statOp<SET, LO>(); statRange<SET, LO + 1, HI>(); }
  }
  template <int G0, int LO, int HI> DEV void gldsRange(int bslot, int aslot) {
    if constexpr (LO < HI) { gldsOp<G0 + LO>(bslot, aslot); gldsRange<G0, LO + 1, HI>(bslot, aslot); }
  }
  template <int U, bool IB, bool IA, bool RN, int Q> DEV void body() {
    if constexpr (Q < NM) {
      constexpr int PAR = U & 1;
      constexpr int G0 = IB ? 0 : C::NB, G1 = (IA && PAR) ? C::NB + C::NA : C::NB, NG = G1 > G0 ? G1 - G0 : 0;
      constexpr int RQ1 = C::RQ1, GQ1 = C::GQ1;
      mmaOp<PAR, Q>();
      gldsRange<G0, spread(NG, 0, GQ1, Q), spread(NG, 0, GQ1, Q + 1)>(U % C::RB, ((U - 1) / 2 + C::RA) % C::RA);
      if constexpr (RN) readRange<PAR ^ 1, spread(NR, 0, RQ1, Q), spread(NR, 0, RQ1, Q + 1)>(((U + 1) / 2) % C::RA, PAR ^ 1, (U + 1) % C::RB);
      statRange<PAR, spread(NRS + NCS, C::SQ0, NM, Q), spread(NRS + NCS, C::SQ0, NM, Q + 1)>();
      sched_fence();
      body<U, IB, IA, RN, Q + 1>();
    }
  }
  template <int U, int VM, bool IB, bool IA, bool RN> DEV void step() {
    if constexpr (RN) { wait_vm<VM>(); bar(); }
    sched_fence();
    body<U, IB, IA, RN, 0>();
    wait_lgkm0();
    sched_fence();
  }
  template <int U> DEV void mainSteps() {
    if constexpr (U < C::P) { step<U, C::vm_main(U), true, true, true>(); mainSteps<U + 1>(); }
  }
  template <int Sx> DEV void tailSteps() {
    if constexpr (Sx < C::NS) {
      step<Sx % C::P, C::vm_at(Sx), (Sx + C::RB < C::NS), ((Sx - 1) / 2 + C::RA < C::NTA), (Sx + 1 < C::NS)>();
      tailSteps<Sx + 1>();
    }
  }
  template <class BF> DEV void init(LAS char* l, const char* Abase, int lda, BF bsrc, long bstep_) {
    lbase = (unsigned)(size_t)l;
    const int tid = threadIdx.x;
    wid = __builtin_amdgcn_readfirstlane(tid >> 6); lane = tid & 63;
    wm = wid / C::WN; wn = wid % C::WN;
    bstep = bstep_;
    abase = Abase;
#pragma unroll
    for (int i = 0; i < C::NA; ++i) {
      int q = (wid * C::NA + i) * 64 + lane, row = q >> 3, p = q & 7, c = p ^ ((row >> 1) & 7);
      ga[i] = row * lda + c * 16;
    }
#pragma unroll
    for (int i = 0; i < C::NB; ++i) {
      int q = wid * C::CPW + i * 64 + lane;
      if (q >= 32 * C::NC) q = 32 * C::NC - 1;
      int k = q / C::NC, p = q % C::NC, idx = p ^ C::bswz(k);
      gb[i] = bsrc(k, idx);
    }
    const int fr = lane & 15, g = lane >> 4;
#pragma unroll
    for (int ks = 0; ks < 2; ++ks)
#pragma unroll
      for (int m = 0; m < (C::RSTAT ? C::MT : 1); ++m)
        ra[ks][m] = lbase + ((wm * C::MT + C::mmap(m, wn)) * 16 + fr) * 128 + (((4 * ks + g) ^ (fr >> 1)) * 16);
    const int q4 = fr >> 2, p4 = fr & 3, k = 8 * g + q4;
#pragma unroll
    for (int j = 0; j < C::NT; ++j) {
      const int ja = wn * C::NT + C::nmap(j, wm);
      const int idx = C::GU ? (p4 >> 1) * (C::NC / 2) + ja : 2 * ja + (p4 >> 1);
      rb[j] = lbase + C::LDS_B0 + k * C::NC * 16 + ((idx ^ C::bswz(k)) * 16) + 8 * (p4 & 1);
    }
#pragma unroll
    for (int m = 0; m < C::MT; ++m)
#pragma unroll
      for (int j = 0; j < C::NT; ++j) acc[m][j] = (f32x4){0.f, 0.f, 0.f, 0.f};
#pragma unroll
    for (int m = 0; m < C::MR; ++m) { rs[m] = 0.f; rq[m] = 0.f; }
#pragma unroll
    for (int j = 0; j < C::NT; ++j) cs[j] = 0.f;
  }
  template <int KID> DEV void run() {
#pragma unroll
    for (int T = 0; T < 8; ++T) {
      if (T < C::RA) issueA(T);
      if (2 * T < C::RB) issueB(2 * T);
      if (2 * T + 1 < C::RB) issueB(2 * T + 1);
    }
    wait_vm<C::vm_at(-1)>();
    bar();
    sched_fence();
    readFrags<0>(0, 0, 0);
    wait_lgkm0();
    sched_fence();
    STAMP(KID, 1);
    for (int it = 0; it < C::NMAIN / C::P; ++it) mainSteps<0>();
    tailSteps<C::NMAIN>();
    wait_vm<0>();
    bar();
    sched_fence();
    STAMP(KID, 2);
  }
};

DEV float phi(float t) { return (t > 0.f ? t + 1.f : __expf(t)) + 1e-6f; }

using C1 = Cfg<128, 24, 4, 2, 2, 6, 2, 4, 32, 4, false, 0x03u, true, 1>;
__global__ __launch_bounds__(C1::NTHR, 2) void k1_qkv(const u16* __restrict__ x, const u16* __restrict__ wqkv,
                                                      u16* __restrict__ pq, float* __restrict__ kvp) {
  extern __shared__ __attribute__((aligned(16))) char smem[];
  LAS char* lds = (LAS char*)smem;
  const int bid = blockIdx.x, xcd = bid & 7, idx = bid >> 3;
  const int rb = (xcd & 1) * 8 + (idx & 7), hp = (xcd >> 1) * 4 + (idx >> 3);
  STAMP(0, 0);
  Loop<C1> L;
  auto bsrc = [&](int k, int c) -> const char* {
    int hh = c / 12, r = c % 12, sec = r >> 2, cc = r & 3;
    return (const char*)(wqkv + (long)k * (3 * D_) + sec * D_ + (hp * 2 + hh) * 32 + cc * 8);
  };
  L.init(lds, (const char*)(x + (long)rb * 128 * D_), D_ * 2, bsrc, 32L * 3 * D_ * 2);
  L.run<0>();
  const int lane = L.lane, fr = lane & 15, g = lane >> 4, wm = L.wm, wn = L.wn, wid = L.wid;
  LAS float* sR = (LAS float*)lds;
  LAS float* sC = (LAS float*)(lds + 2048);
  {
    float a = L.rs[0], b = L.rq[0];
    a += __shfl_xor(a, 16); a += __shfl_xor(a, 32);
    b += __shfl_xor(b, 16); b += __shfl_xor(b, 32);
    const int row = wm * 32 + C1::mmap(0, wn) * 16 + fr;
    if (g == 0) {
      float mean = a * (1.f / D_);
      float var = fmaxf(b * (1.f / D_) - mean * mean, 0.f);
      sR[row * 2] = mean; sR[row * 2 + 1] = rsqrtf(var + 1e-5f);
    }
  }
#pragma unroll
  for (int j = 0; j < 6; j += 2) {
    float a = L.cs[j];
    a += __shfl_xor(a, 16); a += __shfl_xor(a, 32);
    if (g == 0 && wm < 2) sC[wn * 96 + C1::nmap(j, wm) * 16 + fr] = a;
  }
  __syncthreads();
  auto rowstat = [&](int row, float& mean, float& rstd) { mean = sR[row * 2]; rstd = sR[row * 2 + 1]; };
  auto colsum = [&](int col) { return sC[col]; };
  const int head = hp * 2 + wn;
#pragma unroll
  for (int m = 0; m < 2; ++m) {
    const int lr = wm * 32 + C1::mmap(m, wn) * 16 + fr;
    float mean, rstd; rowstat(lr, mean, rstd);
    const long row = (long)rb * 128 + lr;
#pragma unroll
    for (int j = 0; j < 2; ++j) {
      const int ja = C1::nmap(j, wm);
      float v[4];
#pragma unroll
      for (int r = 0; r < 4; ++r) v[r] = phi(rstd * (L.acc[m][j][r] - mean * colsum(wn * 96 + ja * 16 + 4 * g + r)));
      u32x2 w; w.x = pk2(v[0], v[1]); w.y = pk2(v[2], v[3]);
      *(u32x2*)(pq + row * D_ + head * 32 + ja * 16 + 4 * g) = w;
    }
  }
  bf16x8 fk[2], fv[2];
  {
    float mean[2][4], rstd[2][4];
#pragma unroll
    for (int m = 0; m < 2; ++m)
#pragma unroll
      for (int r = 0; r < 4; ++r) rowstat(wm * 32 + C1::mmap(m, wn) * 16 + 4 * g + r, mean[m][r], rstd[m][r]);
#pragma unroll
    for (int t = 0; t < 2; ++t) {
      float ck = colsum(wn * 96 + C1::nmap(2 + t, wm) * 16 + fr), cv = colsum(wn * 96 + C1::nmap(4 + t, wm) * 16 + fr);
      u32x4 a, b;
#pragma unroll
      for (int m = 0; m < 2; ++m) {
        float k[4], v[4];
#pragma unroll
        for (int r = 0; r < 4; ++r) {
          k[r] = phi(rstd[m][r] * (L.acc[m][2 + t][r] - mean[m][r] * ck));
          v[r] = rstd[m][r] * (L.acc[m][4 + t][r] - mean[m][r] * cv);
        }
        a[2 * m] = pk2(k[0], k[1]); a[2 * m + 1] = pk2(k[2], k[3]);
        b[2 * m] = pk2(v[0], v[1]); b[2 * m + 1] = pk2(v[2], v[3]);
      }
      fk[t] = __builtin_bit_cast(bf16x8, a); fv[t] = __builtin_bit_cast(bf16x8, b);
    }
  }
  const u32x4 ones4 = {0x3F803F80u, 0x3F803F80u, 0x3F803F80u, 0x3F803F80u};
  const bf16x8 ones = __builtin_bit_cast(bf16x8, ones4);
  LAS float* sP = (LAS float*)(lds + 8192);
  const f32x4 z4 = {0.f, 0.f, 0.f, 0.f};
#pragma unroll
  for (int dn = 0; dn < 2; ++dn) {
#pragma unroll
    for (int en = 0; en < 2; ++en) {
      f32x4 d = __builtin_amdgcn_mfma_f32_16x16x32_bf16(fk[dn], fv[en], z4, 0, 0, 0);
#pragma unroll
      for (int r = 0; r < 4; ++r) sP[wid * KVP + ((dn ^ (wm & 1)) * 16 + 4 * g + r) * 32 + (en ^ (wm & 1)) * 16 + fr] = d[r];
    }
    f32x4 zz = __builtin_amdgcn_mfma_f32_16x16x32_bf16(fk[dn], ones, z4, 0, 0, 0);
    if (fr == 0) {
#pragma unroll
      for (int r = 0; r < 4; ++r) sP[wid * KVP + 1024 + (dn ^ (wm & 1)) * 16 + 4 * g + r] = zz[r];
    }
  }
  __syncthreads();
  for (int i = threadIdx.x; i < 2 * KVP; i += C1::NTHR) {
    int hh = i / KVP, e = i % KVP;
    float s = (sP[(0 * 2 + hh) * KVP + e] + sP[(1 * 2 + hh) * KVP + e]) + (sP[(2 * 2 + hh) * KVP + e] + sP[(3 * 2 + hh) * KVP + e]);
    kvp[((long)(hp * 2 + hh) * 16 + rb) * KVP + e] = s;
  }
  STAMP(0, 3);
}

__global__ __launch_bounds__(256) void k15_o(const u16* __restrict__ pq, const float* __restrict__ kvp, u16* __restrict__ o) {
  __shared__ __attribute__((aligned(16))) float red[KVP];
  __shared__ __attribute__((aligned(16))) u16 kvT[48 * 40];
  const int head = blockIdx.x & 31, rb = blockIdx.x >> 5;
  const int tid = threadIdx.x, lane = tid & 63, wid = tid >> 6, fr = lane & 15, g = lane >> 4;
  bf16x8 fq[4];
#pragma unroll
  for (int m = 0; m < 4; ++m)
    fq[m] = *(const bf16x8*)(pq + ((long)rb * 256 + wid * 64 + m * 16 + fr) * D_ + head * 32 + g * 8);
  for (int i = tid; i < KVP / 4; i += 256) {
    f32x4 s = {0.f, 0.f, 0.f, 0.f};
    const f32x4* p = (const f32x4*)(kvp + (long)head * 16 * KVP) + i;
    f32x4 v[16];
#pragma unroll
    for (int b = 0; b < 16; ++b) v[b] = p[b * (KVP / 4)];
#pragma unroll
    for (int b = 0; b < 16; ++b) s += v[b];
    *(f32x4*)(red + i * 4) = s;
  }
  __syncthreads();
  for (int i = tid; i < 48 * 32; i += 256) {
    int e = i >> 5, d = i & 31;
    float v = e < 32 ? red[d * 32 + e] : red[1024 + d];
    kvT[e * 40 + d] = (u16)(pk2(v, 0.f) & 0xffff);
  }
  __syncthreads();
  bf16x8 fa[3];
#pragma unroll
  for (int t = 0; t < 3; ++t) fa[t] = *(const bf16x8*)(kvT + (t * 16 + fr) * 40 + g * 8);
  const f32x4 z4 = {0.f, 0.f, 0.f, 0.f};
#pragma unroll
  for (int m = 0; m < 4; ++m) {
    f32x4 den = __builtin_amdgcn_mfma_f32_16x16x32_bf16(fa[2], fq[m], z4, 0, 0, 0);
    float inv = 1.f / (den[0] + 1e-6f);
    const long row = (long)rb * 256 + wid * 64 + m * 16 + fr;
#pragma unroll
    for (int t = 0; t < 2; ++t) {
      f32x4 n = __builtin_amdgcn_mfma_f32_16x16x32_bf16(fa[t], fq[m], z4, 0, 0, 0);
      u32x2 w; w.x = pk2(n[0] * inv, n[1] * inv); w.y = pk2(n[2] * inv, n[3] * inv);
      *(u32x2*)(o + row * D_ + head * 32 + t * 16 + 4 * g) = w;
    }
  }
}

using C2 = Cfg<128, 8, 2, 2, 4, 2, 4, 8, 32, 8, false, 0x3u, false, 0>;
__global__ __launch_bounds__(C2::NTHR, 1) void k2_wo(const u16* __restrict__ o, const u16* __restrict__ wo, const u16* __restrict__ x,
                                                     u16* __restrict__ h, float* __restrict__ hstat) {
  extern __shared__ __attribute__((aligned(16))) char smem[];
  LAS char* lds = (LAS char*)smem;
  const int bid = blockIdx.x, xcd = bid & 7, idx = bid >> 3;
  const int rb = (xcd & 3) * 4 + (idx & 3), ct = (xcd >> 2) * 8 + (idx >> 2);
  STAMP(2, 0);
  Loop<C2> L;
  auto bsrc = [&](int k, int c) -> const char* { return (const char*)(wo + (long)k * D_ + ct * 64 + c * 8); };
  L.init(lds, (const char*)(o + (long)rb * 128 * D_), D_ * 2, bsrc, 32L * D_ * 2);
  L.run<2>();
  const int lane = L.lane, fr = lane & 15, g = lane >> 4, wm = L.wm, wn = L.wn;
  LAS float* sS = (LAS float*)lds;
#pragma unroll
  for (int m = 0; m < 4; ++m) {
    const int lr = wm * 64 + m * 16 + fr;
    const long row = (long)rb * 128 + lr;
    float s = 0.f, q = 0.f;
#pragma unroll
    for (int j = 0; j < 2; ++j) {
      const long off = row * D_ + ct * 64 + wn * 32 + j * 16 + 4 * g;
      u32x2 xr = *(const u32x2*)(x + off);
      u32x2 w;
      w.x = pk2(bflo(xr.x) + L.acc[m][j][0], bfhi(xr.x) + L.acc[m][j][1]);
      w.y = pk2(bflo(xr.y) + L.acc[m][j][2], bfhi(xr.y) + L.acc[m][j][3]);
      *(u32x2*)(h + off) = w;
      s = dot2(w.x, 0x3F803F80u, s); s = dot2(w.y, 0x3F803F80u, s);
      q = dot2(w.x, w.x, q); q = dot2(w.y, w.y, q);
    }
    s += __shfl_xor(s, 16); s += __shfl_xor(s, 32);
    q += __shfl_xor(q, 16); q += __shfl_xor(q, 32);
    if (g == 0) { sS[(wn * 128 + lr) * 2] = s; sS[(wn * 128 + lr) * 2 + 1] = q; }
  }
  __syncthreads();
  if (threadIdx.x < 128) {
    const int r = threadIdx.x;
    f32x2 v; v.x = sS[r * 2] + sS[(128 + r) * 2]; v.y = sS[r * 2 + 1] + sS[(128 + r) * 2 + 1];
    *(f32x2*)(hstat + (((long)rb * 128 + r) * 16 + ct) * 2) = v;
  }
  STAMP(2, 3);
}

using C3 = Cfg<256, 28, 4, 2, 4, 7, 2, 4, 32, 4, true, 0x7Fu, false, 2>;
DEV float gelu_t(float x) {
  float u = x * (1.f + 0.044715f * x * x);
  float e = __builtin_amdgcn_exp2f(u * (-2.f * 0.7978845608028654f * 1.4426950408889634f));
  return x * __builtin_amdgcn_rcpf(1.f + e);
}
__global__ __launch_bounds__(C3::NTHR, 2) void k3_glu(const u16* __restrict__ h, const u16* __restrict__ wg, const u16* __restrict__ wu,
                                                      const float* __restrict__ hstat, u16* __restrict__ act) {
  extern __shared__ __attribute__((aligned(16))) char smem[];
  LAS char* lds = (LAS char*)smem;
  const int bid = blockIdx.x, xcd = bid & 7, idx = bid >> 3;
  const int rb = (xcd & 1) * 4 + (idx & 3), ct = (xcd >> 1) * 8 + (idx >> 2);
  STAMP(3, 0);
  Loop<C3> L;
  auto bsrc = [&](int k, int c) -> const char* {
    const u16* w = c < 14 ? wg : wu; int cc = c < 14 ? c : c - 14;
    return (const char*)(w + (long)k * DF_ + ct * 112 + cc * 8);
  };
  L.init(lds, (const char*)(h + (long)rb * 256 * D_), D_ * 2, bsrc, 32L * DF_ * 2);
  L.run<3>();
  const int lane = L.lane, fr = lane & 15, g = lane >> 4, wm = L.wm, wn = L.wn;
  LAS float* sC = (LAS float*)lds;
  LAS float* sR = (LAS float*)(lds + 4096);
#pragma unroll
  for (int j = 0; j < 2; ++j) {
    float a = L.cs[j];
    a += __shfl_xor(a, 16); a += __shfl_xor(a, 32);
    if (g == 0 && 2 * wm + j < 7) sC[(wn * 7 + 2 * wm + j) * 16 + fr] = a;
  }
  if (threadIdx.x < 256) {
    const f32x4* p = (const f32x4*)(hstat + ((long)rb * 256 + threadIdx.x) * 32);
    float s = 0.f, q = 0.f;
#pragma unroll
    for (int i = 0; i < 8; ++i) { f32x4 v = p[i]; s += v[0] + v[2]; q += v[1] + v[3]; }
    float mean = s * (1.f / D_);
    float var = fmaxf(q * (1.f / D_) - mean * mean, 0.f);
    sR[threadIdx.x * 2] = mean; sR[threadIdx.x * 2 + 1] = rsqrtf(var + 1e-5f);
  }
  __syncthreads();
  const int hi = lane >> 5, g1 = g & 1;
#pragma unroll
  for (int j = 0; j < 7; ++j) {
    const int ja = wn * 7 + C3::nmap(j, wm);
    const f32x4 cg = *(const LAS f32x4*)(sC + ja * 16 + 4 * g1), cu = *(const LAS f32x4*)(sC + ja * 16 + 8 + 4 * g1);
#pragma unroll
    for (int mp = 0; mp < 2; ++mp) {
      const int lr = wm * 64 + (2 * mp + hi) * 16 + fr;
      const float mean = sR[lr * 2], rstd = sR[lr * 2 + 1];
      float v[4];
#pragma unroll
      for (int r = 0; r < 4; ++r) {
        unsigned X = __float_as_uint(L.acc[2 * mp][j][r]), Y = __float_as_uint(L.acc[2 * mp + 1][j][r]);
        auto sw = __builtin_amdgcn_permlane32_swap(X, Y, false, false);
        float gg = __uint_as_float(sw[0]), uu = __uint_as_float(sw[1]);
        gg = rstd * (gg - mean * cg[r]); uu = rstd * (uu - mean * cu[r]);
        v[r] = gelu_t(gg) * uu;
      }
      u32x2 w; w.x = pk2(v[0], v[1]); w.y = pk2(v[2], v[3]);
      *(u32x2*)(act + ((long)rb * 256 + lr) * DF_ + ct * 112 + ja * 8 + 4 * g1) = w;
    }
  }
  STAMP(3, 3);
}

using C4 = Cfg<128, 8, 2, 2, 4, 2, 4, 8, 112, 8, false, 0x3u, false, 0>;
__global__ __launch_bounds__(C4::NTHR, 1) void k4_down(const u16* __restrict__ act, const u16* __restrict__ wd, const u16* __restrict__ h,
                                                       u16* __restrict__ out) {
  extern __shared__ __attribute__((aligned(16))) char smem[];
  LAS char* lds = (LAS char*)smem;
  const int bid = blockIdx.x, xcd = bid & 7, idx = bid >> 3;
  const int rb = (xcd & 3) * 4 + (idx & 3), ct = (xcd >> 2) * 8 + (idx >> 2);
  STAMP(4, 0);
  Loop<C4> L;
  auto bsrc = [&](int k, int c) -> const char* { return (const char*)(wd + (long)k * D_ + ct * 64 + c * 8); };
  L.init(lds, (const char*)(act + (long)rb * 128 * DF_), DF_ * 2, bsrc, 32L * D_ * 2);
  L.run<4>();
  const int lane = L.lane, fr = lane & 15, g = lane >> 4, wm = L.wm, wn = L.wn;
#pragma unroll
  for (int m = 0; m < 4; ++m) {
    const long row = (long)rb * 128 + wm * 64 + m * 16 + fr;
#pragma unroll
    for (int j = 0; j < 2; ++j) {
      const long off = row * D_ + ct * 64 + wn * 32 + j * 16 + 4 * g;
      u32x2 xr = *(const u32x2*)(h + off);
      u32x2 w;
      w.x = pk2(bflo(xr.x) + L.acc[m][j][0], bfhi(xr.x) + L.acc[m][j][1]);
      w.y = pk2(bflo(xr.y) + L.acc[m][j][2], bfhi(xr.y) + L.acc[m][j][3]);
      *(u32x2*)(out + off) = w;
    }
  }
  STAMP(4, 3);
}

extern "C" void kernel_launch(void* const* d_in, const int* in_sizes, int n_in, void* d_out, int out_size, void* d_ws,
                              size_t ws_size, hipStream_t stream) {
  const u16* x = (const u16*)d_in[0];
  const u16* wqkv = (const u16*)d_in[1];
  const u16* wo = (const u16*)d_in[2];
  const u16* wg = (const u16*)d_in[4];
  const u16* wu = (const u16*)d_in[5];
  const u16* wd = (const u16*)d_in[6];
  u16* out = (u16*)d_out;
  char* ws = (char*)d_ws;
  u16* pq = (u16*)ws;                      ws += (size_t)S_ * D_ * 2;
  u16* o = (u16*)ws;                       ws += (size_t)S_ * D_ * 2;
  u16* h = (u16*)ws;                       ws += (size_t)S_ * D_ * 2;
  u16* act = (u16*)ws;                     ws += (size_t)S_ * DF_ * 2;
  float* kvp = (float*)ws;                 ws += (size_t)NH_ * 16 * KVP * 4;
  float* hstat = (float*)ws;               ws += (size_t)S_ * 16 * 2 * 4;
  (void)hipFuncSetAttribute((const void*)k1_qkv, hipFuncAttributeMaxDynamicSharedMemorySize, C1::LDS_BYTES);
  (void)hipFuncSetAttribute((const void*)k2_wo, hipFuncAttributeMaxDynamicSharedMemorySize, C2::LDS_BYTES);
  (void)hipFuncSetAttribute((const void*)k3_glu, hipFuncAttributeMaxDynamicSharedMemorySize, C3::LDS_BYTES);
  (void)hipFuncSetAttribute((const void*)k4_down, hipFuncAttributeMaxDynamicSharedMemorySize, C4::LDS_BYTES);
  k1_qkv<<<256, C1::NTHR, C1::LDS_BYTES, stream>>>(x, wqkv, pq, kvp);
  k15_o<<<256, 256, 0, stream>>>(pq, kvp, o);
  k2_wo<<<256, C2::NTHR, C2::LDS_BYTES, stream>>>(o, wo, x, h, hstat);
  k3_glu<<<256, C3::NTHR, C3::LDS_BYTES, stream>>>(h, wg, wu, hstat, act);
  k4_down<<<256, C4::NTHR, C4::LDS_BYTES, stream>>>(act, wd, h, out);
}
```

```cpp
#include <hip/hip_runtime.h>
#include <hip/hip_bf16.h>
#include <stdint.h>

typedef unsigned short u16;
typedef short bf16x8 __attribute__((ext_vector_type(8)));
typedef short s16x4 __attribute__((ext_vector_type(4)));
typedef float f32x4 __attribute__((ext_vector_type(4)));
typedef float f32x2 __attribute__((ext_vector_type(2)));
typedef unsigned u32x4 __attribute__((ext_vector_type(4)));
typedef unsigned u32x2 __attribute__((ext_vector_type(2)));
typedef __bf16 bf16x2_t __attribute__((ext_vector_type(2)));
#define LAS __attribute__((address_space(3)))
#define DEV __device__ __forceinline__

constexpr int S_ = 2048, D_ = 1024, DF_ = 3584, NH_ = 32;
constexpr int KVP = 1056;

DEV float bflo(unsigned v) { return __uint_as_float(v << 16); }
DEV float bfhi(unsigned v) { return __uint_as_float(v & 0xffff0000u); }
DEV unsigned pk2(float lo, float hi) {
  bf16x2_t r; r.x = (__bf16)lo; r.y = (__bf16)hi; return __builtin_bit_cast(unsigned, r);
}
template <int N> DEV void wait_vm() { asm volatile("s_waitcnt vmcnt(%0)" ::"n"(N) : "memory"); }
DEV void wait_lgkm0() { __builtin_amdgcn_s_waitcnt(0xC07F); }
DEV void bar() { __builtin_amdgcn_s_barrier(); }
DEV void sched_fence() { __builtin_amdgcn_sched_barrier(0); }
DEV void glds_v(const void* g, unsigned lds_addr) {
  asm volatile("s_mov_b32 m0, %1\n\ts_nop 0\n\tglobal_load_lds_dwordx4 %0, off" ::"v"(g), "s"(lds_addr) : "memory");
}
DEV void glds_vm(const void* g, unsigned lds_addr, unsigned long long mask) {
  asm volatile("s_mov_b32 m0, %1\n\ts_mov_b64 exec, %2\n\tglobal_load_lds_dwordx4 %0, off\n\ts_mov_b64 exec, -1"
               ::"v"(g), "s"(lds_addr), "s"(mask) : "memory");
}
DEV void glds_s(unsigned voff, const void* sbase, unsigned lds_addr) {
  asm volatile("s_mov_b32 m0, %2\n\ts_nop 0\n\tglobal_load_lds_dwordx4 %0, %1" ::"v"(voff), "s"(sbase), "s"(lds_addr) : "memory");
}
DEV f32x4 mfma16(bf16x8 a, bf16x8 b, f32x4 c) { return __builtin_amdgcn_mfma_f32_16x16x32_bf16(a, b, c, 0, 0, 0); }

#define STAMP(kid, i)
template <int BM_, int NC_, int WM_, int WN_, int MT_, int NT_, int RA_, int RB_, int KB_, int NS_, int P_, int KIND_,
          unsigned TRANS_, int NX_, int NPF_ = 0>
struct Cfg {
  static constexpr int BM = BM_, NC = NC_, WM = WM_, WN = WN_, MT = MT_, NT = NT_, RA = RA_, RB = RB_, KB = KB_, NS = NS_, P = P_;
  static constexpr int KIND = KIND_;
  static constexpr unsigned TRANS = TRANS_;
  static constexpr int NX = NX_;
  static constexpr int NPF = NPF_;
  static constexpr bool pfAt(int t) { return t >= 0 && t < 4 * NPF && t % 4 == 0; }
  static constexpr bool STATS = KIND == 1;
  static constexpr int NW = WM * WN, NTHR = NW * 64;
  static constexpr int ATILE = BM * 128, BSLOT = KB * NC * 16;
  static constexpr int NA = BM * 8 / NTHR;
  static constexpr int CPW = KB * NC / NW;
  static constexpr int NB = (CPW + 63) / 64;
  static constexpr int NA1 = KB == 64 ? NA / 2 : (NA + 1) / 2, NB1 = KB == 64 ? (NB + 1) / 2 : NB;
  static constexpr int LDS_B0 = RA * ATILE, LDS_RING = RA * ATILE + RB * BSLOT, LDS_BYTES = LDS_RING + (NPF ? NW * 512 : 0);
  static constexpr int NTA = NS / 2;
#define RQN 3
#define RQD 4
  static constexpr int RQ1 = (MT * NT * RQN) / RQD;
  static_assert(BM * 8 % NTHR == 0, "A tile split");
  static_assert((KB * NC) % NW == 0, "B slot split");
  static_assert(P % 2 == 0 && (P / 2) % RA == 0 && (KB == 32 ? P % RB == 0 : (P / 2) % RB == 0), "period");
  static_assert(BM == WM * MT * 16, "M tiling");
  static_assert(NC * 8 == WN * NT * 16, "N tiling");
  DEV static int mmap(int m, int wn) { return STATS ? (m ^ wn) : m; }
  DEV static int nmap(int j, int wm) { return STATS ? (j ^ (wm & 1)) : j; }
  DEV static constexpr int bswz(int k) {
    return NC == 28 ? ((k >> 3) & 1)
         : (NC % 16 == 8) ? 2 * (((k >> 1) & 1) | (((k >> 3) & 1) << 1))
                          : 2 * ((k & 3) | (((k >> 3) & 1) << 2));
  }
  static constexpr bool hasB(int t) {
    if (KB == 32) return t + RB >= 0 && t + RB < NS;
    int T = ((t + 64) & 1) ? (t - 1) / 2 : 0; (void)T;
    int tile = (t & 1) ? ((t - 1) >> 1) + RB : ((t - 2) >> 1) + RB;
    return tile >= 0 && tile < NTA;
  }
  static constexpr bool hasA(int t) {
    int tile = (t & 1) ? ((t - 1) >> 1) + RA : ((t - 2) >> 1) + RA;
    return tile >= 0 && tile < NTA;
  }
  static constexpr int nB(int t) { return !hasB(t) ? 0 : KB == 32 ? NB : (t & 1) ? NB1 : NB - NB1; }
  static constexpr int nA(int t) { return !hasA(t) ? 0 : (t & 1) ? NA1 : NA - NA1; }
  static constexpr int T0 = -(2 * (RA > RB ? RA : RB) + 2);
  static constexpr int lastIssue() { int l = 0; for (int t = 0; t < NS; ++t) if (nA(t) + nB(t) > 0) l = t; return l; }
  static constexpr int XSTEP = KIND == 2 ? NS - 1 : lastIssue() + 1;
  static constexpr int nmain() {
    int lim = 0;
    while (lim < NS && hasA(lim) && hasB(lim)) ++lim;
    return (lim / P) * P;
  }
  static constexpr int vm_at(int s) {
    int posA[64] = {}, posB[128] = {};
    int tot = 0;
    for (int t = T0; t < s || t < 0; ++t) {
      if (pfAt(t)) tot += 1;
      if (nB(t)) { tot += nB(t); if (KB == 32) posB[t + RB] = tot; else posB[((t & 1) ? ((t - 1) >> 1) : ((t - 2) >> 1)) + RB] = tot; }
      if (nA(t)) { tot += nA(t); posA[((t & 1) ? ((t - 1) >> 1) : ((t - 2) >> 1)) + RA] = tot; }
    }
    if (s >= XSTEP) tot += NX;
    int nb = s + 1;
    if (nb >= NS) return 63;
    int pb = KB == 32 ? posB[nb] : posB[nb / 2];
    int need = pb > posA[nb / 2] ? pb : posA[nb / 2];
    int v = tot - need;
    return v > 63 ? 63 : v;
  }
  static constexpr int NMAIN = nmain();
  static constexpr int vm_main(int u) {
    int v = 63;
    for (int s = u; s < NMAIN; s += P) { int x = vm_at(s); v = x < v ? x : v; }
    return v;
  }
};

template <class C> struct Loop {
  unsigned lbase;
  const char* abase;
  unsigned ga[C::NA];
  const char* gb[C::NB];
  long bstep;
  const char* pf;
  int wid, lane, wm, wn, cpar;
  unsigned ra[2][C::STATS ? C::MT : 1];
  unsigned rb[C::NT];
  bf16x8 FA[2][C::MT];
  s16x4 FBh[2][C::NT][2];
  f32x4 acc[C::MT][C::NT];
  float rs, rq;
  f32x4 cs[3];
  bf16x8 ones;

  template <int SET, int I> DEV void readOp(int aslot, int ks, int boff) {
    if constexpr (I < C::MT) {
      unsigned a = C::STATS ? ra[ks][C::STATS ? I : 0] : ra[ks][0] + I * 2048;
      FA[SET][I] = *(const LAS bf16x8*)(size_t)(a + aslot * C::ATILE);
    } else {
      constexpr int j = (I - C::MT) / 2, hf = (I - C::MT) & 1;
      unsigned a = rb[j] + boff + hf * 4 * C::NC * 16;
      FBh[SET][j][hf] = __builtin_amdgcn_ds_read_tr16_b64_v4i16((LAS s16x4*)(size_t)a);
    }
  }
  template <int SET> DEV bf16x8 FB(int j) const { return __builtin_shufflevector(FBh[SET][j][0], FBh[SET][j][1], 0, 1, 2, 3, 4, 5, 6, 7); }
  static constexpr int NMM = C::MT * C::NT, NR = C::MT + 2 * C::NT;
  static constexpr int nm(int U, int CP) { return NMM + ((C::STATS && (U & 1) == CP) ? 3 : 0); }
  template <int SET, int Q> DEV void mmaOp() {
    if constexpr (Q < NMM) {
      constexpr int m = Q / C::NT, j = Q % C::NT;
      if constexpr ((C::TRANS >> j) & 1) acc[m][j] = mfma16(FB<SET>(j), FA[SET][m], acc[m][j]);
      else acc[m][j] = mfma16(FA[SET][m], FB<SET>(j), acc[m][j]);
    } else {
      cs[Q - NMM] = mfma16(ones, FB<SET>(2 * (Q - NMM)), cs[Q - NMM]);
    }
  }
  template <int SET, int I> DEV void statOp() {
    u32x4 v = __builtin_bit_cast(u32x4, FA[SET][0]);
    float f = (I & 1) ? bfhi(v[I / 2]) : bflo(v[I / 2]);
    rs += f; rq = __builtin_fmaf(f, f, rq);
    asm volatile("" : "+v"(rs), "+v"(rq));
  }
  template <int PAR, bool IB, int I> DEV void gldsOp(int bslot, int aslot) {
    constexpr int nb = !IB ? 0 : C::KB == 32 ? C::NB : PAR ? C::NB1 : C::NB - C::NB1;
    if constexpr (I < nb) {
      constexpr int i = (C::KB == 64 && !PAR) ? C::NB1 + I : I;
      constexpr int rem = C::CPW - 64 * i;
      const unsigned dst = lbase + C::LDS_B0 + bslot * C::BSLOT + (wid * C::CPW + i * 64) * 16;
      if constexpr (rem >= 64) glds_v(gb[i], dst);
      else glds_vm(gb[i], dst, (1ull << (rem & 63)) - 1ull);
      gb[i] += bstep;
    } else {
      constexpr int i = (PAR ? 0 : C::NA1) + (I - nb);
      glds_s(ga[i], abase, lbase + aslot * C::ATILE + (wid * C::NA + i) * 1024);
      if constexpr (i == C::NA - 1) abase += 128;
    }
  }
  static constexpr int spread(int N, int Q0, int Q1, int Q) { return Q <= Q0 ? 0 : Q >= Q1 ? N : (N * (Q - Q0) + (Q1 - Q0) - 1) / (Q1 - Q0); }
  template <int SET, int LO, int HI> DEV void readRange(int aslot, int ks, int boff) {
    if constexpr (LO < HI) { readOp<SET, LO>(aslot, ks, boff); readRange<SET, LO + 1, HI>(aslot, ks, boff); }
  }
  template <int SET, int LO, int HI> DEV void statRange() {
    if constexpr (LO < HI) { statOp<SET, LO>(); statRange<SET, LO + 1, HI>(); }
  }
  template <int PAR, bool IB, int LO, int HI> DEV void gldsRange(int bslot, int aslot) {
    if constexpr (LO < HI) { gldsOp<PAR, IB, LO>(bslot, aslot); gldsRange<PAR, IB, LO + 1, HI>(bslot, aslot); }
  }
  static constexpr int wslotA(int U) { return (((U & 1) ? (U - 1) / 2 : (U - 2 + 2 * C::P) / 2)) % C::RA; }
  static constexpr int wslotB(int U) { return C::KB == 32 ? U % C::RB : (((U & 1) ? (U - 1) / 2 : (U - 2 + 2 * C::P) / 2)) % C::RB; }
  static constexpr int rslotA(int U1) { return (U1 / 2) % C::RA; }
  static constexpr int roffB(int U1) { return C::KB == 32 ? (U1 % C::RB) * C::BSLOT : ((U1 / 2) % C::RB) * C::BSLOT + (U1 & 1) * 32 * C::NC * 16; }
  template <int CP, int U, bool IB, bool IA, bool RN, int Q> DEV void body() {
    constexpr int NM = nm(U, CP);
    if constexpr (Q < NM) {
      constexpr int PAR = U & 1;
      constexpr int nb = !IB ? 0 : C::KB == 32 ? C::NB : PAR ? C::NB1 : C::NB - C::NB1;
      constexpr int na = !IA ? 0 : PAR ? C::NA1 : C::NA - C::NA1;
      constexpr int NG = nb + na;
      mmaOp<PAR, Q>();
      gldsRange<PAR, IB, spread(NG, 0, NM, Q), spread(NG, 0, NM, Q + 1)>(wslotB(U), wslotA(U));
      if constexpr (RN) readRange<PAR ^ 1, spread(NR, 0, C::RQ1, Q), spread(NR, 0, C::RQ1, Q + 1)>(rslotA(U + 1), PAR ^ 1, roffB(U + 1));
      if constexpr (C::STATS) statRange<PAR, spread(8, 0, NM, Q), spread(8, 0, NM, Q + 1)>();
      sched_fence();
      body<CP, U, IB, IA, RN, Q + 1>();
    }
  }
  template <int CP, int U, int VM, bool IB, bool IA, bool RN, bool PF = false> DEV void step() {
    if constexpr (RN && (C::KB == 32 || (U & 1))) { wait_vm<VM>(); bar(); }
    if constexpr (PF) { glds_vm(pf, lbase + C::LDS_RING + wid * 512, 0xFFFFFFFFull); pf += 512; }
    sched_fence();
    body<CP, U, IB, IA, RN, 0>();
    if constexpr (C::KB == 32 || !(U & 1)) wait_lgkm0();
    sched_fence();
  }
  template <int CP, int U> DEV void mainSteps() {
    if constexpr (U < C::P) { step<CP, U, C::vm_main(U), true, true, true, (U % 4 == 0 && C::NPF > 0)>(); mainSteps<CP, U + 1>(); }
  }
  template <int CP, int Sx, class H> DEV void tailSteps(H& hook) {
    if constexpr (Sx < C::NS) {
      if constexpr (Sx == C::XSTEP) { hook(); sched_fence(); }
      step<CP, Sx % C::P, C::vm_at(Sx), C::hasB(Sx), C::hasA(Sx), (Sx + 1 < C::NS), C::pfAt(Sx)>();
      tailSteps<CP, Sx + 1>(hook);
    }
  }
  template <int T> DEV void prologue() {
    if constexpr (T < 0) {
      constexpr int U = ((T % C::P) + C::P) % C::P, PAR = U & 1;
      gldsRange<PAR, C::hasB(T), 0, C::nB(T) + C::nA(T)>(wslotB(U), wslotA(U));
      prologue<T + 1>();
    }
  }
  template <class BF> DEV void init(LAS char* l, const char* Abase, int lda, BF bsrc, long bstep_) {
    lbase = (unsigned)(size_t)l;
    const int tid = threadIdx.x;
    wid = __builtin_amdgcn_readfirstlane(tid >> 6); lane = tid & 63;
    wm = wid / C::WN; wn = wid % C::WN; cpar = wm >> 1;
    bstep = bstep_;
    abase = Abase;
#pragma unroll
    for (int i = 0; i < C::NA; ++i) {
      int q = (wid * C::NA + i) * 64 + lane, row = q >> 3, p = q & 7, c = p ^ ((row >> 1) & 7);
      ga[i] = row * lda + c * 16;
    }
#pragma unroll
    for (int i = 0; i < C::NB; ++i) {
      int q = wid * C::CPW + i * 64 + lane;
      if (q >= C::KB * C::NC) q = C::KB * C::NC - 1;
      int k = q / C::NC, p = q % C::NC, idx = p ^ C::bswz(k);
      gb[i] = bsrc(k, idx);
    }
    prologue<C::T0>();
    sched_fence();
    const int fr = lane & 15, g = lane >> 4;
#pragma unroll
    for (int ks = 0; ks < 2; ++ks)
#pragma unroll
      for (int m = 0; m < (C::STATS ? C::MT : 1); ++m)
        ra[ks][m] = lbase + ((wm * C::MT + C::mmap(m, wn)) * 16 + fr) * 128 + (((4 * ks + g) ^ (fr >> 1)) * 16);
    const int q4 = fr >> 2, p4 = fr & 3, k = 8 * g + q4;
#pragma unroll
    for (int j = 0; j < C::NT; ++j) {
      const int jm = C::nmap(j, wm);
      int idx;
      if (C::KIND == 2) idx = (p4 >> 1) * (C::NC / 2) + wn * C::NT + jm;
      else if (C::KIND == 1) idx = (jm >> 1) * 8 + wn * 4 + (jm & 1) * 2 + (p4 >> 1);
      else idx = 2 * (wn * C::NT + jm) + (p4 >> 1);
      rb[j] = lbase + C::LDS_B0 + k * C::NC * 16 + ((idx ^ C::bswz(k)) * 16) + 8 * (p4 & 1);
    }
#pragma unroll
    for (int m = 0; m < C::MT; ++m)
#pragma unroll
      for (int j = 0; j < C::NT; ++j) acc[m][j] = (f32x4){0.f, 0.f, 0.f, 0.f};
    rs = 0.f; rq = 0.f;
    if constexpr (C::STATS) {
      u32x4 o4 = {0x3F803F80u, 0x3F803F80u, 0x3F803F80u, 0x3F803F80u};
      asm volatile("" : "+v"(o4));
      ones = __builtin_bit_cast(bf16x8, o4);
    }
#pragma unroll
    for (int j = 0; j < 3; ++j) cs[j] = (f32x4){0.f, 0.f, 0.f, 0.f};
  }
  template <int KID, class H> DEV void run(H hook) {
    wait_vm<C::vm_at(-1)>();
    bar();
    sched_fence();
    readRange<0, 0, NR>(0, 0, 0);
    wait_lgkm0();
    sched_fence();
    STAMP(KID, 1);
    if (C::STATS && cpar) {
      for (int it = 0; it < C::NMAIN / C::P; ++it) mainSteps<1, 0>();
      tailSteps<1, C::NMAIN>(hook);
    } else {
      for (int it = 0; it < C::NMAIN / C::P; ++it) mainSteps<0, 0>();
      tailSteps<0, C::NMAIN>(hook);
    }
    bar();
    sched_fence();
    STAMP(KID, 2);
  }
};

DEV u32x4 xpose2(u32x2 a, u32x2 b) {
  auto x = __builtin_amdgcn_permlane16_swap(a.x, b.x, false, false);
  auto y = __builtin_amdgcn_permlane16_swap(a.y, b.y, false, false);
  return (u32x4){x[0], y[0], x[1], y[1]};
}
DEV float phi(float t) { return (t > 0.f ? t + 1.f : __builtin_amdgcn_exp2f(t * 1.4426950408889634f)) + 1e-6f; }

#define K1RA 4
#define K1RB 4
#define K1P 8
using C1 = Cfg<128, 24, 4, 2, 2, 6, K1RA, K1RB, 64, 32, K1P, 1, 0x03u, 0>;
__global__ __launch_bounds__(C1::NTHR, 2) void k1_qkv(const u16* __restrict__ x, const u16* __restrict__ wqkv,
                                                      u16* __restrict__ pq, float* __restrict__ kvp,
                                                      const u16* __restrict__ wo, const u16* __restrict__ wg, const u16* __restrict__ wu) {
  extern __shared__ __attribute__((aligned(16))) char smem[];
  LAS char* lds = (LAS char*)smem;
  const int bid = blockIdx.x, xcd = bid & 7, idx = bid >> 3;
  const int rb = (xcd & 1) * 8 + (idx & 7), hp = (xcd >> 1) * 4 + (idx >> 3);
  STAMP(0, 0);
  Loop<C1> L;
  auto bsrc = [&](int k, int c) -> const char* {
    return (const char*)(wqkv + (long)k * (3 * D_) + (c >> 3) * D_ + hp * 64 + (c & 7) * 8);
  };
  L.init(lds, (const char*)(x + (long)rb * 128 * D_), D_ * 2, bsrc, 64L * 3 * D_ * 2);
  L.run<0>([] {});
  glds_v((const char*)wo + ((size_t)bid * 512 + threadIdx.x) * 16, L.lbase + 65536 + L.wid * 1024);
  const int lane = L.lane, fr = lane & 15, g = lane >> 4, wm = L.wm, wn = L.wn, wid = L.wid;
  LAS float* sR = (LAS float*)lds;
  LAS float* sC = (LAS float*)(lds + 2048);
  {
    float a = L.rs, b = L.rq;
    a += __shfl_xor(a, 16); a += __shfl_xor(a, 32);
    b += __shfl_xor(b, 16); b += __shfl_xor(b, 32);
    const int row = wm * 32 + C1::mmap(0, wn) * 16 + fr;
    if (g == 0) {
      float mean = a * (1.f / D_);
      float var = fmaxf(b * (1.f / D_) - mean * mean, 0.f);
      float rstd = rsqrtf(var + 1e-5f);
      *(LAS f32x2*)(sR + row * 2) = (f32x2){rstd, -mean * rstd};
#pragma unroll
      for (int j = 0; j < 3; ++j) sC[(wm >> 1) * 192 + wn * 96 + C1::nmap(2 * j, wm) * 16 + fr] = L.cs[j][0];
    }
  }
  __syncthreads();
  const int head = hp * 2 + wn;
  bf16x8 fk[2], fv[2];
  {
    f32x4 sa[2], sb[2];
#pragma unroll
    for (int m = 0; m < 2; ++m) {
      const int r0 = wm * 32 + C1::mmap(m, wn) * 16 + 4 * g;
      f32x4 u0 = *(const LAS f32x4*)(sR + r0 * 2), u1 = *(const LAS f32x4*)(sR + r0 * 2 + 4);
      sa[m] = (f32x4){u0[0], u0[2], u1[0], u1[2]}; sb[m] = (f32x4){u0[1], u0[3], u1[1], u1[3]};
    }
#pragma unroll
    for (int t = 0; t < 2; ++t) {
      const int ik = wn * 96 + C1::nmap(2 + t, wm) * 16 + fr, iv = wn * 96 + C1::nmap(4 + t, wm) * 16 + fr;
      float ck = sC[ik] + sC[192 + ik], cv = sC[iv] + sC[192 + iv];
      u32x4 a, b;
#pragma unroll
      for (int m = 0; m < 2; ++m) {
        f32x4 k = L.acc[m][2 + t] * sa[m] + sb[m] * ck;
        f32x4 v = L.acc[m][4 + t] * sa[m] + sb[m] * cv;
        a[2 * m] = pk2(phi(k[0]), phi(k[1])); a[2 * m + 1] = pk2(phi(k[2]), phi(k[3]));
        b[2 * m] = pk2(v[0], v[1]); b[2 * m + 1] = pk2(v[2], v[3]);
      }
      fk[t] = __builtin_bit_cast(bf16x8, a); fv[t] = __builtin_bit_cast(bf16x8, b);
    }
  }
  const u32x4 ones4 = {0x3F803F80u, 0x3F803F80u, 0x3F803F80u, 0x3F803F80u};
  const bf16x8 ones = __builtin_bit_cast(bf16x8, ones4);
  constexpr int PP = 36, PW = 33 * PP;
  LAS float* sP = (LAS float*)(lds + 8192);
  const f32x4 z4 = {0.f, 0.f, 0.f, 0.f};
#pragma unroll
  for (int dn = 0; dn < 2; ++dn) {
#pragma unroll
    for (int en = 0; en < 2; ++en) {
      f32x4 d = mfma16(fk[dn], fv[en], z4);
      *(LAS f32x4*)(sP + wid * PW + ((en ^ (wm & 1)) * 16 + fr) * PP + (dn ^ (wm & 1)) * 16 + 4 * g) = d;
    }
    f32x4 zz = mfma16(fk[dn], ones, z4);
    if (fr == 0) *(LAS f32x4*)(sP + wid * PW + 32 * PP + (dn ^ (wm & 1)) * 16 + 4 * g) = zz;
  }
  {
    f32x4 c4[2];
#pragma unroll
    for (int j = 0; j < 2; ++j) {
      const int ic = wn * 96 + C1::nmap(j, wm) * 16 + 4 * g;
      c4[j] = *(const LAS f32x4*)(sC + ic) + *(const LAS f32x4*)(sC + 192 + ic);
    }
#pragma unroll
    for (int m = 0; m < 2; ++m) {
      const int lr = wm * 32 + C1::mmap(m, wn) * 16 + fr;
      const f32x2 st = *(const LAS f32x2*)(sR + lr * 2);
      u32x2 w[2];
#pragma unroll
      for (int j = 0; j < 2; ++j) {
        f32x4 t = L.acc[m][j] * st.x + c4[j] * st.y;
        w[j].x = pk2(phi(t[0]), phi(t[1])); w[j].y = pk2(phi(t[2]), phi(t[3]));
      }
      *(u32x4*)(pq + ((long)rb * 128 + lr) * D_ + head * 32 + ((g ^ wm) & 1) * 16 + (g >> 1) * 8) = xpose2(w[0], w[1]);
    }
  }
  __syncthreads();
  for (int i = threadIdx.x; i < 2 * (KVP / 4); i += C1::NTHR) {
    int hh = i >= KVP / 4, e = i - hh * (KVP / 4);
    const LAS f32x4* p = (const LAS f32x4*)(sP + hh * PW + (e >> 3) * PP + (e & 7) * 4);
    f32x4 s = (p[0] + p[2 * (PW / 4)]) + (p[4 * (PW / 4)] + p[6 * (PW / 4)]);
    *((f32x4*)(kvp + ((long)(hp * 2 + hh) * 16 + rb) * KVP) + e) = s;
  }
  STAMP(0, 3);
}

__global__ __launch_bounds__(512) void k15_o(const u16* __restrict__ pq, const float* __restrict__ kvp, u16* __restrict__ o,
                                             const u16* __restrict__ wg, const u16* __restrict__ wu, float* __restrict__ csp) {
  __shared__ __attribute__((aligned(16))) u16 kvT[33 * 40];
  __shared__ __attribute__((aligned(16))) float cred[9][448];
  const int bid = blockIdx.x, role = (bid >> 3) & 1, id = (bid >> 4) * 8 + (bid & 7);
  const int tid = threadIdx.x, lane = tid & 63, wid = tid >> 6, fr = lane & 15, g = lane >> 4;
  if (role == 0) {
    const int head = id & 31, rb = id >> 5;
    f32x4 v[8];
    {
      const f32x4* p = (const f32x4*)(kvp + ((long)head * 16 + (tid & 1) * 8) * KVP) + (tid >> 1);
#pragma unroll
      for (int b = 0; b < 8; ++b) v[b] = p[b * (KVP / 4)];
    }
    f32x4 z0 = {0.f, 0.f, 0.f, 0.f}, z1 = z0;
    if (wid == 0) {
      const f32x4* p = (const f32x4*)(kvp + ((long)head * 16 + (lane >> 3)) * KVP + 1024) + (lane & 7);
      z0 = p[0]; z1 = p[8 * (KVP / 4)];
    }
    bf16x8 fq[4];
#pragma unroll
    for (int m = 0; m < 4; ++m)
      fq[m] = *(const bf16x8*)(pq + ((long)rb * 512 + wid * 64 + m * 16 + fr) * D_ + head * 32 + g * 8);
    {
      f32x4 s = ((v[0] + v[1]) + (v[2] + v[3])) + ((v[4] + v[5]) + (v[6] + v[7]));
#pragma unroll
      for (int r = 0; r < 4; ++r) s[r] += __shfl_xor(s[r], 1);
      const int it = tid >> 1, e = it >> 3, d = (it & 7) * 4 + (tid & 1) * 2;
      *(unsigned*)(kvT + e * 40 + d) = (tid & 1) ? pk2(s[2], s[3]) : pk2(s[0], s[1]);
    }
    if (wid == 0) {
      f32x4 z = z0 + z1;
#pragma unroll
      for (int r = 0; r < 4; ++r) { z[r] += __shfl_xor(z[r], 8); z[r] += __shfl_xor(z[r], 16); z[r] += __shfl_xor(z[r], 32); }
      if (lane < 8) { u32x2 w; w.x = pk2(z[0], z[1]); w.y = pk2(z[2], z[3]); *(u32x2*)(kvT + 32 * 40 + lane * 4) = w; }
    }
    __syncthreads();
    bf16x8 fa[3];
#pragma unroll
    for (int t = 0; t < 2; ++t) fa[t] = *(const bf16x8*)(kvT + (t * 16 + fr) * 40 + g * 8);
    fa[2] = *(const bf16x8*)(kvT + 32 * 40 + g * 8);
    const f32x4 z4 = {0.f, 0.f, 0.f, 0.f};
#pragma unroll
    for (int m = 0; m < 4; ++m) {
      f32x4 den = mfma16(fa[2], fq[m], z4);
      float inv = 1.f / (den[0] + 1e-6f);
      const long row = (long)rb * 512 + wid * 64 + m * 16 + fr;
      u32x2 w[2];
#pragma unroll
      for (int t = 0; t < 2; ++t) {
        f32x4 n = mfma16(fa[t], fq[m], z4);
        w[t].x = pk2(n[0] * inv, n[1] * inv); w[t].y = pk2(n[2] * inv, n[3] * inv);
      }
      *(u32x4*)(o + row * D_ + head * 32 + (g & 1) * 16 + (g >> 1) * 8) = xpose2(w[0], w[1]);
    }
  } else {
    const int cg = id & 15, rgp = id >> 4;
    const int cc = tid % 56, rl = tid / 56;
    u32x4 wv[15];
    const u16* w = (cg < 8 ? wg : wu) + (long)(rgp * 128) * DF_ + (cg & 7) * 448 + cc * 8;
#pragma unroll
    for (int i = 0; i < 15; ++i) {
      int r = rl + 9 * i;
      wv[i] = (rl < 9 && r < 128) ? *(const u32x4*)(w + (long)r * DF_) : (u32x4){0u, 0u, 0u, 0u};
    }
    float a[8] = {0.f, 0.f, 0.f, 0.f, 0.f, 0.f, 0.f, 0.f};
#pragma unroll
    for (int i = 0; i < 15; ++i)
#pragma unroll
      for (int q = 0; q < 4; ++q) { a[2 * q] += bflo(wv[i][q]); a[2 * q + 1] += bfhi(wv[i][q]); }
    if (rl < 9) {
      *(f32x4*)(&cred[rl][cc * 8]) = (f32x4){a[0], a[1], a[2], a[3]};
      *(f32x4*)(&cred[rl][cc * 8 + 4]) = (f32x4){a[4], a[5], a[6], a[7]};
    }
    __syncthreads();
    if (tid < 448) {
      float s = 0.f;
#pragma unroll
      for (int i = 0; i < 9; ++i) s += cred[i][tid];
      csp[(long)rgp * (2 * DF_) + cg * 448 + tid] = s;
    }
  }
}

using C2 = Cfg<128, 8, 2, 2, 4, 2, 4, 4, 64, 32, 8, 0, 0x3u, 4>;
__global__ __launch_bounds__(C2::NTHR, 1) void k2_wo(const u16* __restrict__ o, const u16* __restrict__ wo, const u16* __restrict__ x,
                                                     u16* __restrict__ h, float* __restrict__ hstat) {
  extern __shared__ __attribute__((aligned(16))) char smem[];
  LAS char* lds = (LAS char*)smem;
  const int bid = blockIdx.x, xcd = bid & 7, idx = bid >> 3;
  const int rb = (xcd & 3) * 4 + (idx & 3), ct = (xcd >> 2) * 8 + (idx >> 2);
  STAMP(2, 0);
  Loop<C2> L;
  auto bsrc = [&](int k, int c) -> const char* { return (const char*)(wo + (long)k * D_ + ct * 64 + c * 8); };
  L.init(lds, (const char*)(o + (long)rb * 128 * D_), D_ * 2, bsrc, 64L * D_ * 2);
  const int lane = L.lane, fr = lane & 15, g = lane >> 4, wm = L.wm, wn = L.wn;
  const long off0 = ((long)rb * 128 + wm * 64 + fr) * D_ + ct * 64 + wn * 32 + (g & 1) * 16 + (g >> 1) * 8;
  u32x4 xr[4];
  L.run<2>([&] {
#pragma unroll
    for (int m = 0; m < 4; ++m) xr[m] = *(const u32x4*)(x + off0 + m * 16 * D_);
  });
  LAS float* sS = (LAS float*)lds;
#pragma unroll
  for (int m = 0; m < 4; ++m) {
    const int lr = wm * 64 + m * 16 + fr;
    float s = 0.f, q = 0.f;
    const u32x4 r = xpose2((u32x2){xr[m][0], xr[m][1]}, (u32x2){xr[m][2], xr[m][3]});
    u32x2 w[2];
#pragma unroll
    for (int j = 0; j < 2; ++j) {
      w[j].x = pk2(bflo(r[2 * j]) + L.acc[m][j][0], bfhi(r[2 * j]) + L.acc[m][j][1]);
      w[j].y = pk2(bflo(r[2 * j + 1]) + L.acc[m][j][2], bfhi(r[2 * j + 1]) + L.acc[m][j][3]);
      float f0 = bflo(w[j].x), f1 = bfhi(w[j].x), f2 = bflo(w[j].y), f3 = bfhi(w[j].y);
      s += (f0 + f1) + (f2 + f3);
      q += (f0 * f0 + f1 * f1) + (f2 * f2 + f3 * f3);
    }
    *(u32x4*)(h + off0 + m * 16 * D_) = xpose2(w[0], w[1]);
    s += __shfl_xor(s, 16); s += __shfl_xor(s, 32);
    q += __shfl_xor(q, 16); q += __shfl_xor(q, 32);
    if (g == 0) *(LAS f32x2*)(sS + (wn * 128 + lr) * 2) = (f32x2){s, q};
  }
  __syncthreads();
  if (threadIdx.x < 128) {
    const int r = threadIdx.x;
    f32x2 v = *(LAS f32x2*)(sS + r * 2) + *(LAS f32x2*)(sS + (128 + r) * 2);
    *(f32x2*)(hstat + (((long)rb * 128 + r) * 16 + ct) * 2) = v;
  }
  STAMP(2, 3);
}

#define K3RA 3
#define K3RB 4
#define K3P 12
#define K3PF 7
using C3 = Cfg<256, 28, 4, 2, 4, 7, K3RA, K3RB, 32, 32, K3P, 2, 0x7Fu, 8, K3PF>;
static_assert(K3PF == 0 || (C3::P % 4 == 0 && C3::NMAIN <= 28 && C3::XSTEP > 24), "wd prefetch schedule");
__global__ __launch_bounds__(C3::NTHR, 2) void k3_glu(const u16* __restrict__ h, const u16* __restrict__ wg, const u16* __restrict__ wu,
                                                      const float* __restrict__ hstat, const float* __restrict__ csp,
                                                      u16* __restrict__ act, const u16* __restrict__ wd) {
  extern __shared__ __attribute__((aligned(16))) char smem[];
  LAS char* lds = (LAS char*)smem;
  const int bid = blockIdx.x, xcd = bid & 7, idx = bid >> 3;
  const int rb = (xcd & 1) * 4 + (idx & 3), ct = (xcd >> 1) * 8 + (idx >> 2);
  STAMP(3, 0);
  Loop<C3> L;
  auto bsrc = [&](int k, int c) -> const char* {
    const u16* w = c < 14 ? wg : wu; int cc = c < 14 ? c : c - 14;
    return (const char*)(w + (long)k * DF_ + ct * 112 + cc * 8);
  };
  L.init(lds, (const char*)(h + (long)rb * 256 * D_), D_ * 2, bsrc, 32L * DF_ * 2);
  L.pf = (const char*)wd + (size_t)(bid * 8 + L.wid) * (7 * 512) + (L.lane & 31) * 16;
  f32x4 hs[4];
  const int tid = threadIdx.x;
  float cpart[4];
  L.run<3>([&] {
    const f32x4* p = (const f32x4*)(hstat + ((long)rb * 256 + (tid >> 1)) * 32 + (tid & 1) * 16);
#pragma unroll
    for (int i = 0; i < 4; ++i) hs[i] = p[i];
    const int c = tid < 448 ? tid >> 1 : 0;
    const float* q = csp + (c < 112 ? 0 : DF_ - 112) + ct * 112 + c + (long)(tid & 1) * (4 * 2 * DF_);
#pragma unroll
    for (int i = 0; i < 4; ++i) cpart[i] = q[(long)i * (2 * DF_)];
  });
  const int lane = L.lane, fr = lane & 15, g = lane >> 4, wm = L.wm, wn = L.wn;
  LAS float* sC = (LAS float*)lds;
  LAS float* sR = (LAS float*)(lds + 1024);
  {
    float c = (cpart[0] + cpart[1]) + (cpart[2] + cpart[3]);
    c += __shfl_xor(c, 1);
    if (tid < 448 && (tid & 1) == 0) sC[tid >> 1] = c;
  }
  {
    f32x4 t = (hs[0] + hs[1]) + (hs[2] + hs[3]);
    float s = t[0] + t[2], q = t[1] + t[3];
    s += __shfl_xor(s, 1); q += __shfl_xor(q, 1);
    float mean = s * (1.f / D_);
    float var = fmaxf(q * (1.f / D_) - mean * mean, 0.f);
    if ((tid & 1) == 0) *(LAS f32x2*)(sR + (tid >> 1) * 2) = (f32x2){mean, rsqrtf(var + 1e-5f)};
  }
  __syncthreads();
  const int hi = lane >> 5, g1 = g & 1;
  f32x2 st[2];
#pragma unroll
  for (int mp = 0; mp < 2; ++mp) st[mp] = *(const LAS f32x2*)(sR + (wm * 64 + (2 * mp + hi) * 16 + fr) * 2);
  LAS char* tb = lds + 4096 + L.wid * 7168;
  LAS char* tw = tb + (hi * 16 + fr) * 112 + g1 * 8;
  u16* ap = act + ((long)rb * 256 + wm * 64) * DF_ + ct * 112 + wn * 56;
  constexpr float K1c = -2.f * 0.7978845608028654f * 1.4426950408889634f, K2c = K1c * 0.044715f;
#pragma unroll
  for (int mp = 0; mp < 2; ++mp) {
    const float nm = -st[mp].x, rstd = st[mp].y;
#pragma unroll
    for (int j = 0; j < 7; ++j) {
      const f32x4 cg = *(const LAS f32x4*)(sC + (wn * 7 + j) * 8 + 4 * g1), cu = *(const LAS f32x4*)(sC + 112 + (wn * 7 + j) * 8 + 4 * g1);
      f32x4 G, U;
#pragma unroll
      for (int r = 0; r < 4; ++r) {
        auto sw = __builtin_amdgcn_permlane32_swap(__float_as_uint(L.acc[2 * mp][j][r]), __float_as_uint(L.acc[2 * mp + 1][j][r]), false, false);
        G[r] = __uint_as_float(sw[0]); U[r] = __uint_as_float(sw[1]);
      }
      G = (G + cg * nm) * rstd;
      U = (U + cu * nm) * rstd;
      f32x4 z = G * ((G * G) * K2c + K1c);
      f32x4 e;
#pragma unroll
      for (int r = 0; r < 4; ++r) e[r] = __builtin_amdgcn_exp2f(z[r]);
      e = e + 1.f;
#pragma unroll
      for (int r = 0; r < 4; ++r) e[r] = __builtin_amdgcn_rcpf(e[r]);
      f32x4 o = (G * U) * e;
      u32x2 w; w.x = pk2(o[0], o[1]); w.y = pk2(o[2], o[3]);
      *(LAS u32x2*)(tw + mp * 32 * 112 + j * 16) = w;
    }
#pragma unroll
    for (int i = mp * 3; i < (mp ? 7 : 3); ++i) {
      const int q = i * 64 + lane, row = q / 7, c = q - row * 7;
      const u32x4 v = *(const LAS u32x4*)(tb + q * 16);
      *(u32x4*)(ap + (long)row * DF_ + c * 8) = v;
    }
  }
  STAMP(3, 3);
}

using C4 = Cfg<128, 8, 2, 2, 4, 2, 4, 4, 64, 112, 8, 0, 0x3u, 4>;
__global__ __launch_bounds__(C4::NTHR, 1) void k4_down(const u16* __restrict__ act, const u16* __restrict__ wd, const u16* __restrict__ h,
                                                       u16* __restrict__ out) {
  extern __shared__ __attribute__((aligned(16))) char smem[];
  LAS char* lds = (LAS char*)smem;
  const int bid = blockIdx.x, xcd = bid & 7, idx = bid >> 3;
  const int rb = (xcd & 3) * 4 + (idx & 3), ct = (xcd >> 2) * 8 + (idx >> 2);
  STAMP(4, 0);
  Loop<C4> L;
  auto bsrc = [&](int k, int c) -> const char* { return (const char*)(wd + (long)k * D_ + ct * 64 + c * 8); };
  L.init(lds, (const char*)(act + (long)rb * 128 * DF_), DF_ * 2, bsrc, 64L * D_ * 2);
  const int lane = L.lane, fr = lane & 15, g = lane >> 4, wm = L.wm, wn = L.wn;
  const long off0 = ((long)rb * 128 + wm * 64 + fr) * D_ + ct * 64 + wn * 32 + (g & 1) * 16 + (g >> 1) * 8;
  u32x4 xr[4];
  L.run<4>([&] {
#pragma unroll
    for (int m = 0; m < 4; ++m) xr[m] = *(const u32x4*)(h + off0 + m * 16 * D_);
  });
#pragma unroll
  for (int m = 0; m < 4; ++m) {
    const u32x4 r = xpose2((u32x2){xr[m][0], xr[m][1]}, (u32x2){xr[m][2], xr[m][3]});
    u32x2 w[2];
#pragma unroll
    for (int j = 0; j < 2; ++j) {
      w[j].x = pk2(bflo(r[2 * j]) + L.acc[m][j][0], bfhi(r[2 * j]) + L.acc[m][j][1]);
      w[j].y = pk2(bflo(r[2 * j + 1]) + L.acc[m][j][2], bfhi(r[2 * j + 1]) + L.acc[m][j][3]);
    }
    *(u32x4*)(out + off0 + m * 16 * D_) = xpose2(w[0], w[1]);
  }
  STAMP(4, 3);
}

extern "C" void kernel_launch(void* const* d_in, const int* in_sizes, int n_in, void* d_out, int out_size, void* d_ws,
                              size_t ws_size, hipStream_t stream) {
  const u16* x = (const u16*)d_in[0];
  const u16* wqkv = (const u16*)d_in[1];
  const u16* wo = (const u16*)d_in[2];
  const u16* wg = (const u16*)d_in[4];
  const u16* wu = (const u16*)d_in[5];
  const u16* wd = (const u16*)d_in[6];
  u16* out = (u16*)d_out;
  char* ws = (char*)d_ws;
  u16* pq = (u16*)ws;                      ws += (size_t)S_ * D_ * 2;
  u16* o = (u16*)ws;                       ws += (size_t)S_ * D_ * 2;
  u16* h = (u16*)ws;                       ws += (size_t)S_ * D_ * 2;
  u16* act = (u16*)ws;                     ws += (size_t)S_ * DF_ * 2;
  float* kvp = (float*)ws;                 ws += (size_t)NH_ * 16 * KVP * 4;
  float* hstat = (float*)ws;               ws += (size_t)S_ * 16 * 2 * 4;
  float* csp = (float*)ws;                 ws += (size_t)8 * 2 * DF_ * 4;
  (void)hipFuncSetAttribute((const void*)k1_qkv, hipFuncAttributeMaxDynamicSharedMemorySize, C1::LDS_BYTES);
  (void)hipFuncSetAttribute((const void*)k2_wo, hipFuncAttributeMaxDynamicSharedMemorySize, C2::LDS_BYTES);
  (void)hipFuncSetAttribute((const void*)k3_glu, hipFuncAttributeMaxDynamicSharedMemorySize, C3::LDS_BYTES);
  (void)hipFuncSetAttribute((const void*)k4_down, hipFuncAttributeMaxDynamicSharedMemorySize, C4::LDS_BYTES);
  k1_qkv<<<256, C1::NTHR, C1::LDS_BYTES, stream>>>(x, wqkv, pq, kvp, wo, wg, wu);
  k15_o<<<256, 512, 0, stream>>>(pq, kvp, o, wg, wu, csp);
  k2_wo<<<256, C2::NTHR, C2::LDS_BYTES, stream>>>(o, wo, x, h, hstat);
  k3_glu<<<256, C3::NTHR, C3::LDS_BYTES, stream>>>(h, wg, wu, hstat, csp, act, wd);
  k4_down<<<256, C4::NTHR, C4::LDS_BYTES, stream>>>(act, wd, h, out);
}
```

```cpp
#include <hip/hip_runtime.h>
#include <hip/hip_bf16.h>
#include <stdint.h>

typedef unsigned short u16;
typedef short bf16x8 __attribute__((ext_vector_type(8)));
typedef short s16x4 __attribute__((ext_vector_type(4)));
typedef float f32x4 __attribute__((ext_vector_type(4)));
typedef float f32x2 __attribute__((ext_vector_type(2)));
typedef unsigned u32x4 __attribute__((ext_vector_type(4)));
typedef unsigned u32x2 __attribute__((ext_vector_type(2)));
typedef __bf16 bf16x2_t __attribute__((ext_vector_type(2)));
#define LAS __attribute__((address_space(3)))
#define DEV __device__ __forceinline__
template <int V> struct IC { static constexpr int value = V; };

constexpr int S_ = 2048, D_ = 1024, DF_ = 3584, NH_ = 32;
constexpr int KVP = 1056;
constexpr unsigned SENT = 0xFFFFFFFFu;
constexpr int XBUF_BYTES = 256 * 16384;

DEV float bflo(unsigned v) { return __uint_as_float(v << 16); }
DEV float bfhi(unsigned v) { return __uint_as_float(v & 0xffff0000u); }
DEV unsigned pk2(float lo, float hi) {
  bf16x2_t r; r.x = (__bf16)lo; r.y = (__bf16)hi; return __builtin_bit_cast(unsigned, r);
}
template <int N> DEV void wait_vm() { asm volatile("s_waitcnt vmcnt(%0)" ::"n"(N) : "memory"); }
DEV void wait_lgkm0() { __builtin_amdgcn_s_waitcnt(0xC07F); }
DEV void bar() { __builtin_amdgcn_s_barrier(); }
DEV void lbar() { wait_lgkm0(); bar(); asm volatile("" ::: "memory"); }
DEV void sched_fence() { __builtin_amdgcn_sched_barrier(0); }
DEV void glds_v(const void* g, unsigned lds_addr) {
  asm volatile("s_mov_b32 m0, %1\n\ts_nop 0\n\tglobal_load_lds_dwordx4 %0, off" ::"v"(g), "s"(lds_addr) : "memory");
}
DEV void glds_vm(const void* g, unsigned lds_addr, unsigned long long mask) {
  asm volatile("s_mov_b32 m0, %1\n\ts_mov_b64 exec, %2\n\tglobal_load_lds_dwordx4 %0, off\n\ts_mov_b64 exec, -1"
               ::"v"(g), "s"(lds_addr), "s"(mask) : "memory");
}
DEV void glds_s(unsigned voff, const void* sbase, unsigned lds_addr) {
  asm volatile("s_mov_b32 m0, %2\n\ts_nop 0\n\tglobal_load_lds_dwordx4 %0, %1" ::"v"(voff), "s"(sbase), "s"(lds_addr) : "memory");
}
DEV void touch(const void* g, unsigned lds_addr) {
  asm volatile("s_mov_b32 m0, %1\n\ts_nop 0\n\tglobal_load_lds_dword %0, off" ::"v"(g), "s"(lds_addr) : "memory");
}
DEV f32x4 mfma16(bf16x8 a, bf16x8 b, f32x4 c) { return __builtin_amdgcn_mfma_f32_16x16x32_bf16(a, b, c, 0, 0, 0); }
DEV void mfma_acc(f32x4& c, bf16x8 a, bf16x8 b) {
  asm volatile("v_mfma_f32_16x16x32_bf16 %0, %1, %2, %0" : "+v"(c) : "v"(a), "v"(b));
}
DEV bf16x8 cat(s16x4 a, s16x4 b) { return __builtin_shufflevector(a, b, 0, 1, 2, 3, 4, 5, 6, 7); }
DEV s16x4 rdtr(unsigned a) { return __builtin_amdgcn_ds_read_tr16_b64_v4i16((LAS s16x4*)(size_t)a); }

#define STAMP(kid, i)
template <int BM_, int NC_, int WM_, int WN_, int MT_, int NT_, int RA_, int RB_, int KB_, int NS_, int P_, int KIND_, unsigned TRANS_>
struct Cfg {
  static constexpr int BM = BM_, NC = NC_, WM = WM_, WN = WN_, MT = MT_, NT = NT_, RA = RA_, RB = RB_, KB = KB_, NS = NS_, P = P_;
  static constexpr int KIND = KIND_;
  static constexpr unsigned TRANS = TRANS_;
  static constexpr bool STATS = KIND == 1, PERM = KIND == 1 || KIND == 3;
#define ROTMASK 0
  static constexpr bool ROT = (ROTMASK >> KIND) & 1;
  static constexpr int NW = WM * WN, NTHR = NW * 64;
  static constexpr int ATILE = BM * 128, BSLOT = KB * NC * 16;
  static constexpr int NA = BM * 8 / NTHR;
  static constexpr int CPW = KB * NC / NW;
  static constexpr int NB = (CPW + 63) / 64;
  static constexpr int NA1 = KB == 64 ? NA / 2 : (NA + 1) / 2, NB1 = KB == 64 ? (NB + 1) / 2 : NB;
  static constexpr int LDS_B0 = RA * ATILE, LDS_BYTES = RA * ATILE + RB * BSLOT;
  static constexpr int NTA = NS / 2;
#define RQN 7
#define RQD 8
  static constexpr int RQ1 = (MT * NT * RQN) / RQD;
  static_assert(BM * 8 % NTHR == 0, "A tile split");
  static_assert((KB * NC) % NW == 0, "B slot split");
  static_assert(P % 2 == 0 && (P / 2) % RA == 0 && (KB == 32 ? P % RB == 0 : (P / 2) % RB == 0), "period");
  static_assert(BM == WM * MT * 16, "M tiling");
  static_assert(NC * 8 == WN * NT * 16, "N tiling");
  DEV static constexpr int bswz(int k) {
    return NC == 28 ? 2 * ((k >> 3) & 1)
         : (NC % 16 == 8) ? 2 * (((k >> 1) & 1) | (((k >> 3) & 1) << 1))
                          : 2 * ((k & 3) | (((k >> 3) & 1) << 2));
  }
  static constexpr bool hasB(int t) {
    if (KB == 32) return t + RB >= 0 && t + RB < NS;
    int tile = (t & 1) ? ((t - 1) >> 1) + RB : ((t - 2) >> 1) + RB;
    return tile >= 0 && tile < NTA;
  }
  static constexpr bool hasA(int t) {
    int tile = (t & 1) ? ((t - 1) >> 1) + RA : ((t - 2) >> 1) + RA;
    return tile >= 0 && tile < NTA;
  }
  static constexpr int nB(int t) { return !hasB(t) ? 0 : KB == 32 ? NB : (t & 1) ? NB1 : NB - NB1; }
  static constexpr int nA(int t) { return !hasA(t) ? 0 : (t & 1) ? NA1 : NA - NA1; }
  static constexpr int T0 = -(2 * (RA > RB ? RA : RB) + 2);
  static constexpr int lastIssue() { int l = 0; for (int t = 0; t < NS; ++t) if (nA(t) + nB(t) > 0) l = t; return l; }
  static constexpr int XS = lastIssue() + 1;
  static constexpr int nX(int t) {
    if (KIND == 0) return t == XS ? 8 : 0;
    if (KIND == 1) return (t >= XS && t < XS + 4) ? 2 : 0;
    if (KIND == 2) return 0;
    return t == XS ? 2 : 0;
  }
  static constexpr int nXP(int t) { return KIND == 1 && t % 8 == 0 && t <= 24 ? 1 : 0; }
  static constexpr int TPRE = KB == 32 ? -3 : -4, RATE = KB == 32 ? 2 : 4;
  static constexpr int vlast(int s) { int v = TPRE + RATE * (s + 1); return s < 0 ? TPRE : (v < s ? v : s); }
  static constexpr int nhead() { int s = 0; while (vlast(s - 1) != s - 1) ++s; return s; }
  static constexpr int H = nhead();
  static constexpr int npro() { int n = 0; for (int t = T0; t <= TPRE; ++t) n += nA(t) + nB(t); return n; }
  static constexpr int nmain() {
    int lim = 0;
    while (lim < NS && hasA(lim) && hasB(lim)) ++lim;
    return H + ((lim - H) / P) * P;
  }
  static constexpr int vm_at(int s) {
    int posA[64] = {}, posB[128] = {}, cntA[64] = {}, cntB[128] = {};
    int tot = 0;
    for (int r = -1; r < s || r < 0; ++r) {
      if (r >= 0) tot += nX(r);
      for (int t = (r < 0 ? T0 : vlast(r - 1) + 1); t <= vlast(r); ++t) {
        const int tl = (t & 1) ? ((t - 1) >> 1) : ((t - 2) >> 1);
        if (nB(t)) { tot += nB(t); const int i = KB == 32 ? t + RB : tl + RB; posB[i] = tot; cntB[i] += nB(t); }
        if (nA(t)) { tot += nA(t); posA[tl + RA] = tot; cntA[tl + RA] += nA(t); }
      }
      if (r >= 0) tot += nXP(r);
    }
    int nb = s + 1;
    if (nb >= NS) return 63;
    const int ib = KB == 32 ? nb : nb / 2;
    if (cntB[ib] != NB || cntA[nb / 2] != NA) return -1;
    int need = posB[ib] > posA[nb / 2] ? posB[ib] : posA[nb / 2];
    int v = tot - need;
    return v > 63 ? 63 : v;
  }
  static constexpr int NMAIN = nmain();
  static constexpr int vm_main(int u) {
    int v = 63;
    for (int s = H + u; s < NMAIN; s += P) { int x = vm_at(s); v = x < v ? x : v; }
    return v;
  }
};

template <class C> struct Loop {
  unsigned lbase;
  const char* abase;
  unsigned ga[C::NA];
  const char* gb[C::NB];
  unsigned gbo[C::NB];
  const char* bbase;
  int acnt, bcnt;
  long bstep;
  int wid, lane, wm, wn;
  unsigned ra[2][C::PERM ? C::MT : 1];
  unsigned rb[C::NT];
  bf16x8 FA[2][C::MT];
  s16x4 FBh[2][C::NT][2];
  f32x4 acc[C::MT][C::NT];
  f32x4 rs, rq;
  f32x4 cs[3];
  bf16x8 ones;

  template <int SET, int I> DEV void readOp(int aslot, int ks, int boff) {
    if constexpr (I < C::MT) {
      unsigned a = C::PERM ? ra[ks][C::PERM ? I : 0] : ra[ks][0] + I * 2048;
      FA[SET][I] = *(const LAS bf16x8*)(size_t)(a + aslot * C::ATILE);
    } else {
      constexpr int j = (I - C::MT) / 2, hf = (I - C::MT) & 1;
      FBh[SET][j][hf] = rdtr(rb[j] + boff + hf * 4 * C::NC * 16);
    }
  }
  static constexpr int rord(int i) {
    if (i == 0) return 0;
    if (i <= 2 * C::NT) return C::MT + i - 1;
    return i - 2 * C::NT;
  }
  template <int SET> DEV bf16x8 FB(int j) const { return cat(FBh[SET][j][0], FBh[SET][j][1]); }
  static constexpr int NMM = C::MT * C::NT, NR = C::MT + 2 * C::NT;
  static constexpr int nm(int U, int CP) { return NMM + (C::STATS ? ((U & 1) == CP ? 5 : 2) : 0); }
  template <int SET, int Q> DEV void mmaOp() {
    if constexpr (Q < NMM) {
      constexpr int m = Q / C::NT, j = Q % C::NT;
      if constexpr ((C::TRANS >> j) & 1) mfma_acc(acc[m][j], FB<SET>(j), FA[SET][m]);
      else mfma_acc(acc[m][j], FA[SET][m], FB<SET>(j));
    } else if constexpr (Q == NMM) {
      mfma_acc(rs, ones, FA[SET][0]);
    } else if constexpr (Q == NMM + 1) {
      mfma_acc(rq, FA[SET][0], FA[SET][0]);
    } else {
      mfma_acc(cs[Q - NMM - 2], ones, FB<SET>(Q - NMM - 2));
    }
  }
  template <int PAR, bool IB, int I> DEV void gldsOp(int bslot, int aslot) {
    constexpr int nb = !IB ? 0 : C::KB == 32 ? C::NB : PAR ? C::NB1 : C::NB - C::NB1;
    if constexpr (I < nb) {
      constexpr int i = (C::KB == 64 && !PAR) ? C::NB1 + I : I;
      constexpr int rem = C::CPW - 64 * i;
      const unsigned dst = lbase + C::LDS_B0 + bslot * C::BSLOT + (wid * C::CPW + i * 64) * 16;
      if constexpr (C::KIND == 2) {
        if constexpr (rem >= 64) glds_v(gb[i], dst);
        else glds_vm(gb[i], dst, (1ull << (rem & 63)) - 1ull);
        gb[i] += bstep;
      } else {
        static_assert(C::KIND == 2 || rem >= 64, "full pieces only");
        glds_s(gbo[i], bbase, dst);
        if constexpr (i == C::NB - 1) {
          bbase += bstep;
          if constexpr (C::ROT) { if (--bcnt == 0) bbase -= bstep * C::NTA; }
        }
      }
    } else {
      constexpr int i = (PAR ? 0 : C::NA1) + (I - nb);
      glds_s(ga[i], abase, lbase + aslot * C::ATILE + (wid * C::NA + i) * 1024);
      if constexpr (i == C::NA - 1) {
        abase += 128;
        if constexpr (C::ROT) { if (--acnt == 0) abase -= 128 * C::NTA; }
      }
    }
  }
  static constexpr int spread(int N, int Q0, int Q1, int Q) { return Q <= Q0 ? 0 : Q >= Q1 ? N : (N * (Q - Q0) + (Q1 - Q0) - 1) / (Q1 - Q0); }
  template <int SET, int LO, int HI> DEV void readRange(int aslot, int ks, int boff) {
    if constexpr (LO < HI) { readOp<SET, rord(LO)>(aslot, ks, boff); readRange<SET, LO + 1, HI>(aslot, ks, boff); }
  }
  static constexpr int wslotA(int U) { return (((U & 1) ? (U - 1) / 2 : (U - 2 + 2 * C::P) / 2)) % C::RA; }
  static constexpr int wslotB(int U) { return C::KB == 32 ? U % C::RB : (((U & 1) ? (U - 1) / 2 : (U - 2 + 2 * C::P) / 2)) % C::RB; }
  static constexpr int rslotA(int U1) { return (U1 / 2) % C::RA; }
  static constexpr int roffB(int U1) { return C::KB == 32 ? (U1 % C::RB) * C::BSLOT : ((U1 / 2) % C::RB) * C::BSLOT + (U1 & 1) * 32 * C::NC * 16; }
  static constexpr int nops(int V, int VHI, bool MAIN) {
    int n = 0;
    for (int v = V; v <= VHI; ++v)
      n += MAIN ? ((C::KB == 32 ? C::NB : (v & 1) ? C::NB1 : C::NB - C::NB1) + ((v & 1) ? C::NA1 : C::NA - C::NA1)) : C::nB(v) + C::nA(v);
    return n;
  }
  template <int V, int VHI, bool MAIN, int I> DEV void vop() {
    if constexpr (V <= VHI) {
      constexpr int n = nops(V, V, MAIN), U = ((V % C::P) + C::P) % C::P;
      if constexpr (I < n) gldsOp<U & 1, MAIN || C::hasB(V), I>(wslotB(U), wslotA(U));
      else vop<V + 1, VHI, MAIN, I - n>();
    }
  }
  template <int V, int VHI, bool MAIN, int LO, int HI> DEV void vopRange() {
    if constexpr (LO < HI) { vop<V, VHI, MAIN, LO>(); vopRange<V, VHI, MAIN, LO + 1, HI>(); }
  }
  template <int CP, int U, int V, int VHI, bool MAIN, bool RN, int Q> DEV void body() {
    constexpr int NM = nm(U, CP);
    if constexpr (Q < NM) {
      constexpr int PAR = U & 1;
      constexpr int NG = nops(V, VHI, MAIN);
      constexpr int RQ = C::RQ1 + (NM - NMM);
      mmaOp<PAR, Q>();
      vopRange<V, VHI, MAIN, spread(NG, 0, NM, Q), spread(NG, 0, NM, Q + 1)>();
      if constexpr (RN) readRange<PAR ^ 1, spread(NR, 0, RQ, Q), spread(NR, 0, RQ, Q + 1)>(rslotA(U + 1), PAR ^ 1, roffB(U + 1));
      sched_fence();
      body<CP, U, V, VHI, MAIN, RN, Q + 1>();
    }
  }
  template <int CP, int S, int VM, bool MAIN, class H> DEV void step(H& hook) {
    constexpr int U = S % C::P;
    constexpr bool RN = MAIN || S + 1 < C::NS;
    if constexpr (RN && (C::KB == 32 || (U & 1))) {
      static_assert(VM >= 0, "schedule"); wait_vm<VM>(); bar();
    }
    sched_fence();
    if constexpr (!MAIN) { if constexpr (C::nX(S) > 0) { hook(IC<S>{}); sched_fence(); } }
    body<CP, U, C::vlast(S - 1) + 1, C::vlast(S), MAIN, RN, 0>();
    if constexpr (C::nXP(S) > 0) { hook(IC<MAIN ? 2000 : 1000>{}); sched_fence(); }
    if constexpr (C::KB == 32 || !(U & 1)) wait_lgkm0();
    sched_fence();
  }
  template <int CP, int U, class H> DEV void mainSteps(H& hook) {
    if constexpr (U < C::P) { step<CP, C::H + U, C::vm_main(U), true>(hook); mainSteps<CP, U + 1>(hook); }
  }
  template <int CP, int Sx, int SE, class H> DEV void seqSteps(H& hook) {
    if constexpr (Sx < SE) { step<CP, Sx, C::vm_at(Sx), false>(hook); seqSteps<CP, Sx + 1, SE>(hook); }
  }
  template <int CP, class H> DEV void allSteps(H& hook) {
#pragma unroll
    for (int m = 0; m < C::MT; ++m)
#pragma unroll
      for (int j = 0; j < C::NT; ++j) asm volatile("" : "+v"(acc[m][j]));
    if constexpr (C::STATS) asm volatile("" : "+v"(rs), "+v"(rq), "+v"(cs[0]), "+v"(cs[1]), "+v"(cs[2]));
    asm volatile("s_nop 7" ::: "memory");
    sched_fence();
    seqSteps<CP, 0, C::H>(hook);
    for (int it = 0; it < (C::NMAIN - C::H) / C::P; ++it) mainSteps<CP, 0>(hook);
    seqSteps<CP, C::NMAIN, C::NS>(hook);
    asm volatile("s_nop 7\n\ts_nop 7\n\ts_nop 7" ::: "memory");
    sched_fence();
  }
  template <class BF> DEV void init(LAS char* l, const char* Abase, int lda, const char* Bbase, BF bsrc, long bstep_, int mperm, int rot) {
    lbase = (unsigned)(size_t)l;
    const int tid = threadIdx.x;
    wid = __builtin_amdgcn_readfirstlane(tid >> 6); lane = tid & 63;
    wm = wid / C::WN; wn = wid % C::WN;
    if (mperm < 0) mperm = wn;
    bstep = bstep_;
    abase = Abase; bbase = Bbase;
    if constexpr (C::ROT) { abase += rot * 128; bbase += rot * bstep; acnt = bcnt = C::NTA - rot; }
#pragma unroll
    for (int i = 0; i < C::NA; ++i) {
      int q = (wid * C::NA + i) * 64 + lane, row = q >> 3, p = q & 7, c = p ^ ((row >> 1) & 7);
      ga[i] = row * lda + c * 16;
    }
#pragma unroll
    for (int i = 0; i < C::NB; ++i) {
      int q = wid * C::CPW + i * 64 + lane;
      if (q >= C::KB * C::NC) q = C::KB * C::NC - 1;
      int k = q / C::NC, p = q % C::NC, idx = p ^ C::bswz(k);
      if constexpr (C::KIND == 2) gb[i] = bsrc(k, idx);
      else gbo[i] = bsrc(k, idx);
    }
    vopRange<C::T0, C::TPRE, false, 0, C::npro()>();
    sched_fence();
    const int fr = lane & 15, g = lane >> 4;
#pragma unroll
    for (int ks = 0; ks < 2; ++ks)
#pragma unroll
      for (int m = 0; m < (C::PERM ? C::MT : 1); ++m)
        ra[ks][m] = lbase + ((wm * C::MT + (C::PERM ? (m ^ mperm) : m)) * 16 + fr) * 128 + (((4 * ks + g) ^ (fr >> 1)) * 16);
    const int q4 = fr >> 2, p4 = fr & 3, k = 8 * g + q4;
#pragma unroll
    for (int j = 0; j < C::NT; ++j) {
      int idx;
      if (C::KIND == 1) idx = j * 8 + wn * 2 + (p4 >> 1);
      else idx = 2 * (wn * C::NT + j) + (p4 >> 1);
      rb[j] = lbase + C::LDS_B0 + k * C::NC * 16 + ((idx ^ C::bswz(k)) * 16) + 8 * (p4 & 1);
    }
#pragma unroll
    for (int m = 0; m < C::MT; ++m)
#pragma unroll
      for (int j = 0; j < C::NT; ++j) acc[m][j] = (f32x4){0.f, 0.f, 0.f, 0.f};
    rs = (f32x4){0.f, 0.f, 0.f, 0.f}; rq = rs;
    if constexpr (C::STATS) {
      u32x4 o4 = {0x3F803F80u, 0x3F803F80u, 0x3F803F80u, 0x3F803F80u};
      asm volatile("" : "+v"(o4));
      ones = __builtin_bit_cast(bf16x8, o4);
    }
#pragma unroll
    for (int j = 0; j < 3; ++j) cs[j] = (f32x4){0.f, 0.f, 0.f, 0.f};
  }
  template <int KID, class H> DEV void run(H hook) {
    sched_fence();
    wait_vm<C::vm_at(-1)>();
    bar();
    sched_fence();
    readRange<0, 0, NR>(0, 0, 0);
    wait_lgkm0();
    sched_fence();
    STAMP(KID, 1);
    if (C::STATS && wm) allSteps<1>(hook);
    else allSteps<0>(hook);
    bar();
    sched_fence();
    STAMP(KID, 2);
  }
};

DEV u32x4 xpose2(u32x2 a, u32x2 b) {
  auto x = __builtin_amdgcn_permlane16_swap(a.x, b.x, false, false);
  auto y = __builtin_amdgcn_permlane16_swap(a.y, b.y, false, false);
  return (u32x4){x[0], y[0], x[1], y[1]};
}
#define STAUX 0
template <int AUX> DEV void st16(void* base, long byte_off, u32x4 v) {
  if constexpr (AUX == 0) *(u32x4*)((char*)base + byte_off) = v;
  else {
    const __amdgpu_buffer_rsrc_t r = __builtin_amdgcn_make_buffer_rsrc(base, 0, 0x7fffffff, 0x00020000);
    __builtin_amdgcn_raw_buffer_store_b128(v, r, (int)byte_off, 0, AUX);
  }
}
DEV float phi(float t) { return (t > 0.f ? t + 1.f : __builtin_amdgcn_exp2f(t * 1.4426950408889634f)) + 1e-6f; }

using C1 = Cfg<128, 24, 2, 4, 4, 3, 4, 4, 64, 32, 8, 1, 0x1u>;
static_assert(C1::XS == 25 && C1::LDS_BYTES == 163840 && C1::H == 1 && C1::NMAIN == 25 && C1::P == 8, "K1 schedule");
__global__ __launch_bounds__(C1::NTHR, 2) void k1_qkv(const u16* __restrict__ x, const u16* __restrict__ wqkv,
                                                      u16* __restrict__ pqT, float* __restrict__ kvp, float* __restrict__ csp,
                                                      const u16* __restrict__ wo, const u16* __restrict__ wg, const u16* __restrict__ wu) {
  extern __shared__ __attribute__((aligned(16))) char smem[];
  LAS char* lds = (LAS char*)smem;
  const int bid = blockIdx.x, xcd = bid & 7, idx = bid >> 3;
  const int rb = (xcd & 1) * 8 + (idx & 7), hp = (xcd >> 1) * 4 + (idx >> 3);
  STAMP(0, 0);
  Loop<C1> L;
  auto bsrc = [&](int k, int c) -> unsigned { return (k * (3 * D_) + (c >> 3) * D_ + (c & 7) * 8) * 2; };
  L.init(lds, (const char*)(x + (long)rb * 128 * D_), D_ * 2, (const char*)(wqkv + hp * 64), bsrc, 64L * 3 * D_ * 2, -1,
         (2 * (idx & 7) + 5 * (idx >> 3) + xcd) & 15);
  const int lane = L.lane, fr = lane & 15, g = lane >> 4, wm = L.wm, wn = L.wn, wid = L.wid;
  const char* tp;
  {
    const int f = (idx * 8 + wid) * 60 + (lane < 60 ? lane : 0);
    const bool isx = f < 6144;
    const int f2 = isx ? f : f - 6144, row = f2 / 24, c = f2 - row * 24;
    tp = isx ? (const char*)x + (long)((xcd & 1) * 1024 + (xcd >> 1) * 256 + row) * 2048 + 512 + c * 64
             : (const char*)wqkv + (long)(256 + 2 * row + (xcd & 1)) * 6144 + (c >> 3) * 2048 + (xcd >> 1) * 512 + (c & 7) * 64;
  }
  const int rgp = (idx >> 3) * 2 + (xcd & 1), c8 = idx & 7, cgl = (xcd >> 1) * 4 + (c8 & 3);
  const char* cq[2];
#pragma unroll
  for (int i = 0; i < 2; ++i) {
    int q = wid * 112 + i * 64 + lane;
    if (q >= 896) q = 895;
    const int k = q / 28, p = q % 28, c = p ^ ((k >> 3) & 1);
    cq[i] = (const char*)((c8 < 4 ? wg : wu) + (long)(rgp * 128 + k) * DF_ + cgl * 224 + c * 8);
  }
  const char* tq;
  {
    int f = wid * 39 + (lane < 39 ? lane : 0);
    if (f >= 308) f = 307;
    const int row = f / 7, part = f - row * 7;
    tq = (const char*)((c8 < 4 ? wg : wu) + (long)(rgp * 128 + row) * DF_ + cgl * 224) + part * 64;
  }
  L.run<0>([&](auto sx) {
    if constexpr (decltype(sx)::value == 2000) {
      asm volatile("global_load_dword v255, %0, off" :: "v"(tq) : "memory", "v255");
      tq += 42L * DF_ * 2;
    } else if constexpr (decltype(sx)::value == 1000) {
      asm volatile("global_load_dword v255, %0, off" :: "v"(tp) : "memory", "v255");
    } else {
      constexpr int gi = decltype(sx)::value - C1::XS;
      constexpr int off = (gi & 1) ? C1::LDS_B0 + (gi >> 1) * C1::BSLOT : (gi >> 1) * C1::ATILE;
      glds_v(cq[0] + (long)gi * 32 * DF_ * 2, L.lbase + off + wid * 112 * 16);
      glds_vm(cq[1] + (long)gi * 32 * DF_ * 2, L.lbase + off + (wid * 112 + 64) * 16, (1ull << 48) - 1ull);
    }
  });
  touch((const char*)wo + (long)(idx * 32 + wid * 4 + (lane >> 4)) * 2048 + (xcd >> 2) * 1024 + (lane & 15) * 64, L.lbase + 2 * C1::ATILE + wid * 256);
  LAS char* ep = lds + C1::LDS_B0 + 2 * C1::BSLOT;
  LAS float* sR = (LAS float*)ep;
  LAS float* sC = (LAS float*)(ep + 1024);
  LAS char* sF = ep + 4096;
  {
    const float a = L.rs[0];
    const float b = (fr & 2) ? ((fr & 1) ? L.rq[3] : L.rq[2]) : ((fr & 1) ? L.rq[1] : L.rq[0]);
    if (g == (fr >> 2)) {
      float mean = a * (1.f / D_);
      float var = fmaxf(b * (1.f / D_) - mean * mean, 0.f);
      float rstd = rsqrtf(var + 1e-5f);
      *(LAS f32x2*)(sR + (wm * 64 + wn * 16 + fr) * 2) = (f32x2){rstd, -mean * rstd};
#pragma unroll
      for (int j = 0; j < 3; ++j) sC[wm * 192 + j * 64 + wn * 16 + fr] = L.cs[j][0];
    }
  }
  lbar();
  {
    const int ik = 64 + wn * 16 + fr;
    const float ck = sC[ik] + sC[192 + ik], cv = sC[64 + ik] + sC[256 + ik];
#pragma unroll
    for (int m = 0; m < 4; ++m) {
      const int am = m ^ wn, r0 = wm * 64 + am * 16 + 4 * g;
      const f32x4 u0 = *(const LAS f32x4*)(sR + r0 * 2), u1 = *(const LAS f32x4*)(sR + r0 * 2 + 4);
      const f32x4 sa = {u0[0], u0[2], u1[0], u1[2]}, sb = {u0[1], u0[3], u1[1], u1[3]};
      const f32x4 k = L.acc[m][1] * sa + sb * ck;
      const f32x4 v = L.acc[m][2] * sa + sb * cv;
      u32x2 a, b;
      a.x = pk2(phi(k[0]), phi(k[1])); a.y = pk2(phi(k[2]), phi(k[3]));
      b.x = pk2(v[0], v[1]); b.y = pk2(v[2], v[3]);
      LAS char* f = sF + (wn * 4 + wm * 2 + (am >> 1)) * 1024 + lane * 16 + (am & 1) * 8;
      *(LAS u32x2*)f = a;
      *(LAS u32x2*)(f + 16384) = b;
    }
  }
  {
    const int ic = wn * 16 + 4 * g;
    const f32x4 c4 = *(const LAS f32x4*)(sC + ic) + *(const LAS f32x4*)(sC + 192 + ic);
    u32x2 w[4];
#pragma unroll
    for (int m = 0; m < 4; ++m) {
      const f32x2 st = *(const LAS f32x2*)(sR + (wm * 64 + (m ^ wn) * 16 + fr) * 2);
      const f32x4 t = L.acc[m][0] * st.x + c4 * st.y;
      w[m].x = pk2(phi(t[0]), phi(t[1])); w[m].y = pk2(phi(t[2]), phi(t[3]));
    }
    u16* pb = pqT + ((long)(hp * 4 + wn) * S_ + rb * 128 + wm * 64 + fr) * 16 + (g >> 1) * 8;
#pragma unroll
    for (int m = 0; m < 4; m += 2) *(u32x4*)(pb + (((m + (g & 1)) ^ wn) * 16) * 16) = xpose2(w[m], w[m + 1]);
  }
  lbar();
  {
    f32x4 d = {0.f, 0.f, 0.f, 0.f}, z = d;
#pragma unroll
    for (int G = 0; G < 4; ++G) {
      const bf16x8 fk = *(const LAS bf16x8*)(sF + (wn * 4 + G) * 1024 + lane * 16);
      const bf16x8 fv = *(const LAS bf16x8*)(sF + 16384 + (((wn & 2) + wm) * 4 + G) * 1024 + lane * 16);
      d = mfma16(fk, fv, d);
      z = mfma16(fk, L.ones, z);
    }
    float* kp = kvp + ((long)(hp * 2 + (wn >> 1)) * 16 + rb) * KVP;
    *(f32x4*)(kp + ((wn & 1) * 2 + wm) * 256 + lane * 4) = d;
    if (wm == 0 && fr == 0) *(f32x4*)(kp + 1024 + (wn & 1) * 16 + 4 * g) = z;
  }
  wait_vm<4>();
  bar();
  asm volatile("" ::: "memory");
  {
    const int q4 = fr >> 2, p4 = fr & 3, k = 8 * g + q4;
#pragma unroll
    for (int t = 0; t < 2; ++t) {
      const int j = wid + 8 * t;
      if (j < 14) {
        const unsigned a0 = L.lbase + k * 448 + ((((p4 >> 1) * 14 + j) ^ ((k >> 3) & 1)) * 16) + 8 * (p4 & 1);
        f32x4 c = {0.f, 0.f, 0.f, 0.f};
#pragma unroll
        for (int gi = 0; gi < 4; ++gi) {
          const int off = (gi & 1) ? C1::LDS_B0 + (gi >> 1) * C1::BSLOT : (gi >> 1) * C1::ATILE;
          c = mfma16(L.ones, cat(rdtr(a0 + off), rdtr(a0 + off + 4 * 448)), c);
        }
        if (g == 0) csp[(long)rgp * (2 * DF_) + (c8 < 4 ? 0 : DF_) + cgl * 224 + (fr < 8 ? j * 8 + fr : (j + 14) * 8 + fr - 8)] = c[0];
      }
    }
  }
  wait_vm<0>();
  STAMP(0, 3);
}

__global__ __launch_bounds__(576) void k15_o(const u16* __restrict__ pqT, const float* __restrict__ kvp, u16* __restrict__ o,
                                             const u16* __restrict__ wd) {
  __shared__ __attribute__((aligned(16))) u16 kvT[33 * 40];
  __shared__ __attribute__((aligned(16))) unsigned dummy[64];
  const int bid = blockIdx.x, head = bid & 31, rb = bid >> 5;
  const int tid = threadIdx.x, lane = tid & 63, wid = __builtin_amdgcn_readfirstlane(tid >> 6), fr = lane & 15, g = lane >> 4;
  STAMP(1, 0);
  if (wid == 8) {
#pragma unroll
    for (int i = 0; i < 7; ++i) touch((const char*)wd + ((long)bid * 448 + i * 64 + lane) * 64, (unsigned)(size_t)(LAS unsigned*)dummy);
    bar();
    wait_vm<0>();
    return;
  }
  f32x4 v[8];
  {
    const f32x4* p = (const f32x4*)(kvp + ((long)head * 16 + (tid & 1) * 8) * KVP) + (tid >> 1);
#pragma unroll
    for (int b = 0; b < 8; ++b) v[b] = p[b * (KVP / 4)];
  }
  f32x4 z0 = {0.f, 0.f, 0.f, 0.f}, z1 = z0;
  if (wid == 0) {
    const f32x4* p = (const f32x4*)(kvp + ((long)head * 16 + (lane >> 3)) * KVP + 1024) + (lane & 7);
    z0 = p[0]; z1 = p[8 * (KVP / 4)];
  }
  bf16x8 fq[2];
#pragma unroll
  for (int m = 0; m < 2; ++m)
    fq[m] = *(const bf16x8*)(pqT + ((long)(head * 2 + (g >> 1)) * S_ + rb * 256 + wid * 32 + m * 16 + fr) * 16 + (g & 1) * 8);
  {
    f32x4 s = ((v[0] + v[1]) + (v[2] + v[3])) + ((v[4] + v[5]) + (v[6] + v[7]));
#pragma unroll
    for (int r = 0; r < 4; ++r) s[r] += __shfl_xor(s[r], 1);
    const int it = tid >> 1, blk = it >> 6, l2 = it & 63;
    const int e = (blk & 1) * 16 + (l2 & 15), d = (blk >> 1) * 16 + (l2 >> 4) * 4 + (tid & 1) * 2;
    *(unsigned*)(kvT + e * 40 + d) = (tid & 1) ? pk2(s[2], s[3]) : pk2(s[0], s[1]);
  }
  if (wid == 0) {
    f32x4 z = z0 + z1;
#pragma unroll
    for (int r = 0; r < 4; ++r) { z[r] += __shfl_xor(z[r], 8); z[r] += __shfl_xor(z[r], 16); z[r] += __shfl_xor(z[r], 32); }
    if (lane < 8) { u32x2 w; w.x = pk2(z[0], z[1]); w.y = pk2(z[2], z[3]); *(u32x2*)(kvT + 32 * 40 + lane * 4) = w; }
  }
  STAMP(1, 1);
  lbar();
  STAMP(1, 2);
  bf16x8 fa[3];
#pragma unroll
  for (int t = 0; t < 2; ++t) fa[t] = *(const bf16x8*)(kvT + (t * 16 + fr) * 40 + g * 8);
  fa[2] = *(const bf16x8*)(kvT + 32 * 40 + g * 8);
  const f32x4 z4 = {0.f, 0.f, 0.f, 0.f};
#pragma unroll
  for (int m = 0; m < 2; ++m) {
    f32x4 den = mfma16(fa[2], fq[m], z4);
    float inv = 1.f / (den[0] + 1e-6f);
    const long row = (long)rb * 256 + wid * 32 + m * 16 + fr;
    u32x2 w[2];
#pragma unroll
    for (int t = 0; t < 2; ++t) {
      f32x4 n = mfma16(fa[t], fq[m], z4);
      w[t].x = pk2(n[0] * inv, n[1] * inv); w[t].y = pk2(n[2] * inv, n[3] * inv);
    }
    *(u32x4*)(o + row * D_ + head * 32 + (g & 1) * 16 + (g >> 1) * 8) = xpose2(w[0], w[1]);
  }
  STAMP(1, 3);
}

using C2 = Cfg<128, 8, 2, 2, 4, 2, 4, 4, 64, 32, 8, 0, 0x3u>;
__global__ __launch_bounds__(C2::NTHR, 1) void k2_wo(const u16* __restrict__ o, const u16* __restrict__ wo, const u16* __restrict__ x,
                                                     u16* __restrict__ h, float* __restrict__ hstat, unsigned* __restrict__ xbuf) {
  extern __shared__ __attribute__((aligned(16))) char smem[];
  LAS char* lds = (LAS char*)smem;
  const int bid = blockIdx.x, xcd = bid & 7, idx = bid >> 3;
  const int rb = (xcd & 3) * 4 + (idx & 3), ct = (xcd >> 2) * 8 + (idx >> 2);
  STAMP(2, 0);
  Loop<C2> L;
  auto bsrc = [&](int k, int c) -> unsigned { return (k * D_ + c * 8) * 2; };
  L.init(lds, (const char*)(o + (long)rb * 128 * D_), D_ * 2, (const char*)(wo + ct * 64), bsrc, 64L * D_ * 2, 0,
         (2 * (idx >> 2) + 5 * (idx & 3) + xcd) & 15);
  const int lane = L.lane, fr = lane & 15, g = lane >> 4, wm = L.wm, wn = L.wn;
  const long off0 = ((long)rb * 128 + wm * 64 + fr) * D_ + ct * 64 + wn * 32 + (g & 1) * 16 + (g >> 1) * 8;
  u32x4 xr[4];
  L.run<2>([&](auto) {
#pragma unroll
    for (int m = 0; m < 4; ++m) xr[m] = *(const u32x4*)(x + off0 + m * 16 * D_);
    const __amdgpu_buffer_rsrc_t rx = __builtin_amdgcn_make_buffer_rsrc((void*)xbuf, 0, XBUF_BYTES, 0x00020000);
    const u32x4 sv = {SENT, SENT, SENT, SENT};
#pragma unroll
    for (int i = 0; i < 4; ++i) __builtin_amdgcn_raw_buffer_store_b128(sv, rx, bid * 16384 + i * 4096 + threadIdx.x * 16, 0, 16);
  });
  LAS float* sS = (LAS float*)lds;
#pragma unroll
  for (int m = 0; m < 4; ++m) {
    const int lr = wm * 64 + m * 16 + fr;
    float s = 0.f, q = 0.f;
    const u32x4 r = xpose2((u32x2){xr[m][0], xr[m][1]}, (u32x2){xr[m][2], xr[m][3]});
    u32x2 w[2];
#pragma unroll
    for (int j = 0; j < 2; ++j) {
      w[j].x = pk2(bflo(r[2 * j]) + L.acc[m][j][0], bfhi(r[2 * j]) + L.acc[m][j][1]);
      w[j].y = pk2(bflo(r[2 * j + 1]) + L.acc[m][j][2], bfhi(r[2 * j + 1]) + L.acc[m][j][3]);
      float f0 = bflo(w[j].x), f1 = bfhi(w[j].x), f2 = bflo(w[j].y), f3 = bfhi(w[j].y);
      s += (f0 + f1) + (f2 + f3);
      q += (f0 * f0 + f1 * f1) + (f2 * f2 + f3 * f3);
    }
    *(u32x4*)(h + off0 + m * 16 * D_) = xpose2(w[0], w[1]);
    s += __shfl_xor(s, 16); s += __shfl_xor(s, 32);
    q += __shfl_xor(q, 16); q += __shfl_xor(q, 32);
    if (g == 0) *(LAS f32x2*)(sS + (wn * 128 + lr) * 2) = (f32x2){s, q};
  }
  lbar();
  if (threadIdx.x < 128) {
    const int r = threadIdx.x;
    f32x2 v = *(LAS f32x2*)(sS + r * 2) + *(LAS f32x2*)(sS + (128 + r) * 2);
    *(f32x2*)(hstat + (((long)rb * 128 + r) * 16 + ct) * 2) = v;
  }
  STAMP(2, 3);
}

using C3 = Cfg<256, 28, 4, 2, 4, 7, 3, 4, 32, 32, 12, 2, 0x7Fu>;
constexpr int K3_LDS = C3::LDS_BYTES + 3072;
__global__ __launch_bounds__(C3::NTHR, 2) void k3_glu(const u16* __restrict__ h, const u16* __restrict__ wg, const u16* __restrict__ wu,
                                                      const float* __restrict__ hstat, const float* __restrict__ csp,
                                                      u16* __restrict__ act) {
  extern __shared__ __attribute__((aligned(16))) char smem[];
  LAS char* lds = (LAS char*)smem;
  const int bid = blockIdx.x, xcd = bid & 7, idx = bid >> 3;
  const int rb = (xcd & 1) * 4 + (idx & 3), ct = (xcd >> 1) * 8 + (idx >> 2);
  const int tid = threadIdx.x;
  STAMP(3, 0);
  auto cmap = [](int c, bool& up) -> int {
    const int w = c >= 14, r = c - 14 * w;
    up = r >= 12 ? (r & 1) : r >= 6;
    return w * 7 + (r >= 12 ? 6 : r >= 6 ? r - 6 : r);
  };
  f32x4 hs[4];
  float cp[4];
  {
    const f32x4* p = (const f32x4*)(hstat + ((long)rb * 256 + (tid >> 1)) * 32 + (tid & 1) * 16);
#pragma unroll
    for (int i = 0; i < 4; ++i) asm volatile("global_load_dwordx4 %0, %1, off offset:%2" : "=v"(hs[i]) : "v"(p), "n"(i * 16) : "memory");
    const int c = tid < 448 ? tid >> 1 : 0;
    bool up; const int ch = cmap(c >> 3, up);
    const float* q = csp + (up ? DF_ : 0) + ct * 112 + ch * 8 + (c & 7) + (long)(tid & 1) * (4 * 2 * DF_);
#pragma unroll
    for (int i = 0; i < 4; ++i) asm volatile("global_load_dword %0, %1, off" : "=v"(cp[i]) : "v"(q + (long)i * (2 * DF_)) : "memory");
  }
  Loop<C3> L;
  auto bsrc = [&](int k, int c) -> const char* {
    bool up; const int ch = cmap(c, up);
    return (const char*)((up ? wu : wg) + (long)k * DF_ + ct * 112 + ch * 8);
  };
  L.init(lds, (const char*)(h + (long)rb * 256 * D_), D_ * 2, nullptr, bsrc, 32L * DF_ * 2, 0, 0);
  const int lane = L.lane, fr = lane & 15, g = lane >> 4, wm = L.wm, wn = L.wn;
  LAS float* sC = (LAS float*)(lds + C3::LDS_BYTES);
  LAS float* sR = (LAS float*)(lds + C3::LDS_BYTES + 1024);
  asm volatile("s_waitcnt vmcnt(%8)" : "+v"(hs[0]), "+v"(hs[1]), "+v"(hs[2]), "+v"(hs[3]), "+v"(cp[0]), "+v"(cp[1]), "+v"(cp[2]), "+v"(cp[3])
               : "n"(C3::npro()) : "memory");
  {
    float c = (cp[0] + cp[1]) + (cp[2] + cp[3]);
    c += __shfl_xor(c, 1);
    if (tid < 448 && (tid & 1) == 0) sC[tid >> 1] = c;
    f32x4 t = (hs[0] + hs[1]) + (hs[2] + hs[3]);
    float s = t[0] + t[2], q = t[1] + t[3];
    s += __shfl_xor(s, 1); q += __shfl_xor(q, 1);
    float mean = s * (1.f / D_);
    float var = fmaxf(q * (1.f / D_) - mean * mean, 0.f);
    if ((tid & 1) == 0) *(LAS f32x2*)(sR + (tid >> 1) * 2) = (f32x2){-mean, var + 1e-5f};
  }
  lbar();
  {
    float nm[4];
#pragma unroll
    for (int m = 0; m < 4; ++m) nm[m] = sR[(wm * 64 + m * 16 + fr) * 2];
#pragma unroll
    for (int j = 0; j < 7; ++j) {
      const f32x4 c4 = *(const LAS f32x4*)(sC + (wn * 14 + 2 * j) * 8 + 4 * g);
#pragma unroll
      for (int m = 0; m < 4; ++m) L.acc[m][j] = c4 * nm[m];
    }
  }
  L.run<3>([&](auto) {});
  const int hi = lane >> 5, g1 = g & 1;
  LAS char* tb = lds + L.wid * 7168;
  const long apo = ((long)rb * 256 + wm * 64) * DF_ + ct * 112 + wn * 56;
  constexpr float K1c = -2.f * 0.7978845608028654f * 1.4426950408889634f, K2c = K1c * 0.044715f;
  auto glu = [&](f32x4 a, f32x4 b, float q, float r1, float r3) -> u32x2 {
    const f32x4 z = a * ((a * a) * r3 + r1);
    f32x4 e;
#pragma unroll
    for (int r = 0; r < 4; ++r) e[r] = __builtin_amdgcn_exp2f(z[r]);
    e = e * q + q;
#pragma unroll
    for (int r = 0; r < 4; ++r) e[r] = __builtin_amdgcn_rcpf(e[r]);
    const f32x4 o = (a * b) * e;
    u32x2 w; w.x = pk2(o[0], o[1]); w.y = pk2(o[2], o[3]);
    return w;
  };
#pragma unroll
  for (int mp = 0; mp < 2; ++mp) {
#pragma unroll
    for (int mm = 0; mm < 2; ++mm) {
      const int m = 2 * mp + mm;
      const float q = sR[(wm * 64 + m * 16 + fr) * 2 + 1];
      const float rstd = __builtin_amdgcn_rsqf(q), r1 = K1c * rstd, r3 = K2c * rstd * rstd * rstd;
#pragma unroll
      for (int j = 0; j < 3; ++j)
        *(LAS u32x2*)(tb + (m * 16 + fr) * 112 + j * 32 + g * 8) = glu(L.acc[m][j], L.acc[m][3 + j], q, r1, r3);
    }
    {
      const float q = sR[(wm * 64 + (2 * mp + hi) * 16 + fr) * 2 + 1];
      const float rstd = __builtin_amdgcn_rsqf(q), r1 = K1c * rstd, r3 = K2c * rstd * rstd * rstd;
      f32x4 G, U;
#pragma unroll
      for (int r = 0; r < 4; ++r) {
        auto sw = __builtin_amdgcn_permlane32_swap(__float_as_uint(L.acc[2 * mp][6][r]), __float_as_uint(L.acc[2 * mp + 1][6][r]), false, false);
        G[r] = __uint_as_float(sw[0]); U[r] = __uint_as_float(sw[1]);
      }
      *(LAS u32x2*)(tb + ((2 * mp + hi) * 16 + fr) * 112 + 96 + g1 * 8) = glu(G, U, q, r1, r3);
    }
#pragma unroll
    for (int i = mp * 3; i < (mp ? 7 : 3); ++i) {
      const int q = i * 64 + lane, row = q / 7, c = q - row * 7;
      const u32x4 v = *(const LAS u32x4*)(tb + q * 16);
      st16<STAUX>(act, (apo + (long)row * DF_ + c * 8) * 2, v);
    }
  }
  STAMP(3, 3);
}

using C4 = Cfg<128, 16, 2, 4, 4, 2, 4, 4, 64, 56, 8, 3, 0x3u>;
__global__ __launch_bounds__(C4::NTHR, 2) void k4_down(const u16* __restrict__ act, const u16* __restrict__ wd, const u16* __restrict__ h,
                                                       u16* __restrict__ out, unsigned* __restrict__ xbuf) {
  extern __shared__ __attribute__((aligned(16))) char smem[];
  LAS char* lds = (LAS char*)smem;
  const int bid = blockIdx.x, xcd = bid & 7, idx = bid >> 3;
  const int kh = xcd & 1, rb = (xcd >> 1) * 4 + (idx & 3), cb = idx >> 2;
  STAMP(4, 0);
  Loop<C4> L;
  auto bsrc = [&](int k, int c) -> unsigned { return (k * D_ + c * 8) * 2; };
  L.init(lds, (const char*)(act + (long)rb * 128 * DF_ + kh * 1792), DF_ * 2, (const char*)(wd + (long)kh * 1792 * D_ + cb * 128), bsrc, 64L * D_ * 2, 2 * kh, 0);
  const int lane = L.lane, fr = lane & 15, g = lane >> 4, wm = L.wm, wn = L.wn;
  const long off0 = ((long)rb * 128 + wm * 64 + kh * 32 + fr) * D_ + cb * 128 + wn * 32 + (g & 1) * 16 + (g >> 1) * 8;
  u32x4 xr[2];
  L.run<4>([&](auto) {
#pragma unroll
    for (int m = 0; m < 2; ++m) xr[m] = *(const u32x4*)(h + off0 + m * 16 * D_);
  });
  const __amdgpu_buffer_rsrc_t rx = __builtin_amdgcn_make_buffer_rsrc((void*)xbuf, 0, XBUF_BYTES, 0x00020000);
  const int xo = L.wid * 2048 + lane * 16;
#pragma unroll
  for (int i = 0; i < 2; ++i) {
    const f32x4 a = L.acc[2 + i][0], b = L.acc[2 + i][1];
    const u32x4 v = {pk2(a[0], a[1]), pk2(a[2], a[3]), pk2(b[0], b[1]), pk2(b[2], b[3])};
    __builtin_amdgcn_raw_buffer_store_b128(v, rx, bid * 16384 + xo + i * 1024, 0, 16);
  }
  u32x4 rv[2];
  for (int spin = 0; spin < (1 << 22); ++spin) {
    asm volatile("" ::: "memory");
    bool ok = true;
#pragma unroll
    for (int i = 0; i < 2; ++i) rv[i] = __builtin_amdgcn_raw_buffer_load_b128(rx, (bid ^ 1) * 16384 + xo + i * 1024, 0, 16);
#pragma unroll
    for (int i = 0; i < 2; ++i) ok = ok && rv[i][0] != SENT && rv[i][1] != SENT && rv[i][2] != SENT && rv[i][3] != SENT;
    if (__all(ok)) break;
  }
#pragma unroll
  for (int m = 0; m < 2; ++m) {
    const u32x4 r = xpose2((u32x2){xr[m][0], xr[m][1]}, (u32x2){xr[m][2], xr[m][3]});
    u32x2 w[2];
#pragma unroll
    for (int j = 0; j < 2; ++j) {
      const f32x4 a = L.acc[m][j];
      w[j].x = pk2(bflo(r[2 * j]) + (a[0] + bflo(rv[m][2 * j])), bfhi(r[2 * j]) + (a[1] + bfhi(rv[m][2 * j])));
      w[j].y = pk2(bflo(r[2 * j + 1]) + (a[2] + bflo(rv[m][2 * j + 1])), bfhi(r[2 * j + 1]) + (a[3] + bfhi(rv[m][2 * j + 1])));
    }
    *(u32x4*)(out + off0 + m * 16 * D_) = xpose2(w[0], w[1]);
  }
  STAMP(4, 3);
}

extern "C" void kernel_launch(void* const* d_in, const int* in_sizes, int n_in, void* d_out, int out_size, void* d_ws,
                              size_t ws_size, hipStream_t stream) {
  const u16* x = (const u16*)d_in[0];
  const u16* wqkv = (const u16*)d_in[1];
  const u16* wo = (const u16*)d_in[2];
  const u16* wg = (const u16*)d_in[4];
  const u16* wu = (const u16*)d_in[5];
  const u16* wd = (const u16*)d_in[6];
  u16* out = (u16*)d_out;
  char* ws = (char*)d_ws;
  u16* pq = (u16*)ws;                      ws += (size_t)S_ * D_ * 2;
  u16* o = (u16*)ws;                       ws += (size_t)S_ * D_ * 2;
  u16* h = (u16*)ws;                       ws += (size_t)S_ * D_ * 2;
  u16* act = (u16*)ws;                     ws += (size_t)S_ * DF_ * 2;
  float* kvp = (float*)ws;                 ws += (size_t)NH_ * 16 * KVP * 4;
  float* hstat = (float*)ws;               ws += (size_t)S_ * 16 * 2 * 4;
  float* csp = (float*)ws;                 ws += (size_t)8 * 2 * DF_ * 4;
  unsigned* xbuf = (unsigned*)ws;          ws += (size_t)XBUF_BYTES;
  (void)hipFuncSetAttribute((const void*)k1_qkv, hipFuncAttributeMaxDynamicSharedMemorySize, C1::LDS_BYTES);
  (void)hipFuncSetAttribute((const void*)k2_wo, hipFuncAttributeMaxDynamicSharedMemorySize, C2::LDS_BYTES);
  (void)hipFuncSetAttribute((const void*)k3_glu, hipFuncAttributeMaxDynamicSharedMemorySize, K3_LDS);
  (void)hipFuncSetAttribute((const void*)k4_down, hipFuncAttributeMaxDynamicSharedMemorySize, C4::LDS_BYTES);
  k1_qkv<<<256, C1::NTHR, C1::LDS_BYTES, stream>>>(x, wqkv, pq, kvp, csp, wo, wg, wu);
  k15_o<<<256, 576, 0, stream>>>(pq, kvp, o, wd);
  k2_wo<<<256, C2::NTHR, C2::LDS_BYTES, stream>>>(o, wo, x, h, hstat, xbuf);
  k3_glu<<<256, C3::NTHR, K3_LDS, stream>>>(h, wg, wu, hstat, csp, act);
  k4_down<<<256, C4::NTHR, C4::LDS_BYTES, stream>>>(act, wd, h, out, xbuf);
}
```

```cpp
#include <hip/hip_runtime.h>
#include <hip/hip_bf16.h>
#include <stdint.h>

typedef unsigned short u16;
typedef short bf16x8 __attribute__((ext_vector_type(8)));
typedef short s16x4 __attribute__((ext_vector_type(4)));
typedef float f32x4 __attribute__((ext_vector_type(4)));
typedef float f32x2 __attribute__((ext_vector_type(2)));
typedef unsigned u32x4 __attribute__((ext_vector_type(4)));
typedef unsigned u32x2 __attribute__((ext_vector_type(2)));
typedef __bf16 bf16x2_t __attribute__((ext_vector_type(2)));
#define LAS __attribute__((address_space(3)))
#define DEV __device__ __forceinline__
template <int V> struct IC { static constexpr int value = V; };

constexpr int S_ = 2048, D_ = 1024, DF_ = 3584, NH_ = 32;
constexpr int KVP = 1056;
constexpr unsigned SENT = 0xFFFFFFFFu;
constexpr int XBUF_BYTES = 256 * 16384;

DEV float bflo(unsigned v) { return __uint_as_float(v << 16); }
DEV float bfhi(unsigned v) { return __uint_as_float(v & 0xffff0000u); }
DEV unsigned pk2(float lo, float hi) {
  bf16x2_t r; r.x = (__bf16)lo; r.y = (__bf16)hi; return __builtin_bit_cast(unsigned, r);
}
template <int N> DEV void wait_vm() { asm volatile("s_waitcnt vmcnt(%0)" ::"n"(N) : "memory"); }
DEV void wait_lgkm0() { __builtin_amdgcn_s_waitcnt(0xC07F); }
DEV void bar() { __builtin_amdgcn_s_barrier(); }
DEV void lbar() { wait_lgkm0(); bar(); asm volatile("" ::: "memory"); }
DEV void sched_fence() { __builtin_amdgcn_sched_barrier(0); }
DEV void glds_v(const void* g, unsigned lds_addr) {
  asm volatile("s_mov_b32 m0, %1\n\ts_nop 0\n\tglobal_load_lds_dwordx4 %0, off" ::"v"(g), "s"(lds_addr) : "memory");
}
DEV void glds_vm(const void* g, unsigned lds_addr, unsigned long long mask) {
  asm volatile("s_mov_b32 m0, %1\n\ts_mov_b64 exec, %2\n\tglobal_load_lds_dwordx4 %0, off\n\ts_mov_b64 exec, -1"
               ::"v"(g), "s"(lds_addr), "s"(mask) : "memory");
}
DEV void glds_s(unsigned voff, const void* sbase, unsigned lds_addr) {
  asm volatile("s_mov_b32 m0, %2\n\ts_nop 0\n\tglobal_load_lds_dwordx4 %0, %1" ::"v"(voff), "s"(sbase), "s"(lds_addr) : "memory");
}
DEV void glds_sm(unsigned voff, const void* sbase, unsigned lds_addr, unsigned long long mask) {
  asm volatile("s_mov_b32 m0, %2\n\ts_mov_b64 exec, %3\n\tglobal_load_lds_dwordx4 %0, %1\n\ts_mov_b64 exec, -1"
               ::"v"(voff), "s"(sbase), "s"(lds_addr), "s"(mask) : "memory");
}
DEV void touch(const void* g, unsigned lds_addr) {
  asm volatile("s_mov_b32 m0, %1\n\ts_nop 0\n\tglobal_load_lds_dword %0, off" ::"v"(g), "s"(lds_addr) : "memory");
}
DEV void touchv(unsigned voff, const void* sbase) {
  asm volatile("global_load_dword v255, %0, %1" :: "v"(voff), "s"(sbase) : "memory", "v255");
}
typedef unsigned u32x8 __attribute__((ext_vector_type(8)));
DEV u32x4 karg4() { u32x4 r; asm volatile("s_load_dwordx4 %0, %1, 0x0" : "=s"(r) : "s"(__builtin_amdgcn_kernarg_segment_ptr())); return r; }
DEV u32x8 karg8() { u32x8 r; asm volatile("s_load_dwordx8 %0, %1, 0x0" : "=s"(r) : "s"(__builtin_amdgcn_kernarg_segment_ptr())); return r; }
template <class T> DEV void karg_wait(T& r) { asm volatile("s_waitcnt lgkmcnt(0)" : "+s"(r)); }
template <class T> DEV const char* karg_ptr(const T& r, int i) { return (const char*)(((unsigned long long)r[2 * i + 1] << 32) | r[2 * i]); }
template <int OFF> DEV void ld4(f32x4& d, unsigned voff, const void* sbase) {
  asm volatile("global_load_dwordx4 %0, %1, %2 offset:%3" : "=v"(d) : "v"(voff), "s"(sbase), "n"(OFF) : "memory");
}
DEV void ld1(float& d, unsigned voff, const void* sbase) {
  asm volatile("global_load_dword %0, %1, %2" : "=v"(d) : "v"(voff), "s"(sbase) : "memory");
}
template <int CTRL> DEV float dpp(float v) { return __int_as_float(__builtin_amdgcn_update_dpp(0, __float_as_int(v), CTRL, 0xF, 0xF, true)); }
#define SENTSC ""
DEV void stsent(unsigned voff, const void* sbase) {
  const u32x4 sv = {SENT, SENT, SENT, SENT};
  asm volatile("global_store_dwordx4 %0, %1, %2 " SENTSC "\n\ts_nop 1" :: "v"(voff), "v"(sv), "s"(sbase) : "memory");
}
DEV float fsel(bool c, float a, float b) { return c ? a : b; }
DEV f32x4 mfma16(bf16x8 a, bf16x8 b, f32x4 c) { return __builtin_amdgcn_mfma_f32_16x16x32_bf16(a, b, c, 0, 0, 0); }
DEV void mfma_acc(f32x4& c, bf16x8 a, bf16x8 b) {
  asm volatile("v_mfma_f32_16x16x32_bf16 %0, %1, %2, %0" : "+v"(c) : "v"(a), "v"(b));
}
DEV void mfma_z(f32x4& c, bf16x8 a, bf16x8 b) {
  asm volatile("v_mfma_f32_16x16x32_bf16 %0, %1, %2, 0" : "=&v"(c) : "v"(a), "v"(b));
}
template <bool Z> DEV void mfma_a(f32x4& c, bf16x8 a, bf16x8 b) { if constexpr (Z) mfma_z(c, a, b); else mfma_acc(c, a, b); }
DEV bf16x8 cat(s16x4 a, s16x4 b) { return __builtin_shufflevector(a, b, 0, 1, 2, 3, 4, 5, 6, 7); }
DEV s16x4 rdtr(unsigned a) { return __builtin_amdgcn_ds_read_tr16_b64_v4i16((LAS s16x4*)(size_t)a); }

#define FINEW 0
#define STAMP(kid, i)
#define SENTS 1
template <int BM_, int NC_, int WM_, int WN_, int MT_, int NT_, int RA_, int RB_, int KB_, int NS_, int P_, int KIND_, unsigned TRANS_>
struct Cfg {
  static constexpr int BM = BM_, NC = NC_, WM = WM_, WN = WN_, MT = MT_, NT = NT_, RA = RA_, RB = RB_, KB = KB_, NS = NS_, P = P_;
  static constexpr int KIND = KIND_;
  static constexpr unsigned TRANS = TRANS_;
  static constexpr bool STATS = KIND == 1, PERM = KIND == 1 || KIND == 3;
  static constexpr int NW = WM * WN, NTHR = NW * 64;
  static constexpr int ATILE = BM * 128, BSLOT = KIND_ == 2 ? 15360 : KB * NC * 16;
  static constexpr int BHF = KIND_ == 2 ? 4 * 224 : 4 * NC * 16;
  static constexpr int NA = BM * 8 / NTHR;
  static constexpr int CPW = KB * NC / NW;
  static constexpr int NB = (CPW + 63) / 64;
  static constexpr int NA1 = KB == 64 ? NA / 2 : (NA + 1) / 2, NB1 = KB == 64 ? (NB + 1) / 2 : NB;
  static constexpr int LDS_B0 = RA * ATILE, LDS_BYTES = RA * ATILE + RB * BSLOT;
  static constexpr int NTA = NS / 2;
#define RQN 7
#define RQD 8
  static constexpr int RQ1 = (MT * NT * RQN) / RQD;
  static_assert(BM * 8 % NTHR == 0, "A tile split");
  static_assert((KB * NC) % NW == 0, "B slot split");
  static_assert(P % 2 == 0 && (P / 2) % RA == 0 && (KB == 32 ? P % RB == 0 : (P / 2) % RB == 0), "period");
  static_assert(BM == WM * MT * 16, "M tiling");
  static_assert(NC * 8 == WN * NT * 16, "N tiling");
  DEV static constexpr int bswz(int k) {
    return NC == 28 ? 2 * ((k >> 3) & 1)
         : (NC % 16 == 8) ? 2 * (((k >> 1) & 1) | (((k >> 3) & 1) << 1))
                          : 2 * ((k & 3) | (((k >> 3) & 1) << 2));
  }
  static constexpr bool hasB(int t) {
    if (KB == 32) return t + RB >= 0 && t + RB < NS;
    int tile = (t & 1) ? ((t - 1) >> 1) + RB : ((t - 2) >> 1) + RB;
    return tile >= 0 && tile < NTA;
  }
  static constexpr bool hasA(int t) {
    int tile = (t & 1) ? ((t - 1) >> 1) + RA : ((t - 2) >> 1) + RA;
    return tile >= 0 && tile < NTA;
  }
  static constexpr int nB(int t) { return !hasB(t) ? 0 : KB == 32 ? NB : (t & 1) ? NB1 : NB - NB1; }
  static constexpr int nA(int t) { return !hasA(t) ? 0 : (t & 1) ? NA1 : NA - NA1; }
  static constexpr int T0 = -(2 * (RA > RB ? RA : RB) + 2);
  static constexpr int lastIssue() { int l = 0; for (int t = 0; t < NS; ++t) if (nA(t) + nB(t) > 0) l = t; return l; }
  static constexpr int XS = lastIssue() + 1;
  static constexpr int nX(int t) {
    if (KIND == 0) return t == XS ? 4 : 0;
    if (KIND == 1) return t == XS ? 2 : 0;
    if (KIND == 2) return (t == 30 ? 1 : 0) + ((t == 26 || t == 27) ? 4 : 0) + (t == SENTS ? 2 : 0);
    return t == XS ? 2 : 0;
  }
  static constexpr int hk(int t) { return KIND == 2 && ((t >= 26 && t <= 30) || t == SENTS) ? 2 : nX(t) > 0 ? 1 : 0; }
  static constexpr int nXP(int t) { return KIND == 1 && t == 0 ? 1 : 0; }
  static constexpr int TPRE = KB == 32 ? -3 : -4, RATE = KB == 32 ? 2 : 4;
  static constexpr int vlast(int s) { int v = TPRE + RATE * (s + 1); return s < 0 ? TPRE : (v < s ? v : s); }
  static constexpr int nhead() { int s = 0; while (vlast(s - 1) != s - 1) ++s; return s; }
  static constexpr int H = nhead();
  static constexpr int TF = KB == 32 ? (-RB > -2 * RA + 2 ? -RB : -2 * RA + 2) : -2 * (RA < RB ? RA : RB) + 2;
  static constexpr int nfirst() { int n = 0; for (int t = T0; t <= TF; ++t) n += nA(t) + nB(t); return n; }
  static constexpr int npro() { int n = 0; for (int t = T0; t <= TPRE; ++t) n += nA(t) + nB(t); return n; }
  static constexpr int nmain() {
    int lim = 0;
    while (lim < NS && hasA(lim) && hasB(lim)) ++lim;
    return H + ((lim - H) / P) * P;
  }
  static constexpr int vm_at(int s) {
    int posA[64] = {}, posB[128] = {}, cntA[64] = {}, cntB[128] = {};
    int tot = 0;
    for (int r = -1; r < s || r < 0; ++r) {
      if (r >= 0) tot += nX(r);
      for (int t = (r < 0 ? T0 : vlast(r - 1) + 1); t <= vlast(r); ++t) {
        const int tl = (t & 1) ? ((t - 1) >> 1) : ((t - 2) >> 1);
        if (nB(t)) { tot += nB(t); const int i = KB == 32 ? t + RB : tl + RB; posB[i] = tot; cntB[i] += nB(t); }
        if (nA(t)) { tot += nA(t); posA[tl + RA] = tot; cntA[tl + RA] += nA(t); }
      }
      if (r >= 0) tot += nXP(r);
    }
    int nb = s + 1;
    if (nb >= NS) return 63;
    const int ib = KB == 32 ? nb : nb / 2;
    if (cntB[ib] != NB || cntA[nb / 2] != NA) return -1;
    int need = posB[ib] > posA[nb / 2] ? posB[ib] : posA[nb / 2];
    int v = tot - need;
    return v > 63 ? 63 : v;
  }
  static constexpr int NMAIN = nmain();
  static constexpr int vm_main(int u) {
    int v = 63;
    for (int s = H + u; s < NMAIN; s += P) { int x = vm_at(s); v = x < v ? x : v; }
    return v;
  }
};

template <class C> struct Loop {
  unsigned lbase;
  const char* abase;
  unsigned ga[C::NA];
  unsigned gbo[C::NB];
  const char* bbase;
  unsigned bdst;
  long bstep;
  int wid, lane, wm, wn;
  unsigned ra[2][C::PERM ? C::MT : 1];
  unsigned rb[C::NT];
  bf16x8 FA[2][C::MT];
  s16x4 FBh[2][C::NT][2];
  f32x4 acc[C::MT][C::NT];
  f32x4 rs, rq;
  f32x4 cs[3];
  bf16x8 ones;

  template <int SET, int I> DEV void readOp(int aslot, int ks, int boff) {
    if constexpr (I < C::MT) {
      unsigned a = C::PERM ? ra[ks][C::PERM ? I : 0] : ra[ks][0] + I * 2048;
      FA[SET][I] = *(const LAS bf16x8*)(size_t)(a + aslot * C::ATILE);
    } else {
      constexpr int j = (I - C::MT) / 2, hf = (I - C::MT) & 1;
      FBh[SET][j][hf] = rdtr(rb[j] + boff + hf * C::BHF);
    }
  }
  static constexpr int rord(int i) {
    if (i == 0) return 0;
    if (i <= 2 * C::NT) return C::MT + i - 1;
    return i - 2 * C::NT;
  }
  template <int SET> DEV bf16x8 FB(int j) const { return cat(FBh[SET][j][0], FBh[SET][j][1]); }
  static constexpr int NMM = C::MT * C::NT, NR = C::MT + 2 * C::NT;
  static constexpr int nm(int U, int CP) { return NMM + (C::STATS ? ((U & 1) == CP ? 5 : 2) : 0); }
  template <int SET, int Q, bool Z> DEV void mmaOp() {
    if constexpr (Q < NMM) {
      constexpr int m = Q / C::NT, j = Q % C::NT;
      if constexpr ((C::TRANS >> j) & 1) mfma_a<Z>(acc[m][j], FB<SET>(j), FA[SET][m]);
      else mfma_a<Z>(acc[m][j], FA[SET][m], FB<SET>(j));
    } else if constexpr (Q == NMM) {
      mfma_a<Z>(rs, ones, FA[SET][0]);
    } else if constexpr (Q == NMM + 1) {
      mfma_a<Z>(rq, FA[SET][0], FA[SET][0]);
    } else {
      mfma_acc(cs[Q - NMM - 2], ones, FB<SET>(Q - NMM - 2));
    }
  }
  template <int PAR, bool IB, int I> DEV void gldsOp(int bslot, int aslot) {
    constexpr int nb = !IB ? 0 : C::KB == 32 ? C::NB : PAR ? C::NB1 : C::NB - C::NB1;
    if constexpr (I < nb) {
      constexpr int i = (C::KB == 64 && !PAR) ? C::NB1 + I : I;
      constexpr int rem = C::CPW - 64 * i;
      const unsigned dst = bdst + bslot * C::BSLOT + i * 1024;
      if constexpr (rem >= 64) glds_s(gbo[i], bbase, dst);
      else glds_sm(gbo[i], bbase, dst, (1ull << (rem & 63)) - 1ull);
      if constexpr (i == C::NB - 1) { bbase += bstep; asm volatile("" : "+s"(bbase)); }
    } else {
      constexpr int i = (PAR ? 0 : C::NA1) + (I - nb);
      glds_s(ga[i], abase, lbase + aslot * C::ATILE + (wid * C::NA + i) * 1024);
      if constexpr (i == C::NA - 1) { abase += 128; asm volatile("" : "+s"(abase)); }
    }
  }
  static constexpr int spread(int N, int Q0, int Q1, int Q) { return Q <= Q0 ? 0 : Q >= Q1 ? N : (N * (Q - Q0) + (Q1 - Q0) - 1) / (Q1 - Q0); }
  template <int SET, int LO, int HI> DEV void readRange(int aslot, int ks, int boff) {
    if constexpr (LO < HI) { readOp<SET, rord(LO)>(aslot, ks, boff); readRange<SET, LO + 1, HI>(aslot, ks, boff); }
  }
  static constexpr int wslotA(int U) { return (((U & 1) ? (U - 1) / 2 : (U - 2 + 2 * C::P) / 2)) % C::RA; }
  static constexpr int wslotB(int U) { return C::KB == 32 ? U % C::RB : (((U & 1) ? (U - 1) / 2 : (U - 2 + 2 * C::P) / 2)) % C::RB; }
  static constexpr int rslotA(int U1) { return (U1 / 2) % C::RA; }
  static constexpr int roffB(int U1) { return C::KB == 32 ? (U1 % C::RB) * C::BSLOT : ((U1 / 2) % C::RB) * C::BSLOT + (U1 & 1) * 32 * C::NC * 16; }
  static constexpr int nops(int V, int VHI, bool MAIN) {
    int n = 0;
    for (int v = V; v <= VHI; ++v)
      n += MAIN ? ((C::KB == 32 ? C::NB : (v & 1) ? C::NB1 : C::NB - C::NB1) + ((v & 1) ? C::NA1 : C::NA - C::NA1)) : C::nB(v) + C::nA(v);
    return n;
  }
  template <int V, int VHI, bool MAIN, int I> DEV void vop() {
    if constexpr (V <= VHI) {
      constexpr int n = nops(V, V, MAIN), U = ((V % C::P) + C::P) % C::P;
      if constexpr (I < n) gldsOp<U & 1, MAIN || C::hasB(V), I>(wslotB(U), wslotA(U));
      else vop<V + 1, VHI, MAIN, I - n>();
    }
  }
  template <int V, int VHI, bool MAIN, int LO, int HI> DEV void vopRange() {
    if constexpr (LO < HI) { vop<V, VHI, MAIN, LO>(); vopRange<V, VHI, MAIN, LO + 1, HI>(); }
  }
  template <int CP, int U, int V, int VHI, bool MAIN, bool RN, int Q, int HS, class H> DEV void body(H& hook) {
    constexpr int NM = nm(U, CP);
    if constexpr (Q < NM) {
      constexpr int PAR = U & 1;
      constexpr int NG = nops(V, VHI, MAIN);
      constexpr int RQ = C::RQ1 + (NM - NMM);
      mmaOp<PAR, Q, !MAIN && V == C::TPRE + 1>();
      vopRange<V, VHI, MAIN, spread(NG, 0, NM, Q), spread(NG, 0, NM, Q + 1)>();
      if constexpr (RN) readRange<PAR ^ 1, spread(NR, 0, RQ, Q), spread(NR, 0, RQ, Q + 1)>(rslotA(U + 1), PAR ^ 1, roffB(U + 1));
      if constexpr (HS >= 0) hook(IC<HS + Q>{});
      sched_fence();
      body<CP, U, V, VHI, MAIN, RN, Q + 1, HS>(hook);
    }
  }
  template <int CP, int S, int VM, bool MAIN, class H> DEV void step(H& hook) {
    constexpr int U = S % C::P;
    constexpr bool RN = MAIN || S + 1 < C::NS;
    if constexpr (RN && (C::KB == 32 || (U & 1))) {
      static_assert(VM >= 0, "schedule"); wait_vm<VM>(); bar();
    }
    sched_fence();
    constexpr int HM = MAIN ? 0 : C::hk(S);
    if constexpr (HM == 1) { hook(IC<S>{}); sched_fence(); }
    if constexpr (HM == 3) { if (wid >= C::NW / 2) hook(IC<S>{}); sched_fence(); }
    body<CP, U, C::vlast(S - 1) + 1, C::vlast(S), MAIN, RN, 0, HM == 2 ? S * 100 : -1>(hook);
    if constexpr (HM == 3) { if (wid < C::NW / 2) hook(IC<S>{}); sched_fence(); }
    if constexpr (C::nXP(S) > 0) { hook(IC<MAIN ? 2000 : 1000>{}); sched_fence(); }
    if constexpr (C::KB == 32 || !(U & 1)) wait_lgkm0();
    sched_fence();
  }
  template <int CP, int U, class H> DEV void mainSteps(H& hook) {
    if constexpr (U < C::P) { step<CP, C::H + U, C::vm_main(U), true>(hook); mainSteps<CP, U + 1>(hook); }
  }
  template <int CP, int Sx, int SE, class H> DEV void seqSteps(H& hook) {
    if constexpr (Sx < SE) { step<CP, Sx, C::vm_at(Sx), false>(hook); seqSteps<CP, Sx + 1, SE>(hook); }
  }
  template <int KID, int CP, class H> DEV void allSteps(H& hook) {
    pinAcc();
    sched_fence();
    ts();
    wait_vm<C::vm_at(-1)>();
    ts();
    bar();
    sched_fence();
    ts();
    readRange<0, 0, NR>(0, 0, 0);
    wait_lgkm0();
    sched_fence();
    STAMP(KID, 1);
    ts();
    pinAcc();
    asm volatile("s_nop 7" ::: "memory");
    sched_fence();
    seqSteps<CP, 0, C::H>(hook);
    for (int it = 0; it < (C::NMAIN - C::H) / C::P; ++it) mainSteps<CP, 0>(hook);
    seqSteps<CP, C::NMAIN, C::NS>(hook);
    asm volatile("s_nop 7\n\ts_nop 7\n\ts_nop 7" ::: "memory");
    sched_fence();
  }
  DEV void ts() {
  }
  template <int KID> DEV void tsdump() {
  }
  template <class BF> DEV void init0(LAS char* l, int lda, BF bsrc, int mperm) {
    lbase = (unsigned)(size_t)l;
    const int tid = threadIdx.x;
    wid = __builtin_amdgcn_readfirstlane(tid >> 6); lane = tid & 63;
    wm = wid / C::WN; wn = wid % C::WN;
    if (mperm < 0) mperm = wn;
#pragma unroll
    for (int i = 0; i < C::NA; ++i) {
      int q = (wid * C::NA + i) * 64 + lane, row = q >> 3, p = q & 7, c = p ^ ((row >> 1) & 7);
      ga[i] = row * lda + c * 16;
      asm volatile("" : "+v"(ga[i]));
    }
#pragma unroll
    for (int i = 0; i < C::NB; ++i) {
      if constexpr (C::KIND == 2) {
        int q = i * 64 + lane;
        if (q >= 112) q = 111;
        const int k = q / 14, c = q - k * 14;
        gbo[i] = bsrc((wid & 3) * 8 + k, c);
      } else {
        const int q = wid * C::CPW + i * 64 + lane;
        const int k = q / C::NC, p = q % C::NC, idx = p ^ C::bswz(k);
        gbo[i] = bsrc(k, idx);
      }
      asm volatile("" : "+v"(gbo[i]));
    }
    bdst = lbase + C::LDS_B0 + (C::KIND == 2 ? wid * 1920 + (wid >> 2) * 16 : wid * C::CPW * 16);
    mp = mperm;
    sched_fence();
  }
  int mp;
  DEV void init2() {
    const int mperm = mp;
    const int fr = lane & 15, g = lane >> 4;
#pragma unroll
    for (int ks = 0; ks < 2; ++ks)
#pragma unroll
      for (int m = 0; m < (C::PERM ? C::MT : 1); ++m) {
        ra[ks][m] = lbase + ((wm * C::MT + (C::PERM ? (m ^ mperm) : m)) * 16 + fr) * 128 + (((4 * ks + g) ^ (fr >> 1)) * 16);
        asm volatile("" : "+v"(ra[ks][m]));
      }
    const int q4 = fr >> 2, p4 = fr & 3, k = 8 * g + q4;
#pragma unroll
    for (int j = 0; j < C::NT; ++j) {
      if constexpr (C::KIND == 2) {
        const int mat = j < 3 ? 0 : j < 6 ? 1 : (p4 >> 1);
        const int ch = wn * 7 + (j < 6 ? 2 * (j % 3) + (p4 >> 1) : 6);
        rb[j] = lbase + C::LDS_B0 + (mat * 4 + g) * 1920 + mat * 16 + q4 * 224 + ch * 16 + 8 * (p4 & 1);
      } else {
        int idx;
        if (C::KIND == 1) idx = j * 8 + wn * 2 + (p4 >> 1);
        else idx = 2 * (wn * C::NT + j) + (p4 >> 1);
        rb[j] = lbase + C::LDS_B0 + k * C::NC * 16 + ((idx ^ C::bswz(k)) * 16) + 8 * (p4 & 1);
      }
      asm volatile("" : "+v"(rb[j]));
    }
    sched_fence();
  }
  DEV void init1(const char* Abase, const char* Bbase, long bstep_) {
    bstep = bstep_;
    abase = Abase; bbase = Bbase;
    vopRange<C::T0, C::TPRE, false, 0, C::nfirst()>();
    sched_fence();
    bar();
    sched_fence();
    vopRange<C::T0, C::TPRE, false, C::nfirst(), C::npro()>();
    sched_fence();
    init2();
    if constexpr (C::STATS) {
      u32x4 o4 = {0x3F803F80u, 0x3F803F80u, 0x3F803F80u, 0x3F803F80u};
      asm volatile("" : "+v"(o4));
      ones = __builtin_bit_cast(bf16x8, o4);
    }
#pragma unroll
    for (int j = 0; j < 3; ++j) cs[j] = (f32x4){0.f, 0.f, 0.f, 0.f};
  }
  DEV void pinAcc() {
    if constexpr (C::STATS) asm volatile("" : "+v"(cs[0]), "+v"(cs[1]), "+v"(cs[2]));
  }
  template <int KID, class H> DEV void run(H hook) {
    if (C::STATS && wm) allSteps<KID, 1>(hook);
    else allSteps<KID, 0>(hook);
    ts();
    STAMP(KID, 2);
  }
};

DEV u32x4 xpose2(u32x2 a, u32x2 b) {
  auto x = __builtin_amdgcn_permlane16_swap(a.x, b.x, false, false);
  auto y = __builtin_amdgcn_permlane16_swap(a.y, b.y, false, false);
  return (u32x4){x[0], y[0], x[1], y[1]};
}
#define STAUX 0
template <int AUX> DEV void st16(void* base, long byte_off, u32x4 v) {
  if constexpr (AUX == 0) *(u32x4*)((char*)base + byte_off) = v;
  else {
    const __amdgpu_buffer_rsrc_t r = __builtin_amdgcn_make_buffer_rsrc(base, 0, 0x7fffffff, 0x00020000);
    __builtin_amdgcn_raw_buffer_store_b128(v, r, (int)byte_off, 0, AUX);
  }
}
DEV u32x2 phi4(f32x4 u) {
  const f32x4 a = u * 1.4426950408889634f;
  f32x4 e, m;
#pragma unroll
  for (int r = 0; r < 4; ++r) { e[r] = __builtin_amdgcn_fmed3f(__builtin_amdgcn_exp2f(a[r]), 0.f, 1.f); m[r] = __builtin_fmaxf(u[r], 1e-6f); }
  const f32x4 p = m + e;
  u32x2 w; w.x = pk2(p[0], p[1]); w.y = pk2(p[2], p[3]);
  return w;
}

using C1 = Cfg<128, 24, 2, 4, 4, 3, 4, 4, 64, 32, 8, 1, 0x1u>;
static_assert(C1::XS == 25 && C1::LDS_BYTES == 163840 && C1::H == 1 && C1::NMAIN == 25 && C1::P == 8, "K1 schedule");
__global__ __launch_bounds__(C1::NTHR, 2) void k1_qkv(const u16* __restrict__ x, const u16* __restrict__ wqkv,
                                                      u16* __restrict__ pqT, float* __restrict__ kvp,
                                                      const u16* __restrict__ wo, const u16* __restrict__ wg) {
  extern __shared__ __attribute__((aligned(16))) char smem[];
  LAS char* lds = (LAS char*)smem;
  const int bid = blockIdx.x, xcd = bid & 7, idx = bid >> 3;
  const int rb = (xcd & 1) * 8 + (idx & 7), hp = (xcd >> 1) * 4 + (idx >> 3);
  STAMP(0, 0);
  u32x4 ka = karg4();
  sched_fence();
  Loop<C1> L;
  auto bsrc = [&](int k, int c) -> unsigned { return (k * (3 * D_) + (c >> 3) * D_ + (c & 7) * 8) * 2; };
  L.init0(lds, D_ * 2, bsrc, -1);
  karg_wait(ka);
  L.init1(karg_ptr(ka, 0) + (long)rb * 128 * D_ * 2, karg_ptr(ka, 1) + hp * 128, 64L * 3 * D_ * 2);
  const int lane = L.lane, fr = lane & 15, g = lane >> 4, wm = L.wm, wn = L.wn, wid = L.wid;
  const char* tp;
  {
    const int f = (idx * 8 + wid) * 60 + (lane < 60 ? lane : 0);
    const bool isx = f < 6144;
    const int f2 = isx ? f : f - 6144, row = f2 / 24, c = f2 - row * 24;
    const unsigned to = isx ? ((xcd & 1) * 1024 + (xcd >> 1) * 256 + row) * 2048 + 512 + c * 64
                            : (256 + 2 * row + (xcd & 1)) * 6144 + (c >> 3) * 2048 + (xcd >> 1) * 512 + (c & 7) * 64;
    tp = (isx ? karg_ptr(ka, 0) : karg_ptr(ka, 1)) + to;
  }
  L.run<0>([&](auto sx) {
    if constexpr (decltype(sx)::value == 1000) {
      asm volatile("global_load_dword v255, %0, off" :: "v"(tp) : "memory", "v255");
    } else {
      const unsigned o1 = (idx * 32 + wid * 4 + (lane >> 4)) * 2048 + (xcd >> 2) * 1024 + (lane & 15) * 64;
      touchv(o1, wo);
      const unsigned o2 = ((bid * 8 + wid) * 56 + (lane < 56 ? lane : 0)) * 64;
      touchv(o2, wg);
    }
  });
  LAS char* ep = lds;
  LAS float* sR = (LAS float*)ep;
  LAS float* sC = (LAS float*)(ep + 1024);
  LAS char* sF = ep + 4096;
  {
    const float a = L.rs[0];
    const float b = (fr & 2) ? ((fr & 1) ? L.rq[3] : L.rq[2]) : ((fr & 1) ? L.rq[1] : L.rq[0]);
    if (g == (fr >> 2)) {
      float mean = a * (1.f / D_);
      float var = fmaxf(b * (1.f / D_) - mean * mean, 0.f);
      float rstd = rsqrtf(var + 1e-5f);
      *(LAS f32x2*)(sR + (wm * 64 + wn * 16 + fr) * 2) = (f32x2){rstd, -mean * rstd};
#pragma unroll
      for (int j = 0; j < 3; ++j) sC[wm * 192 + j * 64 + wn * 16 + fr] = L.cs[j][0];
    }
  }
  lbar();
  {
    const int ik = 64 + wn * 16 + fr;
    const float ck = sC[ik] + sC[192 + ik], cv = sC[64 + ik] + sC[256 + ik];
#pragma unroll
    for (int m = 0; m < 4; ++m) {
      const int am = m ^ wn, r0 = wm * 64 + am * 16 + 4 * g;
      const f32x4 u0 = *(const LAS f32x4*)(sR + r0 * 2), u1 = *(const LAS f32x4*)(sR + r0 * 2 + 4);
      const f32x4 sa = {u0[0], u0[2], u1[0], u1[2]}, sb = {u0[1], u0[3], u1[1], u1[3]};
      const f32x4 k = L.acc[m][1] * sa + (sb * ck + 1e-6f);
      const f32x4 v = L.acc[m][2] * sa + sb * cv;
      u32x2 a = phi4(k), b;
      b.x = pk2(v[0], v[1]); b.y = pk2(v[2], v[3]);
      LAS char* f = sF + (wn * 4 + wm * 2 + (am >> 1)) * 1024 + lane * 16 + (am & 1) * 8;
      *(LAS u32x2*)f = a;
      *(LAS u32x2*)(f + 16384) = b;
    }
  }
  {
    const int ic = wn * 16 + 4 * g;
    const f32x4 c4 = *(const LAS f32x4*)(sC + ic) + *(const LAS f32x4*)(sC + 192 + ic);
    u32x2 w[4];
#pragma unroll
    for (int m = 0; m < 4; ++m) {
      const f32x2 st = *(const LAS f32x2*)(sR + (wm * 64 + (m ^ wn) * 16 + fr) * 2);
      w[m] = phi4(L.acc[m][0] * st.x + (c4 * st.y + 1e-6f));
    }
    u16* pb = pqT + ((long)(hp * 4 + wn) * S_ + rb * 128 + wm * 64 + fr) * 16 + (g >> 1) * 8;
#pragma unroll
    for (int m = 0; m < 4; m += 2) *(u32x4*)(pb + (((m + (g & 1)) ^ wn) * 16) * 16) = xpose2(w[m], w[m + 1]);
  }
  lbar();
  {
    f32x4 d = {0.f, 0.f, 0.f, 0.f}, z = d;
#pragma unroll
    for (int G = 0; G < 4; ++G) {
      const bf16x8 fk = *(const LAS bf16x8*)(sF + (wn * 4 + G) * 1024 + lane * 16);
      const bf16x8 fv = *(const LAS bf16x8*)(sF + 16384 + (((wn & 2) + wm) * 4 + G) * 1024 + lane * 16);
      d = mfma16(fk, fv, d);
      z = mfma16(fk, L.ones, z);
    }
    float* kp = kvp + ((long)(hp * 2 + (wn >> 1)) * 16 + rb) * KVP;
    *(f32x4*)(kp + ((wn & 1) * 2 + wm) * 256 + lane * 4) = d;
    if (wm == 0 && fr == 0) *(f32x4*)(kp + 1024 + (wn & 1) * 16 + 4 * g) = z;
  }
  wait_vm<0>();
  L.ts();
  STAMP(0, 3);
  L.tsdump<0>();
}

__global__ __launch_bounds__(512) void k15_o(const u16* __restrict__ pqT, const float* __restrict__ kvp, u16* __restrict__ o,
                                             const u16* __restrict__ wg, const u16* __restrict__ wu, float* __restrict__ csp) {
  __shared__ __attribute__((aligned(16))) u16 kvT[33 * 40];
  __shared__ __attribute__((aligned(16))) float sCS[16 * 228];
  const int bid = blockIdx.x, head = bid & 31, rb = bid >> 5;
  const int tid = threadIdx.x, lane = tid & 63, wid = __builtin_amdgcn_readfirstlane(tid >> 6), fr = lane & 15, g = lane >> 4;
  STAMP(1, 0);
  f32x4 z0 = {0.f, 0.f, 0.f, 0.f}, z1 = z0;
  if (wid == 0) {
    const f32x4* p = (const f32x4*)(kvp + ((long)head * 16 + (lane >> 3)) * KVP + 1024) + (lane & 7);
    z0 = p[0]; z1 = p[8 * (KVP / 4)];
  }
  sched_fence();
  f32x4 v[8];
  {
    const f32x4* p = (const f32x4*)(kvp + ((long)head * 16 + (tid & 1) * 8) * KVP) + (tid >> 1);
#pragma unroll
    for (int b = 0; b < 8; ++b) v[b] = p[b * (KVP / 4)];
  }
  bf16x8 fq[2];
#pragma unroll
  for (int m = 0; m < 2; ++m)
    fq[m] = *(const bf16x8*)(pqT + ((long)(head * 2 + (g >> 1)) * S_ + rb * 256 + wid * 32 + m * 16 + fr) * 16 + (g & 1) * 8);
  const int xcd = bid & 7, idx = bid >> 3;
  const int rgp = (idx >> 3) * 2 + (xcd & 1), c8 = idx & 7, cgl = (xcd >> 1) * 4 + (c8 & 3);
  const int ct = tid < 448 ? tid : 447, r0 = ct / 28, cc = ct - r0 * 28;
  sched_fence();
  bar();
  sched_fence();
  u32x4 wv[8];
  {
    const u16* p = (c8 < 4 ? wg : wu) + (long)(rgp * 128 + r0) * DF_ + cgl * 224 + cc * 8;
#pragma unroll
    for (int i = 0; i < 8; ++i) wv[i] = *(const u32x4*)(p + (long)i * 16 * DF_);
  }
  sched_fence();
  {
    f32x4 s = ((v[0] + v[1]) + (v[2] + v[3])) + ((v[4] + v[5]) + (v[6] + v[7]));
#pragma unroll
    for (int r = 0; r < 4; ++r) s[r] += __shfl_xor(s[r], 1);
    const int it = tid >> 1, blk = it >> 6, l2 = it & 63;
    const int e = (blk & 1) * 16 + (l2 & 15), d = (blk >> 1) * 16 + (l2 >> 4) * 4 + (tid & 1) * 2;
    *(unsigned*)(kvT + e * 40 + d) = (tid & 1) ? pk2(s[2], s[3]) : pk2(s[0], s[1]);
  }
  if (wid == 0) {
    f32x4 z = z0 + z1;
#pragma unroll
    for (int r = 0; r < 4; ++r) { z[r] += __shfl_xor(z[r], 8); z[r] += __shfl_xor(z[r], 16); z[r] += __shfl_xor(z[r], 32); }
    if (lane < 8) { u32x2 w; w.x = pk2(z[0], z[1]); w.y = pk2(z[2], z[3]); *(u32x2*)(kvT + 32 * 40 + lane * 4) = w; }
  }
  STAMP(1, 1);
  lbar();
  bf16x8 fa[3];
#pragma unroll
  for (int t = 0; t < 2; ++t) fa[t] = *(const bf16x8*)(kvT + (t * 16 + fr) * 40 + g * 8);
  fa[2] = *(const bf16x8*)(kvT + 32 * 40 + g * 8);
  const f32x4 z4 = {0.f, 0.f, 0.f, 0.f};
#pragma unroll
  for (int m = 0; m < 2; ++m) {
    f32x4 den = mfma16(fa[2], fq[m], z4);
    float inv = 1.f / (den[0] + 1e-6f);
    const long row = (long)rb * 256 + wid * 32 + m * 16 + fr;
    u32x2 w[2];
#pragma unroll
    for (int t = 0; t < 2; ++t) {
      f32x4 n = mfma16(fa[t], fq[m], z4);
      w[t].x = pk2(n[0] * inv, n[1] * inv); w[t].y = pk2(n[2] * inv, n[3] * inv);
    }
    *(u32x4*)(o + row * D_ + head * 32 + (g & 1) * 16 + (g >> 1) * 8) = xpose2(w[0], w[1]);
  }
  sched_fence();
  if (wid < 7) {
    f32x4 lo = {0.f, 0.f, 0.f, 0.f}, hi = lo;
#pragma unroll
    for (int i = 0; i < 8; ++i) {
      lo += (f32x4){bflo(wv[i][0]), bflo(wv[i][1]), bflo(wv[i][2]), bflo(wv[i][3])};
      hi += (f32x4){bfhi(wv[i][0]), bfhi(wv[i][1]), bfhi(wv[i][2]), bfhi(wv[i][3])};
    }
    *(f32x4*)(sCS + r0 * 228 + cc * 8) = (f32x4){lo[0], hi[0], lo[1], hi[1]};
    *(f32x4*)(sCS + r0 * 228 + cc * 8 + 4) = (f32x4){lo[2], hi[2], lo[3], hi[3]};
  }
  STAMP(1, 2);
  lbar();
  if (tid < 448) {
    const int c = tid >> 1, hf = tid & 1;
    float t = 0.f;
#pragma unroll
    for (int i = 0; i < 8; ++i) t += sCS[(hf * 8 + i) * 228 + c];
    t += __shfl_xor(t, 1);
    if (hf == 0) csp[(long)rgp * (2 * DF_) + (c8 < 4 ? 0 : DF_) + cgl * 224 + c] = t;
  }
  STAMP(1, 3);
}

using C2 = Cfg<128, 8, 2, 2, 4, 2, 4, 4, 64, 32, 8, 0, 0x3u>;
__global__ __launch_bounds__(C2::NTHR, 1) void k2_wo(const u16* __restrict__ o, const u16* __restrict__ wo, const u16* __restrict__ x,
                                                     u16* __restrict__ h, float* __restrict__ hstat) {
  extern __shared__ __attribute__((aligned(16))) char smem[];
  LAS char* lds = (LAS char*)smem;
  const int bid = blockIdx.x, xcd = bid & 7, idx = bid >> 3;
  const int rb = (xcd & 3) * 4 + (idx & 3), ct = (xcd >> 2) * 8 + (idx >> 2);
  STAMP(2, 0);
  u32x4 ka = karg4();
  sched_fence();
  Loop<C2> L;
  auto bsrc = [&](int k, int c) -> unsigned { return (k * D_ + c * 8) * 2; };
  L.init0(lds, D_ * 2, bsrc, 0);
  karg_wait(ka);
  L.init1(karg_ptr(ka, 0) + (long)rb * 128 * D_ * 2, karg_ptr(ka, 1) + ct * 128, 64L * D_ * 2);
  const int lane = L.lane, fr = lane & 15, g = lane >> 4, wm = L.wm, wn = L.wn;
  const long off0 = ((long)rb * 128 + wm * 64 + fr) * D_ + ct * 64 + wn * 32 + (g & 1) * 16 + (g >> 1) * 8;
  u32x4 xr[4];
  L.run<2>([&](auto) {
#pragma unroll
    for (int m = 0; m < 4; ++m) xr[m] = *(const u32x4*)(x + off0 + m * 16 * D_);
  });
  LAS float* sS = (LAS float*)lds;
#pragma unroll
  for (int m = 0; m < 4; ++m) {
    const int lr = wm * 64 + m * 16 + fr;
    float s = 0.f, q = 0.f;
    const u32x4 r = xpose2((u32x2){xr[m][0], xr[m][1]}, (u32x2){xr[m][2], xr[m][3]});
    u32x2 w[2];
#pragma unroll
    for (int j = 0; j < 2; ++j) {
      w[j].x = pk2(bflo(r[2 * j]) + L.acc[m][j][0], bfhi(r[2 * j]) + L.acc[m][j][1]);
      w[j].y = pk2(bflo(r[2 * j + 1]) + L.acc[m][j][2], bfhi(r[2 * j + 1]) + L.acc[m][j][3]);
      float f0 = bflo(w[j].x), f1 = bfhi(w[j].x), f2 = bflo(w[j].y), f3 = bfhi(w[j].y);
      s += (f0 + f1) + (f2 + f3);
      q += (f0 * f0 + f1 * f1) + (f2 * f2 + f3 * f3);
    }
    *(u32x4*)(h + off0 + m * 16 * D_) = xpose2(w[0], w[1]);
    s += __shfl_xor(s, 16); s += __shfl_xor(s, 32);
    q += __shfl_xor(q, 16); q += __shfl_xor(q, 32);
    if (g == 0) *(LAS f32x2*)(sS + (wn * 128 + lr) * 2) = (f32x2){s, q};
  }
  lbar();
  if (threadIdx.x < 128) {
    const int r = threadIdx.x;
    f32x2 v = *(LAS f32x2*)(sS + r * 2) + *(LAS f32x2*)(sS + (128 + r) * 2);
    *(f32x2*)(hstat + (((long)rb * 128 + r) * 16 + ct) * 2) = v;
  }
  L.ts();
  STAMP(2, 3);
  L.tsdump<2>();
}

using C3 = Cfg<256, 28, 4, 2, 4, 7, 3, 4, 32, 32, 12, 2, 0x7Fu>;
constexpr int K3_LDS = C3::LDS_BYTES + 3072;
__global__ __launch_bounds__(C3::NTHR, 2) void k3_glu(const u16* __restrict__ h, const u16* __restrict__ wg, const u16* __restrict__ wu,
                                                      const float* __restrict__ hstat,
                                                      u16* __restrict__ act, const u16* __restrict__ wd, unsigned* __restrict__ xbuf) {
  extern __shared__ __attribute__((aligned(16))) char smem[];
  LAS char* lds = (LAS char*)smem;
  const int bid = blockIdx.x, xcd = bid & 7, idx = bid >> 3;
  const int rb = (xcd & 1) * 4 + (idx & 3), ct = (xcd >> 1) * 8 + (idx >> 2);
  const int tid = threadIdx.x;
  STAMP(3, 0);
  u32x8 ka = karg8();
  sched_fence();
  auto cmap = [](int c, bool& up) -> int {
    const int w = c >= 14, r = c - 14 * w;
    up = r >= 12 ? (r & 1) : r >= 6;
    return w * 7 + (r >= 12 ? 6 : r >= 6 ? r - 6 : r);
  };
  f32x4 hs[4];
  float cp[4];
  Loop<C3> L;
  L.ts();
  auto bsrc = [&](int k, int c) -> unsigned { return (k * DF_ + c * 8) * 2; };
  L.init0(lds, D_ * 2, bsrc, 0);
  L.ts();
  karg_wait(ka);
  L.ts();
  L.init1(karg_ptr(ka, 0) + (long)rb * 256 * D_ * 2, (L.wid >> 2 ? karg_ptr(ka, 2) : karg_ptr(ka, 1)) + ct * 224, 32L * DF_ * 2);
  const int lane = L.lane, fr = lane & 15, g = lane >> 4, wm = L.wm, wn = L.wn;
  LAS float* sC = (LAS float*)(lds + C3::LDS_BYTES);
  LAS float* sR = (LAS float*)(lds + C3::LDS_BYTES + 1024);
  const char* hsp = karg_ptr(ka, 3);
  unsigned ho = (rb * 256 + L.wid * 32) * 128 + lane * 16, co, to, sra, sca;
  f32x2 pp[4];
  float w8[8];
  {
    const int c = tid < 448 ? tid >> 1 : 0;
    bool up; const int ch = cmap(c >> 3, up);
    co = (S_ * 32 + (up ? DF_ : 0) + ct * 112 + ch * 8 + (c & 7) + (tid & 1) * (4 * 2 * DF_)) * 4;
    to = (((xcd & 1) * 4 + (xcd >> 1)) * 256 + idx * 8 + L.wid) * 3584 + (lane < 56 ? lane : 0) * 64;
    sca = (unsigned)(size_t)(sC + c);
    sra = (unsigned)(size_t)((lane & 4) ? sR + (L.wid * 32 + (lane & 3) * 8 + (lane >> 3)) * 2 : sC + 240);
    asm volatile("" : "+v"(ho), "+v"(co), "+v"(to), "+v"(sra), "+v"(sca));
  }
  L.run<3>([&](auto sx) {
    constexpr int sv = decltype(sx)::value;
    if constexpr (sv == SENTS * 100 + 12 || sv == SENTS * 100 + 26) stsent(bid * 16384 + tid * 16 + (sv == SENTS * 100 + 12 ? 0 : 8192), xbuf);
    if constexpr (sv >= 2600 && sv < 3000) {
      if constexpr (sv == 2602) ld4<0>(hs[0], ho, hsp);
      if constexpr (sv == 2609) ld4<1024>(hs[1], ho, hsp);
      if constexpr (sv == 2616) ld4<2048>(hs[2], ho, hsp);
      if constexpr (sv == 2623) ld4<3072>(hs[3], ho, hsp);
      if constexpr (sv == 2703) ld1(cp[0], co, hsp);
      if constexpr (sv == 2708) ld1(cp[1], co, hsp + 1L * (2 * DF_ * 4));
      if constexpr (sv == 2717) ld1(cp[2], co, hsp + 2L * (2 * DF_ * 4));
      if constexpr (sv == 2722) ld1(cp[3], co, hsp + 3L * (2 * DF_ * 4));
      if constexpr (sv == 2800) asm volatile("s_waitcnt vmcnt(6)" : "+v"(hs[0]), "+v"(hs[1]), "+v"(hs[2]), "+v"(hs[3]) :: "memory");
      if constexpr (sv >= 2800 && sv < 2804) {
        constexpr int i = sv - 2800;
        pp[i] = (f32x2){hs[i][0], hs[i][1]} + (f32x2){hs[i][2], hs[i][3]};
        asm volatile("" : "+v"(pp[i]));
      }
      if constexpr (sv >= 2804 && sv < 2812) {
        constexpr int i = (sv - 2804) >> 2, e = (sv - 2804) & 1, gv = ((sv - 2804) >> 1) & 1;
        w8[sv - 2804] = fsel(lane & 1, pp[2 * i + 1 - gv][e], pp[2 * i + gv][e]);
        asm volatile("" : "+v"(w8[sv - 2804]));
      }
      if constexpr (sv >= 2812 && sv < 2816) {
        constexpr int i = (sv - 2812) >> 1, e = (sv - 2812) & 1;
        pp[i][e] = w8[4 * i + e] + dpp<0xB1>(w8[4 * i + 2 + e]);
        asm volatile("" : "+v"(pp[i]));
      }
      if constexpr (sv >= 2816 && sv < 2820) {
        constexpr int e = (sv - 2816) & 1, gv = (sv - 2816) >> 1;
        w8[sv - 2816] = fsel(lane & 2, pp[1 - gv][e], pp[gv][e]);
        asm volatile("" : "+v"(w8[sv - 2816]));
      }
      if constexpr (sv == 2820 || sv == 2821) { w8[sv - 2816] = w8[sv - 2820] + dpp<0x4E>(w8[sv - 2818]); asm volatile("" : "+v"(w8[sv - 2816])); }
      if constexpr (sv == 2823 || sv == 2824) {
        w8[sv - 2817] = (w8[sv - 2819] + dpp<0x124>(w8[sv - 2819])) * (1.f / D_); asm volatile("" : "+v"(w8[sv - 2817]));
      }
      if constexpr (sv == 2826) { w8[7] = fmaxf(w8[7] - w8[6] * w8[6], 0.f); asm volatile("" : "+v"(w8[7])); }
      if constexpr (sv == 2900) *(LAS f32x2*)(size_t)sra = (f32x2){-w8[6], w8[7] + 1e-5f};
      if constexpr (sv == 2902) {
        asm volatile("s_waitcnt vmcnt(0)" : "+v"(cp[0]), "+v"(cp[1]), "+v"(cp[2]), "+v"(cp[3]) :: "memory");
        cp[0] += cp[1]; asm volatile("" : "+v"(cp[0]));
      }
      if constexpr (sv == 2903) { cp[2] += cp[3]; asm volatile("" : "+v"(cp[2])); }
      if constexpr (sv == 2904) { cp[0] += cp[2]; asm volatile("" : "+v"(cp[0])); }
      if constexpr (sv == 2906) { cp[0] += dpp<0xB1>(cp[0]); asm volatile("" : "+v"(cp[0])); }
      if constexpr (sv == 2908) *(LAS float*)(size_t)sca = cp[0];
    } else if constexpr (sv == 3000) {
      touchv(to, wd);
    }
  });
  static_assert(C3::NMAIN == 26 && C3::XS == 28, "K3 schedule");
  {
    float nm[4];
#pragma unroll
    for (int m = 0; m < 4; ++m) nm[m] = sR[(wm * 64 + m * 16 + fr) * 2];
#pragma unroll
    for (int j = 0; j < 7; ++j) {
      const f32x4 c4 = *(const LAS f32x4*)(sC + (wn * 14 + 2 * j) * 8 + 4 * g);
#pragma unroll
      for (int m = 0; m < 4; ++m) L.acc[m][j] += c4 * nm[m];
    }
  }
  const int hi = lane >> 5, g1 = g & 1;
  LAS char* tb = lds + C3::ATILE + L.wid * 7168;
  const long apo = ((long)rb * 256 + wm * 64) * DF_ + ct * 112 + wn * 56;
  constexpr float K1c = -2.f * 0.7978845608028654f * 1.4426950408889634f, K2c = K1c * 0.044715f;
  auto glu = [&](f32x4 a, f32x4 b, float q, float r1, float r3) -> u32x2 {
    const f32x4 z = a * ((a * a) * r3 + r1);
    f32x4 e;
#pragma unroll
    for (int r = 0; r < 4; ++r) e[r] = __builtin_amdgcn_exp2f(z[r]);
    e = e * q + q;
#pragma unroll
    for (int r = 0; r < 4; ++r) e[r] = __builtin_amdgcn_rcpf(e[r]);
    const f32x4 o = (a * b) * e;
    u32x2 w; w.x = pk2(o[0], o[1]); w.y = pk2(o[2], o[3]);
    return w;
  };
#pragma unroll
  for (int mp = 0; mp < 2; ++mp) {
#pragma unroll
    for (int mm = 0; mm < 2; ++mm) {
      const int m = 2 * mp + mm;
      const float q = sR[(wm * 64 + m * 16 + fr) * 2 + 1];
      const float rstd = __builtin_amdgcn_rsqf(q), r1 = K1c * rstd, r3 = K2c * rstd * rstd * rstd;
#pragma unroll
      for (int j = 0; j < 3; ++j)
        *(LAS u32x2*)(tb + (m * 16 + fr) * 112 + j * 32 + g * 8) = glu(L.acc[m][j], L.acc[m][3 + j], q, r1, r3);
    }
    {
      const float q = sR[(wm * 64 + (2 * mp + hi) * 16 + fr) * 2 + 1];
      const float rstd = __builtin_amdgcn_rsqf(q), r1 = K1c * rstd, r3 = K2c * rstd * rstd * rstd;
      f32x4 G, U;
#pragma unroll
      for (int r = 0; r < 4; ++r) {
        auto sw = __builtin_amdgcn_permlane32_swap(__float_as_uint(L.acc[2 * mp][6][r]), __float_as_uint(L.acc[2 * mp + 1][6][r]), false, false);
        G[r] = __uint_as_float(sw[0]); U[r] = __uint_as_float(sw[1]);
      }
      *(LAS u32x2*)(tb + ((2 * mp + hi) * 16 + fr) * 112 + 96 + g1 * 8) = glu(G, U, q, r1, r3);
    }
#pragma unroll
    for (int i = mp * 3; i < (mp ? 7 : 3); ++i) {
      const int q = i * 64 + lane, row = q / 7, c = q - row * 7;
      const u32x4 v = *(const LAS u32x4*)(tb + q * 16);
      st16<STAUX>(act, (apo + (long)row * DF_ + c * 8) * 2, v);
    }
  }
  wait_vm<0>();
  L.ts();
  STAMP(3, 3);
  L.tsdump<3>();
}

using C4 = Cfg<128, 16, 2, 4, 4, 2, 4, 4, 64, 56, 8, 3, 0x3u>;
__global__ __launch_bounds__(C4::NTHR, 2) void k4_down(const u16* __restrict__ act, const u16* __restrict__ wd, const u16* __restrict__ h,
                                                       u16* __restrict__ out, unsigned* __restrict__ xbuf) {
  extern __shared__ __attribute__((aligned(16))) char smem[];
  LAS char* lds = (LAS char*)smem;
  const int bid = blockIdx.x, xcd = bid & 7, idx = bid >> 3;
  const int kh = xcd & 1, rb = (xcd >> 1) * 4 + (idx & 3), cb = idx >> 2;
  STAMP(4, 0);
  u32x4 ka = karg4();
  sched_fence();
  Loop<C4> L;
  auto bsrc = [&](int k, int c) -> unsigned { return (k * D_ + c * 8) * 2; };
  L.init0(lds, DF_ * 2, bsrc, 2 * kh);
  karg_wait(ka);
  L.init1(karg_ptr(ka, 0) + ((long)rb * 128 * DF_ + kh * 1792) * 2, karg_ptr(ka, 1) + ((long)kh * 1792 * D_ + cb * 128) * 2, 64L * D_ * 2);
  const int lane = L.lane, fr = lane & 15, g = lane >> 4, wm = L.wm, wn = L.wn;
  const long off0 = ((long)rb * 128 + wm * 64 + kh * 32 + fr) * D_ + cb * 128 + wn * 32 + (g & 1) * 16 + (g >> 1) * 8;
  u32x4 xr[2];
  L.run<4>([&](auto) {
#pragma unroll
    for (int m = 0; m < 2; ++m) xr[m] = *(const u32x4*)(h + off0 + m * 16 * D_);
  });
  const __amdgpu_buffer_rsrc_t rx = __builtin_amdgcn_make_buffer_rsrc((void*)xbuf, 0, XBUF_BYTES, 0x00020000);
  const int xo = L.wid * 2048 + lane * 16;
#pragma unroll
  for (int i = 0; i < 2; ++i) {
    const f32x4 a = L.acc[2 + i][0], b = L.acc[2 + i][1];
    const u32x4 v = {pk2(a[0], a[1]), pk2(a[2], a[3]), pk2(b[0], b[1]), pk2(b[2], b[3])};
    __builtin_amdgcn_raw_buffer_store_b128(v, rx, bid * 16384 + xo + i * 1024, 0, 16);
  }
  u32x4 rv[2];
  for (int spin = 0; spin < (1 << 22); ++spin) {
    asm volatile("" ::: "memory");
    bool ok = true;
#pragma unroll
    for (int i = 0; i < 2; ++i) rv[i] = __builtin_amdgcn_raw_buffer_load_b128(rx, (bid ^ 1) * 16384 + xo + i * 1024, 0, 16);
#pragma unroll
    for (int i = 0; i < 2; ++i) ok = ok && rv[i][0] != SENT && rv[i][1] != SENT && rv[i][2] != SENT && rv[i][3] != SENT;
    if (__all(ok)) break;
  }
#pragma unroll
  for (int m = 0; m < 2; ++m) {
    const u32x4 r = xpose2((u32x2){xr[m][0], xr[m][1]}, (u32x2){xr[m][2], xr[m][3]});
    u32x2 w[2];
#pragma unroll
    for (int j = 0; j < 2; ++j) {
      const f32x4 a = L.acc[m][j];
      w[j].x = pk2(bflo(r[2 * j]) + (a[0] + bflo(rv[m][2 * j])), bfhi(r[2 * j]) + (a[1] + bfhi(rv[m][2 * j])));
      w[j].y = pk2(bflo(r[2 * j + 1]) + (a[2] + bflo(rv[m][2 * j + 1])), bfhi(r[2 * j + 1]) + (a[3] + bfhi(rv[m][2 * j + 1])));
    }
    *(u32x4*)(out + off0 + m * 16 * D_) = xpose2(w[0], w[1]);
  }
  L.ts();
  STAMP(4, 3);
  L.tsdump<4>();
}

extern "C" void kernel_launch(void* const* d_in, const int* in_sizes, int n_in, void* d_out, int out_size, void* d_ws,
                              size_t ws_size, hipStream_t stream) {
  const u16* x = (const u16*)d_in[0];
  const u16* wqkv = (const u16*)d_in[1];
  const u16* wo = (const u16*)d_in[2];
  const u16* wg = (const u16*)d_in[4];
  const u16* wu = (const u16*)d_in[5];
  const u16* wd = (const u16*)d_in[6];
  u16* out = (u16*)d_out;
  char* ws = (char*)d_ws;
  u16* pq = (u16*)ws;                      ws += (size_t)S_ * D_ * 2;
  u16* o = (u16*)ws;                       ws += (size_t)S_ * D_ * 2;
  u16* h = (u16*)ws;                       ws += (size_t)S_ * D_ * 2;
  u16* act = (u16*)ws;                     ws += (size_t)S_ * DF_ * 2;
  float* kvp = (float*)ws;                 ws += (size_t)NH_ * 16 * KVP * 4;
  float* hstat = (float*)ws;               ws += (size_t)S_ * 16 * 2 * 4;
  float* csp = (float*)ws;                 ws += (size_t)8 * 2 * DF_ * 4;
  unsigned* xbuf = (unsigned*)ws;          ws += (size_t)XBUF_BYTES;
  (void)hipFuncSetAttribute((const void*)k1_qkv, hipFuncAttributeMaxDynamicSharedMemorySize, C1::LDS_BYTES);
  (void)hipFuncSetAttribute((const void*)k2_wo, hipFuncAttributeMaxDynamicSharedMemorySize, C2::LDS_BYTES);
  (void)hipFuncSetAttribute((const void*)k3_glu, hipFuncAttributeMaxDynamicSharedMemorySize, K3_LDS);
  (void)hipFuncSetAttribute((const void*)k4_down, hipFuncAttributeMaxDynamicSharedMemorySize, C4::LDS_BYTES);
  k1_qkv<<<256, C1::NTHR, C1::LDS_BYTES, stream>>>(x, wqkv, pq, kvp, wo, wg);
  k15_o<<<256, 512, 0, stream>>>(pq, kvp, o, wg, wu, csp);
  k2_wo<<<256, C2::NTHR, C2::LDS_BYTES, stream>>>(o, wo, x, h, hstat);
  k3_glu<<<256, C3::NTHR, K3_LDS, stream>>>(h, wg, wu, hstat, act, wd, xbuf);
  k4_down<<<256, C4::NTHR, C4::LDS_BYTES, stream>>>(act, wd, h, out, xbuf);
}
```

```cpp
#include <hip/hip_runtime.h>
#include <hip/hip_bf16.h>
#include <stdint.h>

typedef unsigned short u16;
typedef short bf16x8 __attribute__((ext_vector_type(8)));
typedef short s16x4 __attribute__((ext_vector_type(4)));
typedef float f32x4 __attribute__((ext_vector_type(4)));
typedef float f32x2 __attribute__((ext_vector_type(2)));
typedef unsigned u32x4 __attribute__((ext_vector_type(4)));
typedef unsigned u32x2 __attribute__((ext_vector_type(2)));
typedef __bf16 bf16x2_t __attribute__((ext_vector_type(2)));
#define LAS __attribute__((address_space(3)))
#define DEV __device__ __forceinline__
template <int V> struct IC { static constexpr int value = V; };

constexpr int S_ = 2048, D_ = 1024, DF_ = 3584, NH_ = 32;
constexpr int KVP = 1056;
constexpr unsigned SENT = 0xFFFFFFFFu;
constexpr int XBUF_BYTES = 256 * 16384;

DEV float bflo(unsigned v) { return __uint_as_float(v << 16); }
DEV float bfhi(unsigned v) { return __uint_as_float(v & 0xffff0000u); }
DEV unsigned pk2(float lo, float hi) {
  bf16x2_t r; r.x = (__bf16)lo; r.y = (__bf16)hi; return __builtin_bit_cast(unsigned, r);
}
template <int N> DEV void wait_vm() { asm volatile("s_waitcnt vmcnt(%0)" ::"n"(N) : "memory"); }
DEV void wait_lgkm0() { __builtin_amdgcn_s_waitcnt(0xC07F); }
DEV void bar() { __builtin_amdgcn_s_barrier(); }
DEV void lbar() { wait_lgkm0(); bar(); asm volatile("" ::: "memory"); }
DEV void sched_fence() { __builtin_amdgcn_sched_barrier(0); }
DEV void glds_v(const void* g, unsigned lds_addr) {
  asm volatile("s_mov_b32 m0, %1\n\ts_nop 0\n\tglobal_load_lds_dwordx4 %0, off" ::"v"(g), "s"(lds_addr) : "memory");
}
DEV void glds_vm(const void* g, unsigned lds_addr, unsigned long long mask) {
  asm volatile("s_mov_b32 m0, %1\n\ts_mov_b64 exec, %2\n\tglobal_load_lds_dwordx4 %0, off\n\ts_mov_b64 exec, -1"
               ::"v"(g), "s"(lds_addr), "s"(mask) : "memory");
}
DEV void glds_s(unsigned voff, const void* sbase, unsigned lds_addr) {
  asm volatile("s_mov_b32 m0, %2\n\ts_nop 0\n\tglobal_load_lds_dwordx4 %0, %1" ::"v"(voff), "s"(sbase), "s"(lds_addr) : "memory");
}
DEV void glds_sm(unsigned voff, const void* sbase, unsigned lds_addr, unsigned long long mask) {
  asm volatile("s_mov_b32 m0, %2\n\ts_mov_b64 exec, %3\n\tglobal_load_lds_dwordx4 %0, %1\n\ts_mov_b64 exec, -1"
               ::"v"(voff), "s"(sbase), "s"(lds_addr), "s"(mask) : "memory");
}
DEV void touch(const void* g, unsigned lds_addr) {
  asm volatile("s_mov_b32 m0, %1\n\ts_nop 0\n\tglobal_load_lds_dword %0, off" ::"v"(g), "s"(lds_addr) : "memory");
}
DEV void touchv(unsigned voff, const void* sbase) {
  asm volatile("global_load_dword v255, %0, %1" :: "v"(voff), "s"(sbase) : "memory", "v255");
}
typedef unsigned u32x8 __attribute__((ext_vector_type(8)));
DEV u32x4 karg4() { u32x4 r; asm volatile("s_load_dwordx4 %0, %1, 0x0" : "=s"(r) : "s"(__builtin_amdgcn_kernarg_segment_ptr())); return r; }
DEV u32x8 karg8() { u32x8 r; asm volatile("s_load_dwordx8 %0, %1, 0x0" : "=s"(r) : "s"(__builtin_amdgcn_kernarg_segment_ptr())); return r; }
template <class T> DEV void karg_wait(T& r) { asm volatile("s_waitcnt lgkmcnt(0)" : "+s"(r)); }
template <class T> DEV const char* karg_ptr(const T& r, int i) { return (const char*)(((unsigned long long)r[2 * i + 1] << 32) | r[2 * i]); }
template <int OFF> DEV void ld4(f32x4& d, unsigned voff, const void* sbase) {
  asm volatile("global_load_dwordx4 %0, %1, %2 offset:%3" : "=v"(d) : "v"(voff), "s"(sbase), "n"(OFF) : "memory");
}
DEV void ld1(float& d, unsigned voff, const void* sbase) {
  asm volatile("global_load_dword %0, %1, %2" : "=v"(d) : "v"(voff), "s"(sbase) : "memory");
}
template <int CTRL> DEV float dpp(float v) { return __int_as_float(__builtin_amdgcn_update_dpp(0, __float_as_int(v), CTRL, 0xF, 0xF, true)); }
#define SENTSC ""
DEV void stsent(unsigned voff, const void* sbase) {
  const u32x4 sv = {SENT, SENT, SENT, SENT};
  asm volatile("global_store_dwordx4 %0, %1, %2 " SENTSC "\n\ts_nop 1" :: "v"(voff), "v"(sv), "s"(sbase) : "memory");
}
DEV float fsel(bool c, float a, float b) { return c ? a : b; }
DEV f32x4 mfma16(bf16x8 a, bf16x8 b, f32x4 c) { return __builtin_amdgcn_mfma_f32_16x16x32_bf16(a, b, c, 0, 0, 0); }
DEV void mfma_acc(f32x4& c, bf16x8 a, bf16x8 b) {
  asm volatile("v_mfma_f32_16x16x32_bf16 %0, %1, %2, %0" : "+v"(c) : "v"(a), "v"(b));
}
DEV void mfma_z(f32x4& c, bf16x8 a, bf16x8 b) {
  asm volatile("v_mfma_f32_16x16x32_bf16 %0, %1, %2, 0" : "=&v"(c) : "v"(a), "v"(b));
}
template <bool Z> DEV void mfma_a(f32x4& c, bf16x8 a, bf16x8 b) { if constexpr (Z) mfma_z(c, a, b); else mfma_acc(c, a, b); }
DEV bf16x8 cat(s16x4 a, s16x4 b) { return __builtin_shufflevector(a, b, 0, 1, 2, 3, 4, 5, 6, 7); }
DEV s16x4 rdtr(unsigned a) { return __builtin_amdgcn_ds_read_tr16_b64_v4i16((LAS s16x4*)(size_t)a); }

#define FINEW 0
#define STAMP(kid, i)
#define SENTS 1
template <int BM_, int NC_, int WM_, int WN_, int MT_, int NT_, int RA_, int RB_, int KB_, int NS_, int P_, int KIND_, unsigned TRANS_>
struct Cfg {
  static constexpr int BM = BM_, NC = NC_, WM = WM_, WN = WN_, MT = MT_, NT = NT_, RA = RA_, RB = RB_, KB = KB_, NS = NS_, P = P_;
  static constexpr int KIND = KIND_;
  static constexpr unsigned TRANS = TRANS_;
  static constexpr bool STATS = KIND == 1, PERM = KIND == 1 || KIND == 3;
  static constexpr bool AFRAG = KIND == 3;
  static constexpr int NW = WM * WN, NTHR = NW * 64;
  static constexpr int ATILE = BM * 128, BSLOT = KIND_ == 2 ? 15360 : KB * NC * 16;
  static constexpr int BHF = KIND_ == 2 ? 4 * 224 : 4 * NC * 16;
  static constexpr int NA = BM * 8 / NTHR;
  static constexpr int CPW = KB * NC / NW;
  static constexpr int NB = (CPW + 63) / 64;
  static constexpr int NA1 = KB == 64 ? NA / 2 : (NA + 1) / 2, NB1 = KB == 64 ? (NB + 1) / 2 : NB;
  static constexpr int LDS_B0 = RA * ATILE, LDS_BYTES = RA * ATILE + RB * BSLOT;
  static constexpr int NTA = NS / 2;
#define RQN 7
#define RQD 8
  static constexpr int RQ1 = (MT * NT * RQN) / RQD;
  static_assert(BM * 8 % NTHR == 0, "A tile split");
  static_assert((KB * NC) % NW == 0, "B slot split");
  static_assert(P % 2 == 0 && (P / 2) % RA == 0 && (KB == 32 ? P % RB == 0 : (P / 2) % RB == 0), "period");
  static_assert(BM == WM * MT * 16, "M tiling");
  static_assert(NC * 8 == WN * NT * 16, "N tiling");
  DEV static constexpr int bswz(int k) {
    return NC == 28 ? 2 * ((k >> 3) & 1)
         : (NC % 16 == 8) ? 2 * (((k >> 1) & 1) | (((k >> 3) & 1) << 1))
                          : 2 * ((k & 3) | (((k >> 3) & 1) << 2));
  }
  static constexpr bool hasB(int t) {
    if (KB == 32) return t + RB >= 0 && t + RB < NS;
    int tile = (t & 1) ? ((t - 1) >> 1) + RB : ((t - 2) >> 1) + RB;
    return tile >= 0 && tile < NTA;
  }
  static constexpr bool hasA(int t) {
    int tile = (t & 1) ? ((t - 1) >> 1) + RA : ((t - 2) >> 1) + RA;
    return tile >= 0 && tile < NTA;
  }
  static constexpr int nB(int t) { return !hasB(t) ? 0 : KB == 32 ? NB : (t & 1) ? NB1 : NB - NB1; }
  static constexpr int nA(int t) { return !hasA(t) ? 0 : (t & 1) ? NA1 : NA - NA1; }
  static constexpr int T0 = -(2 * (RA > RB ? RA : RB) + 2);
  static constexpr int lastIssue() { int l = 0; for (int t = 0; t < NS; ++t) if (nA(t) + nB(t) > 0) l = t; return l; }
  static constexpr int XS = lastIssue() + 1;
  static constexpr int nX(int t) {
    if (KIND == 0) return t == XS ? 4 : 0;
    if (KIND == 1) return t == XS ? 2 : 0;
    if (KIND == 2) return (t == 30 ? 1 : 0) + ((t == 26 || t == 27) ? 4 : 0) + (t == SENTS ? 2 : 0);
    return t == XS ? 2 : 0;
  }
  static constexpr int hk(int t) { return KIND == 2 && ((t >= 26 && t <= 30) || t == SENTS) ? 2 : nX(t) > 0 ? 1 : 0; }
  static constexpr int nXP(int t) { return KIND == 1 && t == 0 ? 1 : 0; }
  static constexpr int TPRE = KB == 32 ? -3 : -4, RATE = KB == 32 ? 2 : 4;
  static constexpr int vlast(int s) { int v = TPRE + RATE * (s + 1); return s < 0 ? TPRE : (v < s ? v : s); }
  static constexpr int nhead() { int s = 0; while (vlast(s - 1) != s - 1) ++s; return s; }
  static constexpr int H = nhead();
  static constexpr int TF = KB == 32 ? (-RB > -2 * RA + 2 ? -RB : -2 * RA + 2) : -2 * (RA < RB ? RA : RB) + 2;
  static constexpr int nfirst() { int n = 0; for (int t = T0; t <= TF; ++t) n += nA(t) + nB(t); return n; }
  static constexpr int npro() { int n = 0; for (int t = T0; t <= TPRE; ++t) n += nA(t) + nB(t); return n; }
  static constexpr int nmain() {
    int lim = 0;
    while (lim < NS && hasA(lim) && hasB(lim)) ++lim;
    return H + ((lim - H) / P) * P;
  }
  static constexpr int vm_at(int s) {
    int posA[64] = {}, posB[128] = {}, cntA[64] = {}, cntB[128] = {};
    int tot = 0;
    for (int r = -1; r < s || r < 0; ++r) {
      if (r >= 0) tot += nX(r);
      for (int t = (r < 0 ? T0 : vlast(r - 1) + 1); t <= vlast(r); ++t) {
        const int tl = (t & 1) ? ((t - 1) >> 1) : ((t - 2) >> 1);
        if (nB(t)) { tot += nB(t); const int i = KB == 32 ? t + RB : tl + RB; posB[i] = tot; cntB[i] += nB(t); }
        if (nA(t)) { tot += nA(t); posA[tl + RA] = tot; cntA[tl + RA] += nA(t); }
      }
      if (r >= 0) tot += nXP(r);
    }
    int nb = s + 1;
    if (nb >= NS) return 63;
    const int ib = KB == 32 ? nb : nb / 2;
    if (cntB[ib] != NB || cntA[nb / 2] != NA) return -1;
    int need = posB[ib] > posA[nb / 2] ? posB[ib] : posA[nb / 2];
    int v = tot - need;
    return v > 63 ? 63 : v;
  }
  static constexpr int NMAIN = nmain();
  static constexpr int vm_main(int u) {
    int v = 63;
    for (int s = H + u; s < NMAIN; s += P) { int x = vm_at(s); v = x < v ? x : v; }
    return v;
  }
};

template <class C> struct Loop {
  unsigned lbase;
  const char* abase;
  unsigned ga[C::NA];
  unsigned gbo[C::NB];
  const char* bbase;
  unsigned bdst;
  long bstep;
  int wid, lane, wm, wn;
  unsigned ra[2][C::PERM ? C::MT : 1];
  unsigned rb[C::NT];
  bf16x8 FA[2][C::MT];
  s16x4 FBh[2][C::NT][2];
  f32x4 acc[C::MT][C::NT];
  f32x4 rs, rq;
  f32x4 cs[3];
  bf16x8 ones;

  template <int SET, int I> DEV void readOp(int aslot, int ks, int boff) {
    if constexpr (I < C::MT) {
      unsigned a = C::PERM ? ra[ks][C::PERM ? I : 0] : ra[ks][0] + I * 2048;
      FA[SET][I] = *(const LAS bf16x8*)(size_t)(a + aslot * C::ATILE);
    } else {
      constexpr int j = (I - C::MT) / 2, hf = (I - C::MT) & 1;
      FBh[SET][j][hf] = rdtr(rb[j] + boff + hf * C::BHF);
    }
  }
  static constexpr int rord(int i) {
    if (i == 0) return 0;
    if (i <= 2 * C::NT) return C::MT + i - 1;
    return i - 2 * C::NT;
  }
  template <int SET> DEV bf16x8 FB(int j) const { return cat(FBh[SET][j][0], FBh[SET][j][1]); }
  static constexpr int NMM = C::MT * C::NT, NR = C::MT + 2 * C::NT;
  static constexpr int nm(int U, int CP) { return NMM + (C::STATS ? ((U & 1) == CP ? 5 : 2) : 0); }
  template <int SET, int Q, bool Z> DEV void mmaOp() {
    if constexpr (Q < NMM) {
      constexpr int m = Q / C::NT, j = Q % C::NT;
      if constexpr ((C::TRANS >> j) & 1) mfma_a<Z>(acc[m][j], FB<SET>(j), FA[SET][m]);
      else mfma_a<Z>(acc[m][j], FA[SET][m], FB<SET>(j));
    } else if constexpr (Q == NMM) {
      mfma_a<Z>(rs, ones, FA[SET][0]);
    } else if constexpr (Q == NMM + 1) {
      mfma_a<Z>(rq, FA[SET][0], FA[SET][0]);
    } else {
      mfma_acc(cs[Q - NMM - 2], ones, FB<SET>(Q - NMM - 2));
    }
  }
  template <int PAR, bool IB, int I> DEV void gldsOp(int bslot, int aslot) {
    constexpr int nb = !IB ? 0 : C::KB == 32 ? C::NB : PAR ? C::NB1 : C::NB - C::NB1;
    if constexpr (I < nb) {
      constexpr int i = (C::KB == 64 && !PAR) ? C::NB1 + I : I;
      constexpr int rem = C::CPW - 64 * i;
      const unsigned dst = bdst + bslot * C::BSLOT + i * 1024;
      if constexpr (rem >= 64) glds_s(gbo[i], bbase, dst);
      else glds_sm(gbo[i], bbase, dst, (1ull << (rem & 63)) - 1ull);
      if constexpr (i == C::NB - 1) { bbase += bstep; asm volatile("" : "+s"(bbase)); }
    } else {
      constexpr int i = (PAR ? 0 : C::NA1) + (I - nb);
      glds_s(ga[i], abase, lbase + aslot * C::ATILE + (wid * C::NA + i) * 1024);
      if constexpr (i == C::NA - 1) { abase += C::AFRAG ? C::ATILE : 128; asm volatile("" : "+s"(abase)); }
    }
  }
  static constexpr int spread(int N, int Q0, int Q1, int Q) { return Q <= Q0 ? 0 : Q >= Q1 ? N : (N * (Q - Q0) + (Q1 - Q0) - 1) / (Q1 - Q0); }
  template <int SET, int LO, int HI> DEV void readRange(int aslot, int ks, int boff) {
    if constexpr (LO < HI) { readOp<SET, rord(LO)>(aslot, ks, boff); readRange<SET, LO + 1, HI>(aslot, ks, boff); }
  }
  static constexpr int wslotA(int U) { return (((U & 1) ? (U - 1) / 2 : (U - 2 + 2 * C::P) / 2)) % C::RA; }
  static constexpr int wslotB(int U) { return C::KB == 32 ? U % C::RB : (((U & 1) ? (U - 1) / 2 : (U - 2 + 2 * C::P) / 2)) % C::RB; }
  static constexpr int rslotA(int U1) { return (U1 / 2) % C::RA; }
  static constexpr int roffB(int U1) { return C::KB == 32 ? (U1 % C::RB) * C::BSLOT : ((U1 / 2) % C::RB) * C::BSLOT + (U1 & 1) * 32 * C::NC * 16; }
  static constexpr int nops(int V, int VHI, bool MAIN) {
    int n = 0;
    for (int v = V; v <= VHI; ++v)
      n += MAIN ? ((C::KB == 32 ? C::NB : (v & 1) ? C::NB1 : C::NB - C::NB1) + ((v & 1) ? C::NA1 : C::NA - C::NA1)) : C::nB(v) + C::nA(v);
    return n;
  }
  template <int V, int VHI, bool MAIN, int I> DEV void vop() {
    if constexpr (V <= VHI) {
      constexpr int n = nops(V, V, MAIN), U = ((V % C::P) + C::P) % C::P;
      if constexpr (I < n) gldsOp<U & 1, MAIN || C::hasB(V), I>(wslotB(U), wslotA(U));
      else vop<V + 1, VHI, MAIN, I - n>();
    }
  }
  template <int V, int VHI, bool MAIN, int LO, int HI> DEV void vopRange() {
    if constexpr (LO < HI) { vop<V, VHI, MAIN, LO>(); vopRange<V, VHI, MAIN, LO + 1, HI>(); }
  }
  template <int CP, int U, int V, int VHI, bool MAIN, bool RN, int Q, int HS, class H> DEV void body(H& hook) {
    constexpr int NM = nm(U, CP);
    if constexpr (Q < NM) {
      constexpr int PAR = U & 1;
      constexpr int NG = nops(V, VHI, MAIN);
      constexpr int RQ = C::RQ1 + (NM - NMM);
      mmaOp<PAR, Q, !MAIN && V == C::TPRE + 1>();
      vopRange<V, VHI, MAIN, spread(NG, 0, NM, Q), spread(NG, 0, NM, Q + 1)>();
      if constexpr (RN) readRange<PAR ^ 1, spread(NR, 0, RQ, Q), spread(NR, 0, RQ, Q + 1)>(rslotA(U + 1), PAR ^ 1, roffB(U + 1));
      if constexpr (HS >= 0) hook(IC<HS + Q>{});
      sched_fence();
      body<CP, U, V, VHI, MAIN, RN, Q + 1, HS>(hook);
    }
  }
  template <int CP, int S, int VM, bool MAIN, class H> DEV void step(H& hook) {
    constexpr int U = S % C::P;
    constexpr bool RN = MAIN || S + 1 < C::NS;
    if constexpr (RN && (C::KB == 32 || (U & 1))) {
      static_assert(VM >= 0, "schedule"); wait_vm<VM>(); bar();
    }
    sched_fence();
    constexpr int HM = MAIN ? 0 : C::hk(S);
    if constexpr (HM == 1) { hook(IC<S>{}); sched_fence(); }
    if constexpr (HM == 3) { if (wid >= C::NW / 2) hook(IC<S>{}); sched_fence(); }
    body<CP, U, C::vlast(S - 1) + 1, C::vlast(S), MAIN, RN, 0, HM == 2 ? S * 100 : -1>(hook);
    if constexpr (HM == 3) { if (wid < C::NW / 2) hook(IC<S>{}); sched_fence(); }
    if constexpr (C::nXP(S) > 0) { hook(IC<MAIN ? 2000 : 1000>{}); sched_fence(); }
    if constexpr (C::KB == 32 || !(U & 1)) wait_lgkm0();
    sched_fence();
  }
  template <int CP, int U, class H> DEV void mainSteps(H& hook) {
    if constexpr (U < C::P) { step<CP, C::H + U, C::vm_main(U), true>(hook); mainSteps<CP, U + 1>(hook); }
  }
  template <int CP, int Sx, int SE, class H> DEV void seqSteps(H& hook) {
    if constexpr (Sx < SE) { step<CP, Sx, C::vm_at(Sx), false>(hook); seqSteps<CP, Sx + 1, SE>(hook); }
  }
  template <int KID, int CP, class H> DEV void allSteps(H& hook) {
    pinAcc();
    sched_fence();
    ts();
    wait_vm<C::vm_at(-1)>();
    ts();
    bar();
    sched_fence();
    ts();
    readRange<0, 0, NR>(0, 0, 0);
    wait_lgkm0();
    sched_fence();
    STAMP(KID, 1);
    ts();
    pinAcc();
    asm volatile("s_nop 7" ::: "memory");
    sched_fence();
    seqSteps<CP, 0, C::H>(hook);
    for (int it = 0; it < (C::NMAIN - C::H) / C::P; ++it) mainSteps<CP, 0>(hook);
    seqSteps<CP, C::NMAIN, C::NS>(hook);
    asm volatile("s_nop 7\n\ts_nop 7\n\ts_nop 7" ::: "memory");
    sched_fence();
  }
  DEV void ts() {
  }
  template <int KID> DEV void tsdump() {
  }
  template <class BF> DEV void init0(LAS char* l, int lda, BF bsrc, int mperm) {
    lbase = (unsigned)(size_t)l;
    const int tid = threadIdx.x;
    wid = __builtin_amdgcn_readfirstlane(tid >> 6); lane = tid & 63;
    wm = wid / C::WN; wn = wid % C::WN;
    if (mperm < 0) mperm = wn;
#pragma unroll
    for (int i = 0; i < C::NA; ++i) {
      int q = (wid * C::NA + i) * 64 + lane, row = q >> 3, p = q & 7, c = p ^ ((row >> 1) & 7);
      ga[i] = C::AFRAG ? q * 16 : row * lda + c * 16;
      asm volatile("" : "+v"(ga[i]));
    }
#pragma unroll
    for (int i = 0; i < C::NB; ++i) {
      if constexpr (C::KIND == 2) {
        int q = i * 64 + lane;
        if (q >= 112) q = 111;
        const int k = q / 14, c = q - k * 14;
        gbo[i] = bsrc((wid & 3) * 8 + k, c);
      } else {
        const int q = wid * C::CPW + i * 64 + lane;
        const int k = q / C::NC, p = q % C::NC, idx = p ^ C::bswz(k);
        gbo[i] = bsrc(k, idx);
      }
      asm volatile("" : "+v"(gbo[i]));
    }
    bdst = lbase + C::LDS_B0 + (C::KIND == 2 ? wid * 1920 + (wid >> 2) * 16 : wid * C::CPW * 16);
    mp = mperm;
    sched_fence();
  }
  int mp;
  DEV void init2() {
    const int mperm = mp;
    const int fr = lane & 15, g = lane >> 4;
#pragma unroll
    for (int ks = 0; ks < 2; ++ks)
#pragma unroll
      for (int m = 0; m < (C::PERM ? C::MT : 1); ++m) {
        ra[ks][m] = C::AFRAG ? lbase + (4 * ks + g) * (C::BM * 16) + ((wm * C::MT + (C::PERM ? (m ^ mperm) : m)) * 16 + fr) * 16
                             : lbase + ((wm * C::MT + (C::PERM ? (m ^ mperm) : m)) * 16 + fr) * 128 + (((4 * ks + g) ^ (fr >> 1)) * 16);
        asm volatile("" : "+v"(ra[ks][m]));
      }
    const int q4 = fr >> 2, p4 = fr & 3, k = 8 * g + q4;
#pragma unroll
    for (int j = 0; j < C::NT; ++j) {
      if constexpr (C::KIND == 2) {
        const int mat = j < 3 ? 0 : j < 6 ? 1 : (p4 >> 1);
        const int ch = wn * 7 + (j < 6 ? 2 * (j % 3) + (p4 >> 1) : 6);
        rb[j] = lbase + C::LDS_B0 + (mat * 4 + g) * 1920 + mat * 16 + q4 * 224 + ch * 16 + 8 * (p4 & 1);
      } else {
        int idx;
        if (C::KIND == 1) idx = j * 8 + wn * 2 + (p4 >> 1);
        else idx = 2 * (wn * C::NT + j) + (p4 >> 1);
        rb[j] = lbase + C::LDS_B0 + k * C::NC * 16 + ((idx ^ C::bswz(k)) * 16) + 8 * (p4 & 1);
      }
      asm volatile("" : "+v"(rb[j]));
    }
    sched_fence();
  }
  DEV void init1(const char* Abase, const char* Bbase, long bstep_) {
    bstep = bstep_;
    abase = Abase; bbase = Bbase;
    vopRange<C::T0, C::TPRE, false, 0, C::nfirst()>();
    sched_fence();
    bar();
    sched_fence();
    vopRange<C::T0, C::TPRE, false, C::nfirst(), C::npro()>();
    sched_fence();
    init2();
    if constexpr (C::STATS) {
      u32x4 o4 = {0x3F803F80u, 0x3F803F80u, 0x3F803F80u, 0x3F803F80u};
      asm volatile("" : "+v"(o4));
      ones = __builtin_bit_cast(bf16x8, o4);
    }
#pragma unroll
    for (int j = 0; j < 3; ++j) cs[j] = (f32x4){0.f, 0.f, 0.f, 0.f};
  }
  DEV void pinAcc() {
    if constexpr (C::STATS) asm volatile("" : "+v"(cs[0]), "+v"(cs[1]), "+v"(cs[2]));
  }
  template <int KID, class H> DEV void run(H hook) {
    if (C::STATS && wm) allSteps<KID, 1>(hook);
    else allSteps<KID, 0>(hook);
    ts();
    STAMP(KID, 2);
  }
};

DEV u32x4 xpose2(u32x2 a, u32x2 b) {
  auto x = __builtin_amdgcn_permlane16_swap(a.x, b.x, false, false);
  auto y = __builtin_amdgcn_permlane16_swap(a.y, b.y, false, false);
  return (u32x4){x[0], y[0], x[1], y[1]};
}
#define STAUX 0
#define SC1M 29
#define AUXK(k) (((SC1M >> (k)) & 1) ? 16 : 0)
template <int AUX> DEV void st16(void* base, long byte_off, u32x4 v) {
  if constexpr (AUX == 0) *(u32x4*)((char*)base + byte_off) = v;
  else {
    const __amdgpu_buffer_rsrc_t r = __builtin_amdgcn_make_buffer_rsrc(base, 0, 0x7fffffff, 0x00020000);
    __builtin_amdgcn_raw_buffer_store_b128(v, r, (int)byte_off, 0, AUX);
  }
}
DEV u32x2 phi4(f32x4 u) {
  const f32x4 a = u * 1.4426950408889634f;
  f32x4 e, m;
#pragma unroll
  for (int r = 0; r < 4; ++r) { e[r] = __builtin_amdgcn_fmed3f(__builtin_amdgcn_exp2f(a[r]), 0.f, 1.f); m[r] = __builtin_fmaxf(u[r], 1e-6f); }
  const f32x4 p = m + e;
  u32x2 w; w.x = pk2(p[0], p[1]); w.y = pk2(p[2], p[3]);
  return w;
}

using C1 = Cfg<128, 24, 2, 4, 4, 3, 4, 4, 64, 32, 8, 1, 0x1u>;
static_assert(C1::XS == 25 && C1::LDS_BYTES == 163840 && C1::H == 1 && C1::NMAIN == 25 && C1::P == 8, "K1 schedule");
__global__ __launch_bounds__(C1::NTHR, 2) void k1_qkv(const u16* __restrict__ x, const u16* __restrict__ wqkv,
                                                      u16* __restrict__ pqT, float* __restrict__ kvp,
                                                      const u16* __restrict__ wo, const u16* __restrict__ wg) {
  extern __shared__ __attribute__((aligned(16))) char smem[];
  LAS char* lds = (LAS char*)smem;
  const int bid = blockIdx.x, xcd = bid & 7, idx = bid >> 3;
  const int rb = (xcd & 1) * 8 + (idx & 7), hp = (xcd >> 1) * 4 + (idx >> 3);
  STAMP(0, 0);
  u32x4 ka = karg4();
  sched_fence();
  Loop<C1> L;
  auto bsrc = [&](int k, int c) -> unsigned { return (k * (3 * D_) + (c >> 3) * D_ + (c & 7) * 8) * 2; };
  L.init0(lds, D_ * 2, bsrc, -1);
  karg_wait(ka);
  L.init1(karg_ptr(ka, 0) + (long)rb * 128 * D_ * 2, karg_ptr(ka, 1) + hp * 128, 64L * 3 * D_ * 2);
  const int lane = L.lane, fr = lane & 15, g = lane >> 4, wm = L.wm, wn = L.wn, wid = L.wid;
  const char* tp;
  {
    const int f = (idx * 8 + wid) * 60 + (lane < 60 ? lane : 0);
    const bool isx = f < 6144;
    const int f2 = isx ? f : f - 6144, row = f2 / 24, c = f2 - row * 24;
    const unsigned to = isx ? ((xcd & 1) * 1024 + (xcd >> 1) * 256 + row) * 2048 + 512 + c * 64
                            : (256 + 2 * row + (xcd & 1)) * 6144 + (c >> 3) * 2048 + (xcd >> 1) * 512 + (c & 7) * 64;
    tp = (isx ? karg_ptr(ka, 0) : karg_ptr(ka, 1)) + to;
  }
  L.run<0>([&](auto sx) {
    if constexpr (decltype(sx)::value == 1000) {
      asm volatile("global_load_dword v255, %0, off" :: "v"(tp) : "memory", "v255");
    } else {
      const unsigned o1 = (idx * 32 + wid * 4 + (lane >> 4)) * 2048 + (xcd >> 2) * 1024 + (lane & 15) * 64;
      touchv(o1, wo);
      const unsigned o2 = ((bid * 8 + wid) * 56 + (lane < 56 ? lane : 0)) * 64;
      touchv(o2, wg);
    }
  });
  LAS char* ep = lds;
  LAS float* sR = (LAS float*)ep;
  LAS float* sC = (LAS float*)(ep + 1024);
  LAS char* sF = ep + 4096;
  {
    const float a = L.rs[0];
    const float b = (fr & 2) ? ((fr & 1) ? L.rq[3] : L.rq[2]) : ((fr & 1) ? L.rq[1] : L.rq[0]);
    if (g == (fr >> 2)) {
      float mean = a * (1.f / D_);
      float var = fmaxf(b * (1.f / D_) - mean * mean, 0.f);
      float rstd = rsqrtf(var + 1e-5f);
      *(LAS f32x2*)(sR + (wm * 64 + wn * 16 + fr) * 2) = (f32x2){rstd, -mean * rstd};
#pragma unroll
      for (int j = 0; j < 3; ++j) sC[wm * 192 + j * 64 + wn * 16 + fr] = L.cs[j][0];
    }
  }
  lbar();
  {
    const int ik = 64 + wn * 16 + fr;
    const float ck = sC[ik] + sC[192 + ik], cv = sC[64 + ik] + sC[256 + ik];
#pragma unroll
    for (int m = 0; m < 4; ++m) {
      const int am = m ^ wn, r0 = wm * 64 + am * 16 + 4 * g;
      const f32x4 u0 = *(const LAS f32x4*)(sR + r0 * 2), u1 = *(const LAS f32x4*)(sR + r0 * 2 + 4);
      const f32x4 sa = {u0[0], u0[2], u1[0], u1[2]}, sb = {u0[1], u0[3], u1[1], u1[3]};
      const f32x4 k = L.acc[m][1] * sa + (sb * ck + 1e-6f);
      const f32x4 v = L.acc[m][2] * sa + sb * cv;
      u32x2 a = phi4(k), b;
      b.x = pk2(v[0], v[1]); b.y = pk2(v[2], v[3]);
      LAS char* f = sF + (wn * 4 + wm * 2 + (am >> 1)) * 1024 + lane * 16 + (am & 1) * 8;
      *(LAS u32x2*)f = a;
      *(LAS u32x2*)(f + 16384) = b;
    }
  }
  {
    const int ic = wn * 16 + 4 * g;
    const f32x4 c4 = *(const LAS f32x4*)(sC + ic) + *(const LAS f32x4*)(sC + 192 + ic);
    u32x2 w[4];
#pragma unroll
    for (int m = 0; m < 4; ++m) {
      const f32x2 st = *(const LAS f32x2*)(sR + (wm * 64 + (m ^ wn) * 16 + fr) * 2);
      w[m] = phi4(L.acc[m][0] * st.x + (c4 * st.y + 1e-6f));
    }
    const long pb = ((long)(hp * 4 + wn) * S_ + rb * 128 + wm * 64 + fr) * 16 + (g >> 1) * 8;
#pragma unroll
    for (int m = 0; m < 4; m += 2) st16<AUXK(0)>(pqT, (pb + (((m + (g & 1)) ^ wn) * 16) * 16) * 2, xpose2(w[m], w[m + 1]));
  }
  lbar();
  {
    f32x4 d = {0.f, 0.f, 0.f, 0.f}, z = d;
#pragma unroll
    for (int G = 0; G < 4; ++G) {
      const bf16x8 fk = *(const LAS bf16x8*)(sF + (wn * 4 + G) * 1024 + lane * 16);
      const bf16x8 fv = *(const LAS bf16x8*)(sF + 16384 + (((wn & 2) + wm) * 4 + G) * 1024 + lane * 16);
      d = mfma16(fk, fv, d);
      z = mfma16(fk, L.ones, z);
    }
    const long kp = ((long)(hp * 2 + (wn >> 1)) * 16 + rb) * KVP;
    st16<AUXK(0)>(kvp, (kp + ((wn & 1) * 2 + wm) * 256 + lane * 4) * 4, __builtin_bit_cast(u32x4, d));
    if (wm == 0 && fr == 0) st16<AUXK(0)>(kvp, (kp + 1024 + (wn & 1) * 16 + 4 * g) * 4, __builtin_bit_cast(u32x4, z));
  }
  wait_vm<0>();
  L.ts();
  STAMP(0, 3);
  L.tsdump<0>();
}

__global__ __launch_bounds__(512) void k15_o(const u16* __restrict__ pqT, const float* __restrict__ kvp, u16* __restrict__ o,
                                             const u16* __restrict__ wg, const u16* __restrict__ wu, float* __restrict__ csp) {
  __shared__ __attribute__((aligned(16))) u16 kvT[33 * 40];
  __shared__ __attribute__((aligned(16))) float sCS[16 * 228];
  const int bid = blockIdx.x, head = bid & 31, rb = bid >> 5;
  const int tid = threadIdx.x, lane = tid & 63, wid = __builtin_amdgcn_readfirstlane(tid >> 6), fr = lane & 15, g = lane >> 4;
  STAMP(1, 0);
  f32x4 z0 = {0.f, 0.f, 0.f, 0.f}, z1 = z0;
  if (wid == 0) {
    const f32x4* p = (const f32x4*)(kvp + ((long)head * 16 + (lane >> 3)) * KVP + 1024) + (lane & 7);
    z0 = p[0]; z1 = p[8 * (KVP / 4)];
  }
  sched_fence();
  f32x4 v[8];
  {
    const f32x4* p = (const f32x4*)(kvp + ((long)head * 16 + (tid & 1) * 8) * KVP) + (tid >> 1);
#pragma unroll
    for (int b = 0; b < 8; ++b) v[b] = p[b * (KVP / 4)];
  }
  bf16x8 fq[2];
#pragma unroll
  for (int m = 0; m < 2; ++m)
    fq[m] = *(const bf16x8*)(pqT + ((long)(head * 2 + (g >> 1)) * S_ + rb * 256 + wid * 32 + m * 16 + fr) * 16 + (g & 1) * 8);
  const int xcd = bid & 7, idx = bid >> 3;
  const int rgp = (idx >> 3) * 2 + (xcd & 1), c8 = idx & 7, cgl = (xcd >> 1) * 4 + (c8 & 3);
  const int ct = tid < 448 ? tid : 447, r0 = ct / 28, cc = ct - r0 * 28;
  sched_fence();
  bar();
  sched_fence();
  u32x4 wv[8];
  {
    const u16* p = (c8 < 4 ? wg : wu) + (long)(rgp * 128 + r0) * DF_ + cgl * 224 + cc * 8;
#pragma unroll
    for (int i = 0; i < 8; ++i) wv[i] = *(const u32x4*)(p + (long)i * 16 * DF_);
  }
  sched_fence();
  {
    f32x4 s = ((v[0] + v[1]) + (v[2] + v[3])) + ((v[4] + v[5]) + (v[6] + v[7]));
#pragma unroll
    for (int r = 0; r < 4; ++r) s[r] += __shfl_xor(s[r], 1);
    const int it = tid >> 1, blk = it >> 6, l2 = it & 63;
    const int e = (blk & 1) * 16 + (l2 & 15), d = (blk >> 1) * 16 + (l2 >> 4) * 4 + (tid & 1) * 2;
    *(unsigned*)(kvT + e * 40 + d) = (tid & 1) ? pk2(s[2], s[3]) : pk2(s[0], s[1]);
  }
  if (wid == 0) {
    f32x4 z = z0 + z1;
#pragma unroll
    for (int r = 0; r < 4; ++r) { z[r] += __shfl_xor(z[r], 8); z[r] += __shfl_xor(z[r], 16); z[r] += __shfl_xor(z[r], 32); }
    if (lane < 8) { u32x2 w; w.x = pk2(z[0], z[1]); w.y = pk2(z[2], z[3]); *(u32x2*)(kvT + 32 * 40 + lane * 4) = w; }
  }
  STAMP(1, 1);
  lbar();
  bf16x8 fa[3];
#pragma unroll
  for (int t = 0; t < 2; ++t) fa[t] = *(const bf16x8*)(kvT + (t * 16 + fr) * 40 + g * 8);
  fa[2] = *(const bf16x8*)(kvT + 32 * 40 + g * 8);
  const f32x4 z4 = {0.f, 0.f, 0.f, 0.f};
#pragma unroll
  for (int m = 0; m < 2; ++m) {
    f32x4 den = mfma16(fa[2], fq[m], z4);
    float inv = 1.f / (den[0] + 1e-6f);
    const long row = (long)rb * 256 + wid * 32 + m * 16 + fr;
    u32x2 w[2];
#pragma unroll
    for (int t = 0; t < 2; ++t) {
      f32x4 n = mfma16(fa[t], fq[m], z4);
      w[t].x = pk2(n[0] * inv, n[1] * inv); w[t].y = pk2(n[2] * inv, n[3] * inv);
    }
    st16<AUXK(1)>(o, (row * D_ + head * 32 + (g & 1) * 16 + (g >> 1) * 8) * 2, xpose2(w[0], w[1]));
  }
  sched_fence();
  if (wid < 7) {
    f32x4 lo = {0.f, 0.f, 0.f, 0.f}, hi = lo;
#pragma unroll
    for (int i = 0; i < 8; ++i) {
      lo += (f32x4){bflo(wv[i][0]), bflo(wv[i][1]), bflo(wv[i][2]), bflo(wv[i][3])};
      hi += (f32x4){bfhi(wv[i][0]), bfhi(wv[i][1]), bfhi(wv[i][2]), bfhi(wv[i][3])};
    }
    *(f32x4*)(sCS + r0 * 228 + cc * 8) = (f32x4){lo[0], hi[0], lo[1], hi[1]};
    *(f32x4*)(sCS + r0 * 228 + cc * 8 + 4) = (f32x4){lo[2], hi[2], lo[3], hi[3]};
  }
  STAMP(1, 2);
  lbar();
  if (tid < 448) {
    const int c = tid >> 1, hf = tid & 1;
    float t = 0.f;
#pragma unroll
    for (int i = 0; i < 8; ++i) t += sCS[(hf * 8 + i) * 228 + c];
    t += __shfl_xor(t, 1);
    if (hf == 0) csp[(long)rgp * (2 * DF_) + (c8 < 4 ? 0 : DF_) + cgl * 224 + c] = t;
  }
  STAMP(1, 3);
}

using C2 = Cfg<128, 8, 2, 2, 4, 2, 4, 4, 64, 32, 8, 0, 0x3u>;
__global__ __launch_bounds__(C2::NTHR, 1) void k2_wo(const u16* __restrict__ o, const u16* __restrict__ wo, const u16* __restrict__ x,
                                                     u16* __restrict__ h, float* __restrict__ hstat) {
  extern __shared__ __attribute__((aligned(16))) char smem[];
  LAS char* lds = (LAS char*)smem;
  const int bid = blockIdx.x, xcd = bid & 7, idx = bid >> 3;
  const int rb = (xcd & 3) * 4 + (idx & 3), ct = (xcd >> 2) * 8 + (idx >> 2);
  STAMP(2, 0);
  u32x4 ka = karg4();
  sched_fence();
  Loop<C2> L;
  auto bsrc = [&](int k, int c) -> unsigned { return (k * D_ + c * 8) * 2; };
  L.init0(lds, D_ * 2, bsrc, 0);
  karg_wait(ka);
  L.init1(karg_ptr(ka, 0) + (long)rb * 128 * D_ * 2, karg_ptr(ka, 1) + ct * 128, 64L * D_ * 2);
  const int lane = L.lane, fr = lane & 15, g = lane >> 4, wm = L.wm, wn = L.wn;
  const long off0 = ((long)rb * 128 + wm * 64 + fr) * D_ + ct * 64 + wn * 32 + (g & 1) * 16 + (g >> 1) * 8;
  u32x4 xr[4];
  L.run<2>([&](auto) {
#pragma unroll
    for (int m = 0; m < 4; ++m) xr[m] = *(const u32x4*)(x + off0 + m * 16 * D_);
  });
  LAS float* sS = (LAS float*)lds;
#pragma unroll
  for (int m = 0; m < 4; ++m) {
    const int lr = wm * 64 + m * 16 + fr;
    float s = 0.f, q = 0.f;
    const u32x4 r = xpose2((u32x2){xr[m][0], xr[m][1]}, (u32x2){xr[m][2], xr[m][3]});
    u32x2 w[2];
#pragma unroll
    for (int j = 0; j < 2; ++j) {
      w[j].x = pk2(bflo(r[2 * j]) + L.acc[m][j][0], bfhi(r[2 * j]) + L.acc[m][j][1]);
      w[j].y = pk2(bflo(r[2 * j + 1]) + L.acc[m][j][2], bfhi(r[2 * j + 1]) + L.acc[m][j][3]);
      float f0 = bflo(w[j].x), f1 = bfhi(w[j].x), f2 = bflo(w[j].y), f3 = bfhi(w[j].y);
      s += (f0 + f1) + (f2 + f3);
      q += (f0 * f0 + f1 * f1) + (f2 * f2 + f3 * f3);
    }
    st16<AUXK(2)>(h, (off0 + m * 16 * D_) * 2, xpose2(w[0], w[1]));
    s += __shfl_xor(s, 16); s += __shfl_xor(s, 32);
    q += __shfl_xor(q, 16); q += __shfl_xor(q, 32);
    if (g == 0) *(LAS f32x2*)(sS + (wn * 128 + lr) * 2) = (f32x2){s, q};
  }
  lbar();
  if (threadIdx.x < 128) {
    const int r = threadIdx.x;
    f32x2 v = *(LAS f32x2*)(sS + r * 2) + *(LAS f32x2*)(sS + (128 + r) * 2);
    *(f32x2*)(hstat + (((long)rb * 128 + r) * 16 + ct) * 2) = v;
  }
  L.ts();
  STAMP(2, 3);
  L.tsdump<2>();
}

using C3 = Cfg<256, 28, 4, 2, 4, 7, 3, 4, 32, 32, 12, 2, 0x7Fu>;
constexpr int K3_LDS = C3::LDS_BYTES + 3072;
__global__ __launch_bounds__(C3::NTHR, 2) void k3_glu(const u16* __restrict__ h, const u16* __restrict__ wg, const u16* __restrict__ wu,
                                                      const float* __restrict__ hstat,
                                                      u16* __restrict__ act, const u16* __restrict__ wd, unsigned* __restrict__ xbuf) {
  extern __shared__ __attribute__((aligned(16))) char smem[];
  LAS char* lds = (LAS char*)smem;
  const int bid = blockIdx.x, xcd = bid & 7, idx = bid >> 3;
  const int rb = (xcd & 1) * 4 + (idx & 3), ct = (xcd >> 1) * 8 + (idx >> 2);
  const int tid = threadIdx.x;
  STAMP(3, 0);
  u32x8 ka = karg8();
  sched_fence();
  auto cmap = [](int c, bool& up) -> int {
    const int w = c >= 14, r = c - 14 * w;
    up = r >= 12 ? (r & 1) : r >= 6;
    return w * 7 + (r >= 12 ? 6 : r >= 6 ? r - 6 : r);
  };
  f32x4 hs[4];
  float cp[4];
  Loop<C3> L;
  L.ts();
  auto bsrc = [&](int k, int c) -> unsigned { return (k * DF_ + c * 8) * 2; };
  L.init0(lds, D_ * 2, bsrc, 0);
  L.ts();
  karg_wait(ka);
  L.ts();
  L.init1(karg_ptr(ka, 0) + (long)rb * 256 * D_ * 2, (L.wid >> 2 ? karg_ptr(ka, 2) : karg_ptr(ka, 1)) + ct * 224, 32L * DF_ * 2);
  const int lane = L.lane, fr = lane & 15, g = lane >> 4, wm = L.wm, wn = L.wn;
  LAS float* sC = (LAS float*)(lds + C3::LDS_BYTES);
  LAS float* sR = (LAS float*)(lds + C3::LDS_BYTES + 1024);
  const char* hsp = karg_ptr(ka, 3);
  unsigned ho = (rb * 256 + L.wid * 32) * 128 + lane * 16, co, to, sra, sca;
  f32x2 pp[4];
  float w8[8];
  {
    const int c = tid < 448 ? tid >> 1 : 0;
    bool up; const int ch = cmap(c >> 3, up);
    co = (S_ * 32 + (up ? DF_ : 0) + ct * 112 + ch * 8 + (c & 7) + (tid & 1) * (4 * 2 * DF_)) * 4;
    to = (((xcd & 1) * 4 + (xcd >> 1)) * 256 + idx * 8 + L.wid) * 3584 + (lane < 56 ? lane : 0) * 64;
    sca = (unsigned)(size_t)(sC + c);
    sra = (unsigned)(size_t)((lane & 4) ? sR + (L.wid * 32 + (lane & 3) * 8 + (lane >> 3)) * 2 : sC + 240);
    asm volatile("" : "+v"(ho), "+v"(co), "+v"(to), "+v"(sra), "+v"(sca));
  }
  L.run<3>([&](auto sx) {
    constexpr int sv = decltype(sx)::value;
    if constexpr (sv == SENTS * 100 + 12 || sv == SENTS * 100 + 26) stsent(bid * 16384 + tid * 16 + (sv == SENTS * 100 + 12 ? 0 : 8192), xbuf);
    if constexpr (sv >= 2600 && sv < 3000) {
      if constexpr (sv == 2602) ld4<0>(hs[0], ho, hsp);
      if constexpr (sv == 2609) ld4<1024>(hs[1], ho, hsp);
      if constexpr (sv == 2616) ld4<2048>(hs[2], ho, hsp);
      if constexpr (sv == 2623) ld4<3072>(hs[3], ho, hsp);
      if constexpr (sv == 2703) ld1(cp[0], co, hsp);
      if constexpr (sv == 2708) ld1(cp[1], co, hsp + 1L * (2 * DF_ * 4));
      if constexpr (sv == 2717) ld1(cp[2], co, hsp + 2L * (2 * DF_ * 4));
      if constexpr (sv == 2722) ld1(cp[3], co, hsp + 3L * (2 * DF_ * 4));
      if constexpr (sv == 2800) asm volatile("s_waitcnt vmcnt(6)" : "+v"(hs[0]), "+v"(hs[1]), "+v"(hs[2]), "+v"(hs[3]) :: "memory");
      if constexpr (sv >= 2800 && sv < 2804) {
        constexpr int i = sv - 2800;
        pp[i] = (f32x2){hs[i][0], hs[i][1]} + (f32x2){hs[i][2], hs[i][3]};
        asm volatile("" : "+v"(pp[i]));
      }
      if constexpr (sv >= 2804 && sv < 2812) {
        constexpr int i = (sv - 2804) >> 2, e = (sv - 2804) & 1, gv = ((sv - 2804) >> 1) & 1;
        w8[sv - 2804] = fsel(lane & 1, pp[2 * i + 1 - gv][e], pp[2 * i + gv][e]);
        asm volatile("" : "+v"(w8[sv - 2804]));
      }
      if constexpr (sv >= 2812 && sv < 2816) {
        constexpr int i = (sv - 2812) >> 1, e = (sv - 2812) & 1;
        pp[i][e] = w8[4 * i + e] + dpp<0xB1>(w8[4 * i + 2 + e]);
        asm volatile("" : "+v"(pp[i]));
      }
      if constexpr (sv >= 2816 && sv < 2820) {
        constexpr int e = (sv - 2816) & 1, gv = (sv - 2816) >> 1;
        w8[sv - 2816] = fsel(lane & 2, pp[1 - gv][e], pp[gv][e]);
        asm volatile("" : "+v"(w8[sv - 2816]));
      }
      if constexpr (sv == 2820 || sv == 2821) { w8[sv - 2816] = w8[sv - 2820] + dpp<0x4E>(w8[sv - 2818]); asm volatile("" : "+v"(w8[sv - 2816])); }
      if constexpr (sv == 2823 || sv == 2824) {
        w8[sv - 2817] = (w8[sv - 2819] + dpp<0x124>(w8[sv - 2819])) * (1.f / D_); asm volatile("" : "+v"(w8[sv - 2817]));
      }
      if constexpr (sv == 2826) { w8[7] = fmaxf(w8[7] - w8[6] * w8[6], 0.f); asm volatile("" : "+v"(w8[7])); }
      if constexpr (sv == 2900) *(LAS f32x2*)(size_t)sra = (f32x2){-w8[6], w8[7] + 1e-5f};
      if constexpr (sv == 2902) {
        asm volatile("s_waitcnt vmcnt(0)" : "+v"(cp[0]), "+v"(cp[1]), "+v"(cp[2]), "+v"(cp[3]) :: "memory");
        cp[0] += cp[1]; asm volatile("" : "+v"(cp[0]));
      }
      if constexpr (sv == 2903) { cp[2] += cp[3]; asm volatile("" : "+v"(cp[2])); }
      if constexpr (sv == 2904) { cp[0] += cp[2]; asm volatile("" : "+v"(cp[0])); }
      if constexpr (sv == 2906) { cp[0] += dpp<0xB1>(cp[0]); asm volatile("" : "+v"(cp[0])); }
      if constexpr (sv == 2908) *(LAS float*)(size_t)sca = cp[0];
    } else if constexpr (sv == 3000) {
      touchv(to, wd);
    }
  });
  static_assert(C3::NMAIN == 26 && C3::XS == 28, "K3 schedule");
  {
    float nm[4];
#pragma unroll
    for (int m = 0; m < 4; ++m) nm[m] = sR[(wm * 64 + m * 16 + fr) * 2];
#pragma unroll
    for (int j = 0; j < 7; ++j) {
      const f32x4 c4 = *(const LAS f32x4*)(sC + (wn * 14 + 2 * j) * 8 + 4 * g);
#pragma unroll
      for (int m = 0; m < 4; ++m) L.acc[m][j] += c4 * nm[m];
    }
  }
  const int hi = lane >> 5;
  const int apo = ((rb * 2 + (wm >> 1)) * 448 + ct * 14 + wn * 7) * 2048 + ((wm & 1) * 64 + fr) * 16;
  constexpr float K1c = -2.f * 0.7978845608028654f * 1.4426950408889634f, K2c = K1c * 0.044715f;
  auto glu = [&](f32x4 a, f32x4 b, float q, float r1, float r3) -> u32x2 {
    const f32x4 z = a * ((a * a) * r3 + r1);
    f32x4 e;
#pragma unroll
    for (int r = 0; r < 4; ++r) e[r] = __builtin_amdgcn_exp2f(z[r]);
    e = e * q + q;
#pragma unroll
    for (int r = 0; r < 4; ++r) e[r] = __builtin_amdgcn_rcpf(e[r]);
    const f32x4 o = (a * b) * e;
    u32x2 w; w.x = pk2(o[0], o[1]); w.y = pk2(o[2], o[3]);
    return w;
  };
  u32x2 w6[2];
#pragma unroll
  for (int mp = 0; mp < 2; ++mp) {
    float q[2], r1[2], r3[2];
#pragma unroll
    for (int mm = 0; mm < 2; ++mm) {
      q[mm] = sR[(wm * 64 + (2 * mp + mm) * 16 + fr) * 2 + 1];
      const float rstd = __builtin_amdgcn_rsqf(q[mm]);
      r1[mm] = K1c * rstd; r3[mm] = K2c * rstd * rstd * rstd;
    }
#pragma unroll
    for (int j = 0; j < 3; ++j) {
      const u32x2 wa = glu(L.acc[2 * mp][j], L.acc[2 * mp][3 + j], q[0], r1[0], r3[0]);
      const u32x2 wb = glu(L.acc[2 * mp + 1][j], L.acc[2 * mp + 1][3 + j], q[1], r1[1], r3[1]);
      st16<AUXK(3)>(act, apo + (2 * j + (g >> 1)) * 2048 + (2 * mp + (g & 1)) * 256, xpose2(wa, wb));
    }
    f32x4 G, U;
#pragma unroll
    for (int r = 0; r < 4; ++r) {
      auto sw = __builtin_amdgcn_permlane32_swap(__float_as_uint(L.acc[2 * mp][6][r]), __float_as_uint(L.acc[2 * mp + 1][6][r]), false, false);
      G[r] = __uint_as_float(sw[0]); U[r] = __uint_as_float(sw[1]);
    }
    w6[mp] = glu(G, U, hi ? q[1] : q[0], hi ? r1[1] : r1[0], hi ? r3[1] : r3[0]);
  }
  st16<AUXK(3)>(act, apo + 6 * 2048 + (2 * (g & 1) + (g >> 1)) * 256, xpose2(w6[0], w6[1]));
  wait_vm<0>();
  L.ts();
  STAMP(3, 3);
  L.tsdump<3>();
}

using C4 = Cfg<128, 16, 2, 4, 4, 2, 4, 4, 64, 56, 8, 3, 0x3u>;
__global__ __launch_bounds__(C4::NTHR, 2) void k4_down(const u16* __restrict__ act, const u16* __restrict__ wd, const u16* __restrict__ h,
                                                       u16* __restrict__ out, unsigned* __restrict__ xbuf) {
  extern __shared__ __attribute__((aligned(16))) char smem[];
  LAS char* lds = (LAS char*)smem;
  const int bid = blockIdx.x, xcd = bid & 7, idx = bid >> 3;
  const int kh = xcd & 1, rb = (xcd >> 1) * 4 + (idx & 3), cb = idx >> 2;
  STAMP(4, 0);
  u32x4 ka = karg4();
  sched_fence();
  Loop<C4> L;
  auto bsrc = [&](int k, int c) -> unsigned { return (k * D_ + c * 8) * 2; };
  L.init0(lds, DF_ * 2, bsrc, 2 * kh);
  karg_wait(ka);
  L.init1(karg_ptr(ka, 0) + (long)(rb * 448 + kh * 224) * 2048, karg_ptr(ka, 1) + ((long)kh * 1792 * D_ + cb * 128) * 2, 64L * D_ * 2);
  const int lane = L.lane, fr = lane & 15, g = lane >> 4, wm = L.wm, wn = L.wn;
  const long off0 = ((long)rb * 128 + wm * 64 + kh * 32 + fr) * D_ + cb * 128 + wn * 32 + (g & 1) * 16 + (g >> 1) * 8;
  u32x4 xr[2];
  L.run<4>([&](auto) {
#pragma unroll
    for (int m = 0; m < 2; ++m) xr[m] = *(const u32x4*)(h + off0 + m * 16 * D_);
  });
  const __amdgpu_buffer_rsrc_t rx = __builtin_amdgcn_make_buffer_rsrc((void*)xbuf, 0, XBUF_BYTES, 0x00020000);
  const int xo = L.wid * 2048 + lane * 16;
#pragma unroll
  for (int i = 0; i < 2; ++i) {
    const f32x4 a = L.acc[2 + i][0], b = L.acc[2 + i][1];
    const u32x4 v = {pk2(a[0], a[1]), pk2(a[2], a[3]), pk2(b[0], b[1]), pk2(b[2], b[3])};
    __builtin_amdgcn_raw_buffer_store_b128(v, rx, bid * 16384 + xo + i * 1024, 0, 16);
  }
  u32x4 rv[2];
  for (int spin = 0; spin < (1 << 22); ++spin) {
    asm volatile("" ::: "memory");
    bool ok = true;
#pragma unroll
    for (int i = 0; i < 2; ++i) rv[i] = __builtin_amdgcn_raw_buffer_load_b128(rx, (bid ^ 1) * 16384 + xo + i * 1024, 0, 16);
#pragma unroll
    for (int i = 0; i < 2; ++i) ok = ok && rv[i][0] != SENT && rv[i][1] != SENT && rv[i][2] != SENT && rv[i][3] != SENT;
    if (__all(ok)) break;
  }
#pragma unroll
  for (int m = 0; m < 2; ++m) {
    const u32x4 r = xpose2((u32x2){xr[m][0], xr[m][1]}, (u32x2){xr[m][2], xr[m][3]});
    u32x2 w[2];
#pragma unroll
    for (int j = 0; j < 2; ++j) {
      const f32x4 a = L.acc[m][j];
      w[j].x = pk2(bflo(r[2 * j]) + (a[0] + bflo(rv[m][2 * j])), bfhi(r[2 * j]) + (a[1] + bfhi(rv[m][2 * j])));
      w[j].y = pk2(bflo(r[2 * j + 1]) + (a[2] + bflo(rv[m][2 * j + 1])), bfhi(r[2 * j + 1]) + (a[3] + bfhi(rv[m][2 * j + 1])));
    }
    st16<AUXK(4)>(out, (off0 + m * 16 * D_) * 2, xpose2(w[0], w[1]));
  }
  L.ts();
  STAMP(4, 3);
  L.tsdump<4>();
}

extern "C" void kernel_launch(void* const* d_in, const int* in_sizes, int n_in, void* d_out, int out_size, void* d_ws,
                              size_t ws_size, hipStream_t stream) {
  const u16* x = (const u16*)d_in[0];
  const u16* wqkv = (const u16*)d_in[1];
  const u16* wo = (const u16*)d_in[2];
  const u16* wg = (const u16*)d_in[4];
  const u16* wu = (const u16*)d_in[5];
  const u16* wd = (const u16*)d_in[6];
  u16* out = (u16*)d_out;
  char* ws = (char*)d_ws;
  u16* pq = (u16*)ws;                      ws += (size_t)S_ * D_ * 2;
  u16* o = (u16*)ws;                       ws += (size_t)S_ * D_ * 2;
  u16* h = (u16*)ws;                       ws += (size_t)S_ * D_ * 2;
  u16* act = (u16*)ws;                     ws += (size_t)S_ * DF_ * 2;
  float* kvp = (float*)ws;                 ws += (size_t)NH_ * 16 * KVP * 4;
  float* hstat = (float*)ws;               ws += (size_t)S_ * 16 * 2 * 4;
  float* csp = (float*)ws;                 ws += (size_t)8 * 2 * DF_ * 4;
  unsigned* xbuf = (unsigned*)ws;          ws += (size_t)XBUF_BYTES;
  (void)hipFuncSetAttribute((const void*)k1_qkv, hipFuncAttributeMaxDynamicSharedMemorySize, C1::LDS_BYTES);
  (void)hipFuncSetAttribute((const void*)k2_wo, hipFuncAttributeMaxDynamicSharedMemorySize, C2::LDS_BYTES);
  (void)hipFuncSetAttribute((const void*)k3_glu, hipFuncAttributeMaxDynamicSharedMemorySize, K3_LDS);
  (void)hipFuncSetAttribute((const void*)k4_down, hipFuncAttributeMaxDynamicSharedMemorySize, C4::LDS_BYTES);
  k1_qkv<<<256, C1::NTHR, C1::LDS_BYTES, stream>>>(x, wqkv, pq, kvp, wo, wg);
  k15_o<<<256, 512, 0, stream>>>(pq, kvp, o, wg, wu, csp);
  k2_wo<<<256, C2::NTHR, C2::LDS_BYTES, stream>>>(o, wo, x, h, hstat);
  k3_glu<<<256, C3::NTHR, K3_LDS, stream>>>(h, wg, wu, hstat, act, wd, xbuf);
  k4_down<<<256, C4::NTHR, C4::LDS_BYTES, stream>>>(act, wd, h, out, xbuf);
}
```

```cpp
#include <hip/hip_runtime.h>
#include <hip/hip_bf16.h>
#include <stdint.h>

typedef unsigned short u16;
typedef short bf16x8 __attribute__((ext_vector_type(8)));
typedef short s16x4 __attribute__((ext_vector_type(4)));
typedef float f32x4 __attribute__((ext_vector_type(4)));
typedef float f32x2 __attribute__((ext_vector_type(2)));
typedef unsigned u32x4 __attribute__((ext_vector_type(4)));
typedef unsigned u32x2 __attribute__((ext_vector_type(2)));
typedef __bf16 bf16x2_t __attribute__((ext_vector_type(2)));
#define LAS __attribute__((address_space(3)))
#define DEV __device__ __forceinline__
template <int V> struct IC { static constexpr int value = V; };

constexpr int S_ = 2048, D_ = 1024, DF_ = 3584, NH_ = 32;
constexpr int KVP = 1056;
constexpr unsigned SENT = 0xFFFFFFFFu;
constexpr int XBUF_BYTES = 256 * 16384;

DEV float bflo(unsigned v) { return __uint_as_float(v << 16); }
DEV float bfhi(unsigned v) { return __uint_as_float(v & 0xffff0000u); }
DEV unsigned pk2(float lo, float hi) {
  bf16x2_t r; r.x = (__bf16)lo; r.y = (__bf16)hi; return __builtin_bit_cast(unsigned, r);
}
template <int N> DEV void wait_vm() { asm volatile("s_waitcnt vmcnt(%0)" ::"n"(N) : "memory"); }
DEV void wait_lgkm0() { __builtin_amdgcn_s_waitcnt(0xC07F); }
DEV void bar() { __builtin_amdgcn_s_barrier(); }
DEV void lbar() { wait_lgkm0(); bar(); asm volatile("" ::: "memory"); }
DEV void sched_fence() { __builtin_amdgcn_sched_barrier(0); }
DEV void glds_v(const void* g, unsigned lds_addr) {
  asm volatile("s_mov_b32 m0, %1\n\ts_nop 0\n\tglobal_load_lds_dwordx4 %0, off" ::"v"(g), "s"(lds_addr) : "memory");
}
DEV void glds_vm(const void* g, unsigned lds_addr, unsigned long long mask) {
  asm volatile("s_mov_b32 m0, %1\n\ts_mov_b64 exec, %2\n\tglobal_load_lds_dwordx4 %0, off\n\ts_mov_b64 exec, -1"
               ::"v"(g), "s"(lds_addr), "s"(mask) : "memory");
}
DEV void glds_s(unsigned voff, const void* sbase, unsigned lds_addr) {
  asm volatile("s_mov_b32 m0, %2\n\ts_nop 0\n\tglobal_load_lds_dwordx4 %0, %1" ::"v"(voff), "s"(sbase), "s"(lds_addr) : "memory");
}
DEV void glds_sm(unsigned voff, const void* sbase, unsigned lds_addr, unsigned long long mask) {
  asm volatile("s_mov_b32 m0, %2\n\ts_mov_b64 exec, %3\n\tglobal_load_lds_dwordx4 %0, %1\n\ts_mov_b64 exec, -1"
               ::"v"(voff), "s"(sbase), "s"(lds_addr), "s"(mask) : "memory");
}
DEV void touch(const void* g, unsigned lds_addr) {
  asm volatile("s_mov_b32 m0, %1\n\ts_nop 0\n\tglobal_load_lds_dword %0, off" ::"v"(g), "s"(lds_addr) : "memory");
}
DEV void touchv(unsigned voff, const void* sbase) {
  asm volatile("global_load_dword v255, %0, %1" :: "v"(voff), "s"(sbase) : "memory", "v255");
}
typedef unsigned u32x8 __attribute__((ext_vector_type(8)));
DEV u32x4 karg4() { u32x4 r; asm volatile("s_load_dwordx4 %0, %1, 0x0" : "=s"(r) : "s"(__builtin_amdgcn_kernarg_segment_ptr())); return r; }
DEV u32x8 karg8() { u32x8 r; asm volatile("s_load_dwordx8 %0, %1, 0x0" : "=s"(r) : "s"(__builtin_amdgcn_kernarg_segment_ptr())); return r; }
template <class T> DEV void karg_wait(T& r) { asm volatile("s_waitcnt lgkmcnt(0)" : "+s"(r)); }
template <class T> DEV const char* karg_ptr(const T& r, int i) { return (const char*)(((unsigned long long)r[2 * i + 1] << 32) | r[2 * i]); }
template <int OFF> DEV void ld4(f32x4& d, unsigned voff, const void* sbase) {
  asm volatile("global_load_dwordx4 %0, %1, %2 offset:%3" : "=v"(d) : "v"(voff), "s"(sbase), "n"(OFF) : "memory");
}
DEV void ld1(float& d, unsigned voff, const void* sbase) {
  asm volatile("global_load_dword %0, %1, %2" : "=v"(d) : "v"(voff), "s"(sbase) : "memory");
}
template <int CTRL> DEV float dpp(float v) { return __int_as_float(__builtin_amdgcn_update_dpp(0, __float_as_int(v), CTRL, 0xF, 0xF, true)); }
#define SENTSC ""
DEV void stsent(unsigned voff, const void* sbase) {
  const u32x4 sv = {SENT, SENT, SENT, SENT};
  asm volatile("global_store_dwordx4 %0, %1, %2 " SENTSC "\n\ts_nop 1" :: "v"(voff), "v"(sv), "s"(sbase) : "memory");
}
DEV float fsel(bool c, float a, float b) { return c ? a : b; }
DEV f32x4 mfma16(bf16x8 a, bf16x8 b, f32x4 c) { return __builtin_amdgcn_mfma_f32_16x16x32_bf16(a, b, c, 0, 0, 0); }
DEV void mfma_acc(f32x4& c, bf16x8 a, bf16x8 b) {
  asm volatile("v_mfma_f32_16x16x32_bf16 %0, %1, %2, %0" : "+v"(c) : "v"(a), "v"(b));
}
DEV void mfma_z(f32x4& c, bf16x8 a, bf16x8 b) {
  asm volatile("v_mfma_f32_16x16x32_bf16 %0, %1, %2, 0" : "=&v"(c) : "v"(a), "v"(b));
}
template <bool Z> DEV void mfma_a(f32x4& c, bf16x8 a, bf16x8 b) { if constexpr (Z) mfma_z(c, a, b); else mfma_acc(c, a, b); }
DEV bf16x8 cat(s16x4 a, s16x4 b) { return __builtin_shufflevector(a, b, 0, 1, 2, 3, 4, 5, 6, 7); }
DEV s16x4 rdtr(unsigned a) { return __builtin_amdgcn_ds_read_tr16_b64_v4i16((LAS s16x4*)(size_t)a); }

#define FINEW 0
#define STAMP(kid, i)
#define SENTS 12
#define SB 5
#define TCH 28
#define TCH2 30
template <int BM_, int NC_, int WM_, int WN_, int MT_, int NT_, int RA_, int RB_, int KB_, int NS_, int P_, int KIND_, unsigned TRANS_>
struct Cfg {
  static constexpr int BM = BM_, NC = NC_, WM = WM_, WN = WN_, MT = MT_, NT = NT_, RA = RA_, RB = RB_, KB = KB_, NS = NS_, P = P_;
  static constexpr int KIND = KIND_;
  static constexpr unsigned TRANS = TRANS_;
  static constexpr bool STATS = KIND == 1, PERM = KIND == 1 || KIND == 3;
  static constexpr bool AFRAG = KIND == 3;
  static constexpr int NW = WM * WN, NTHR = NW * 64;
  static constexpr int ATILE = BM * 128, BSLOT = KIND_ == 2 ? 15360 : KB * NC * 16;
  static constexpr int BHF = KIND_ == 2 ? 4 * 224 : 4 * NC * 16;
  static constexpr int NA = BM * 8 / NTHR;
  static constexpr int CPW = KB * NC / NW;
  static constexpr int NB = (CPW + 63) / 64;
  static constexpr int NA1 = KB == 64 ? NA / 2 : (NA + 1) / 2, NB1 = KB == 64 ? (NB + 1) / 2 : NB;
  static constexpr int LDS_B0 = RA * ATILE, LDS_BYTES = RA * ATILE + RB * BSLOT;
  static constexpr int NTA = NS / 2;
#define RQN 7
#define RQD 8
  static constexpr int RQ1 = (MT * NT * RQN) / RQD;
  static_assert(BM * 8 % NTHR == 0, "A tile split");
  static_assert((KB * NC) % NW == 0, "B slot split");
  static_assert(P % 2 == 0 && (P / 2) % RA == 0 && (KB == 32 ? P % RB == 0 : (P / 2) % RB == 0), "period");
  static_assert(BM == WM * MT * 16, "M tiling");
  static_assert(NC * 8 == WN * NT * 16, "N tiling");
  DEV static constexpr int bswz(int k) {
    return NC == 28 ? 2 * ((k >> 3) & 1)
         : (NC % 16 == 8) ? 2 * (((k >> 1) & 1) | (((k >> 3) & 1) << 1))
                          : 2 * ((k & 3) | (((k >> 3) & 1) << 2));
  }
  static constexpr bool hasB(int t) {
    if (KB == 32) return t + RB >= 0 && t + RB < NS;
    int tile = (t & 1) ? ((t - 1) >> 1) + RB : ((t - 2) >> 1) + RB;
    return tile >= 0 && tile < NTA;
  }
  static constexpr bool hasA(int t) {
    int tile = (t & 1) ? ((t - 1) >> 1) + RA : ((t - 2) >> 1) + RA;
    return tile >= 0 && tile < NTA;
  }
  static constexpr int nB(int t) { return !hasB(t) ? 0 : KB == 32 ? NB : (t & 1) ? NB1 : NB - NB1; }
  static constexpr int nA(int t) { return !hasA(t) ? 0 : (t & 1) ? NA1 : NA - NA1; }
  static constexpr int T0 = -(2 * (RA > RB ? RA : RB) + 2);
  static constexpr int lastIssue() { int l = 0; for (int t = 0; t < NS; ++t) if (nA(t) + nB(t) > 0) l = t; return l; }
  static constexpr int XS = lastIssue() + 1;
  static constexpr int nX(int t) {
    if (KIND == 0) return t == XS ? 4 : 0;
    if (KIND == 1) return t == XS ? 2 : 0;
    if (KIND == 2) return (t == TCH ? 1 : 0) + (t == TCH2 ? 1 : 0) + ((t == SB || t == SB + 1) ? 4 : 0) + (t == SENTS ? 2 : 0);
    return t == XS ? 2 : 0;
  }
  static constexpr int hk(int t) { return KIND == 2 && ((t >= SB && t <= SB + 4) || t == SENTS || t == TCH || t == TCH2) ? 2 : nX(t) > 0 ? 1 : 0; }
  static constexpr int nXP(int t) { return KIND == 1 && t == 0 ? 1 : 0; }
  static constexpr int TPRE = KB == 32 ? -3 : -4, RATE = KB == 32 ? 2 : 4;
  static constexpr int vlast(int s) { int v = TPRE + RATE * (s + 1); return s < 0 ? TPRE : (v < s ? v : s); }
  static constexpr int nhead() { int s = 0; while (vlast(s - 1) != s - 1) ++s; return s; }
  static constexpr int H = nhead();
  static constexpr int TF = KB == 32 ? (-RB > -2 * RA + 2 ? -RB : -2 * RA + 2) : -2 * (RA < RB ? RA : RB) + 2;
  static constexpr int nfirst() { int n = 0; for (int t = T0; t <= TF; ++t) n += nA(t) + nB(t); return n; }
  static constexpr int npro() { int n = 0; for (int t = T0; t <= TPRE; ++t) n += nA(t) + nB(t); return n; }
  static constexpr int nmain() {
    if (KIND == 2) return H;
    int lim = 0;
    while (lim < NS && hasA(lim) && hasB(lim)) ++lim;
    return H + ((lim - H) / P) * P;
  }
  static constexpr int vm_at(int s) {
    int posA[64] = {}, posB[128] = {}, cntA[64] = {}, cntB[128] = {};
    int tot = 0;
    for (int r = -1; r < s || r < 0; ++r) {
      if (r >= 0) tot += nX(r);
      for (int t = (r < 0 ? T0 : vlast(r - 1) + 1); t <= vlast(r); ++t) {
        const int tl = (t & 1) ? ((t - 1) >> 1) : ((t - 2) >> 1);
        if (nB(t)) { tot += nB(t); const int i = KB == 32 ? t + RB : tl + RB; posB[i] = tot; cntB[i] += nB(t); }
        if (nA(t)) { tot += nA(t); posA[tl + RA] = tot; cntA[tl + RA] += nA(t); }
      }
      if (r >= 0) tot += nXP(r);
    }
    int nb = s + 1;
    if (nb >= NS) return 63;
    const int ib = KB == 32 ? nb : nb / 2;
    if (cntB[ib] != NB || cntA[nb / 2] != NA) return -1;
    int need = posB[ib] > posA[nb / 2] ? posB[ib] : posA[nb / 2];
    int v = tot - need;
    return v > 63 ? 63 : v;
  }
  static constexpr int NMAIN = nmain();
  static constexpr int vm_main(int u) {
    int v = 63;
    for (int s = H + u; s < NMAIN; s += P) { int x = vm_at(s); v = x < v ? x : v; }
    return v;
  }
};

template <class C> struct Loop {
  unsigned lbase;
  const char* abase;
  unsigned ga[C::NA];
  unsigned gbo[C::NB];
  const char* bbase;
  unsigned bdst;
  long bstep;
  int wid, lane, wm, wn;
  unsigned ra[2][C::PERM ? C::MT : 1];
  unsigned rb[C::NT];
  bf16x8 FA[2][C::MT];
  s16x4 FBh[2][C::NT][2];
  f32x4 acc[C::MT][C::NT];
  f32x4 rs, rq;
  f32x4 cs[3];
  bf16x8 ones;

  template <int SET, int I> DEV void readOp(int aslot, int ks, int boff) {
    if constexpr (I < C::MT) {
      unsigned a = C::PERM ? ra[ks][C::PERM ? I : 0] : ra[ks][0] + I * 2048;
      FA[SET][I] = *(const LAS bf16x8*)(size_t)(a + aslot * C::ATILE);
    } else {
      constexpr int j = (I - C::MT) / 2, hf = (I - C::MT) & 1;
      FBh[SET][j][hf] = rdtr(rb[j] + boff + hf * C::BHF);
    }
  }
  static constexpr int rord(int i) {
    if (i == 0) return 0;
    if (i <= 2 * C::NT) return C::MT + i - 1;
    return i - 2 * C::NT;
  }
  template <int SET> DEV bf16x8 FB(int j) const { return cat(FBh[SET][j][0], FBh[SET][j][1]); }
  static constexpr int NMM = C::MT * C::NT, NR = C::MT + 2 * C::NT;
  static constexpr int nm(int U, int CP) { return NMM + (C::STATS ? ((U & 1) == CP ? 5 : 2) : 0); }
  template <int SET, int Q, bool Z> DEV void mmaOp() {
    if constexpr (Q < NMM) {
      constexpr int m = Q / C::NT, j = Q % C::NT;
      if constexpr ((C::TRANS >> j) & 1) mfma_a<Z>(acc[m][j], FB<SET>(j), FA[SET][m]);
      else mfma_a<Z>(acc[m][j], FA[SET][m], FB<SET>(j));
    } else if constexpr (Q == NMM) {
      mfma_a<Z>(rs, ones, FA[SET][0]);
    } else if constexpr (Q == NMM + 1) {
      mfma_a<Z>(rq, FA[SET][0], FA[SET][0]);
    } else {
      mfma_acc(cs[Q - NMM - 2], ones, FB<SET>(Q - NMM - 2));
    }
  }
  template <int PAR, bool IB, int I> DEV void gldsOp(int bslot, int aslot) {
    constexpr int nb = !IB ? 0 : C::KB == 32 ? C::NB : PAR ? C::NB1 : C::NB - C::NB1;
    if constexpr (I < nb) {
      constexpr int i = (C::KB == 64 && !PAR) ? C::NB1 + I : I;
      constexpr int rem = C::CPW - 64 * i;
      const unsigned dst = bdst + bslot * C::BSLOT + i * 1024;
      if constexpr (rem >= 64) glds_s(gbo[i], bbase, dst);
      else glds_sm(gbo[i], bbase, dst, (1ull << (rem & 63)) - 1ull);
      if constexpr (i == C::NB - 1) { bbase += bstep; asm volatile("" : "+s"(bbase)); }
    } else {
      constexpr int i = (PAR ? 0 : C::NA1) + (I - nb);
      glds_s(ga[i], abase, lbase + aslot * C::ATILE + (wid * C::NA + i) * 1024);
      if constexpr (i == C::NA - 1) { abase += C::AFRAG ? C::ATILE : 128; asm volatile("" : "+s"(abase)); }
    }
  }
  static constexpr int spread(int N, int Q0, int Q1, int Q) { return Q <= Q0 ? 0 : Q >= Q1 ? N : (N * (Q - Q0) + (Q1 - Q0) - 1) / (Q1 - Q0); }
  template <int SET, int LO, int HI> DEV void readRange(int aslot, int ks, int boff) {
    if constexpr (LO < HI) { readOp<SET, rord(LO)>(aslot, ks, boff); readRange<SET, LO + 1, HI>(aslot, ks, boff); }
  }
  static constexpr int wslotA(int U) { return (((U & 1) ? (U - 1) / 2 : (U - 2 + 2 * C::P) / 2)) % C::RA; }
  static constexpr int wslotB(int U) { return C::KB == 32 ? U % C::RB : (((U & 1) ? (U - 1) / 2 : (U - 2 + 2 * C::P) / 2)) % C::RB; }
  static constexpr int rslotA(int U1) { return (U1 / 2) % C::RA; }
  static constexpr int roffB(int U1) { return C::KB == 32 ? (U1 % C::RB) * C::BSLOT : ((U1 / 2) % C::RB) * C::BSLOT + (U1 & 1) * 32 * C::NC * 16; }
  static constexpr int nops(int V, int VHI, bool MAIN) {
    int n = 0;
    for (int v = V; v <= VHI; ++v)
      n += MAIN ? ((C::KB == 32 ? C::NB : (v & 1) ? C::NB1 : C::NB - C::NB1) + ((v & 1) ? C::NA1 : C::NA - C::NA1)) : C::nB(v) + C::nA(v);
    return n;
  }
  template <int V, int VHI, bool MAIN, int I> DEV void vop() {
    if constexpr (V <= VHI) {
      constexpr int n = nops(V, V, MAIN), U = ((V % C::P) + C::P) % C::P;
      if constexpr (I < n) gldsOp<U & 1, MAIN || C::hasB(V), I>(wslotB(U), wslotA(U));
      else vop<V + 1, VHI, MAIN, I - n>();
    }
  }
  template <int V, int VHI, bool MAIN, int LO, int HI> DEV void vopRange() {
    if constexpr (LO < HI) { vop<V, VHI, MAIN, LO>(); vopRange<V, VHI, MAIN, LO + 1, HI>(); }
  }
  template <int CP, int U, int V, int VHI, bool MAIN, bool RN, int Q, int HS, class H> DEV void body(H& hook) {
    constexpr int NM = nm(U, CP);
    if constexpr (Q < NM) {
      constexpr int PAR = U & 1;
      constexpr int NG = nops(V, VHI, MAIN);
      constexpr int RQ = C::RQ1 + (NM - NMM);
      mmaOp<PAR, Q, !MAIN && V == C::TPRE + 1>();
      vopRange<V, VHI, MAIN, spread(NG, 0, NM, Q), spread(NG, 0, NM, Q + 1)>();
      if constexpr (RN) readRange<PAR ^ 1, spread(NR, 0, RQ, Q), spread(NR, 0, RQ, Q + 1)>(rslotA(U + 1), PAR ^ 1, roffB(U + 1));
      if constexpr (HS >= 0) hook(IC<HS + Q>{});
      sched_fence();
      body<CP, U, V, VHI, MAIN, RN, Q + 1, HS>(hook);
    }
  }
  template <int CP, int S, int VM, bool MAIN, class H> DEV void step(H& hook) {
    constexpr int U = S % C::P;
    constexpr bool RN = MAIN || S + 1 < C::NS;
    if constexpr (RN && (C::KB == 32 || (U & 1))) {
      static_assert(VM >= 0, "schedule"); wait_vm<VM>(); bar();
    }
    sched_fence();
    constexpr int HM = MAIN ? 0 : C::hk(S);
    if constexpr (HM == 1) { hook(IC<S>{}); sched_fence(); }
    if constexpr (HM == 3) { if (wid >= C::NW / 2) hook(IC<S>{}); sched_fence(); }
    body<CP, U, C::vlast(S - 1) + 1, C::vlast(S), MAIN, RN, 0, HM == 2 ? S * 100 : -1>(hook);
    if constexpr (HM == 3) { if (wid < C::NW / 2) hook(IC<S>{}); sched_fence(); }
    if constexpr (C::nXP(S) > 0) { hook(IC<MAIN ? 2000 : 1000>{}); sched_fence(); }
    if constexpr (C::KB == 32 || !(U & 1)) wait_lgkm0();
    sched_fence();
  }
  template <int CP, int U, class H> DEV void mainSteps(H& hook) {
    if constexpr (U < C::P) { step<CP, C::H + U, C::vm_main(U), true>(hook); mainSteps<CP, U + 1>(hook); }
  }
  template <int CP, int Sx, int SE, class H> DEV void seqSteps(H& hook) {
    if constexpr (Sx < SE) { step<CP, Sx, C::vm_at(Sx), false>(hook); seqSteps<CP, Sx + 1, SE>(hook); }
  }
  template <int KID, int CP, class H> DEV void allSteps(H& hook) {
    pinAcc();
    sched_fence();
    ts();
    wait_vm<C::vm_at(-1)>();
    ts();
    bar();
    sched_fence();
    ts();
    readRange<0, 0, NR>(0, 0, 0);
    wait_lgkm0();
    sched_fence();
    STAMP(KID, 1);
    ts();
    pinAcc();
    asm volatile("s_nop 7" ::: "memory");
    sched_fence();
    seqSteps<CP, 0, C::H>(hook);
    for (int it = 0; it < (C::NMAIN - C::H) / C::P; ++it) mainSteps<CP, 0>(hook);
    seqSteps<CP, C::NMAIN, C::NS>(hook);
    asm volatile("s_nop 7\n\ts_nop 7\n\ts_nop 7" ::: "memory");
    sched_fence();
  }
  DEV void ts() {
  }
  template <int KID> DEV void tsdump() {
  }
  template <class BF> DEV void init0(LAS char* l, int lda, BF bsrc, int mperm) {
    lbase = (unsigned)(size_t)l;
    const int tid = threadIdx.x;
    wid = __builtin_amdgcn_readfirstlane(tid >> 6); lane = tid & 63;
    wm = wid / C::WN; wn = wid % C::WN;
    if (mperm < 0) mperm = wn;
#pragma unroll
    for (int i = 0; i < C::NA; ++i) {
      int q = (wid * C::NA + i) * 64 + lane, row = q >> 3, p = q & 7, c = p ^ ((row >> 1) & 7);
      ga[i] = C::AFRAG ? q * 16 : row * lda + c * 16;
      asm volatile("" : "+v"(ga[i]));
    }
#pragma unroll
    for (int i = 0; i < C::NB; ++i) {
      if constexpr (C::KIND == 2) {
        int q = i * 64 + lane;
        if (q >= 112) q = 111;
        const int k = q / 14, c = q - k * 14;
        gbo[i] = bsrc((wid & 3) * 8 + k, c);
      } else {
        const int q = wid * C::CPW + i * 64 + lane;
        const int k = q / C::NC, p = q % C::NC, idx = p ^ C::bswz(k);
        gbo[i] = bsrc(k, idx);
      }
      asm volatile("" : "+v"(gbo[i]));
    }
    bdst = lbase + C::LDS_B0 + (C::KIND == 2 ? wid * 1920 + (wid >> 2) * 16 : wid * C::CPW * 16);
    mp = mperm;
    sched_fence();
  }
  int mp;
  DEV void init2() {
    const int mperm = mp;
    const int fr = lane & 15, g = lane >> 4;
#pragma unroll
    for (int ks = 0; ks < 2; ++ks)
#pragma unroll
      for (int m = 0; m < (C::PERM ? C::MT : 1); ++m) {
        ra[ks][m] = C::AFRAG ? lbase + (4 * ks + g) * (C::BM * 16) + ((wm * C::MT + (C::PERM ? (m ^ mperm) : m)) * 16 + fr) * 16
                             : lbase + ((wm * C::MT + (C::PERM ? (m ^ mperm) : m)) * 16 + fr) * 128 + (((4 * ks + g) ^ (fr >> 1)) * 16);
        asm volatile("" : "+v"(ra[ks][m]));
      }
    const int q4 = fr >> 2, p4 = fr & 3, k = 8 * g + q4;
#pragma unroll
    for (int j = 0; j < C::NT; ++j) {
      if constexpr (C::KIND == 2) {
        const int mat = j < 3 ? 0 : j < 6 ? 1 : (p4 >> 1);
        const int ch = wn * 7 + (j < 6 ? 2 * (j % 3) + (p4 >> 1) : 6);
        rb[j] = lbase + C::LDS_B0 + (mat * 4 + g) * 1920 + mat * 16 + q4 * 224 + ch * 16 + 8 * (p4 & 1);
      } else {
        int idx;
        if (C::KIND == 1) idx = j * 8 + wn * 2 + (p4 >> 1);
        else idx = 2 * (wn * C::NT + j) + (p4 >> 1);
        rb[j] = lbase + C::LDS_B0 + k * C::NC * 16 + ((idx ^ C::bswz(k)) * 16) + 8 * (p4 & 1);
      }
      asm volatile("" : "+v"(rb[j]));
    }
    sched_fence();
  }
  DEV void init1(const char* Abase, const char* Bbase, long bstep_) {
    bstep = bstep_;
    abase = Abase; bbase = Bbase;
    vopRange<C::T0, C::TPRE, false, 0, C::nfirst()>();
    sched_fence();
    bar();
    sched_fence();
    vopRange<C::T0, C::TPRE, false, C::nfirst(), C::npro()>();
    sched_fence();
    init2();
    if constexpr (C::STATS) {
      u32x4 o4 = {0x3F803F80u, 0x3F803F80u, 0x3F803F80u, 0x3F803F80u};
      asm volatile("" : "+v"(o4));
      ones = __builtin_bit_cast(bf16x8, o4);
    }
#pragma unroll
    for (int j = 0; j < 3; ++j) cs[j] = (f32x4){0.f, 0.f, 0.f, 0.f};
  }
  DEV void pinAcc() {
    if constexpr (C::STATS) asm volatile("" : "+v"(cs[0]), "+v"(cs[1]), "+v"(cs[2]));
  }
  template <int KID, class H> DEV void run(H hook) {
    if (C::STATS && wm) allSteps<KID, 1>(hook);
    else allSteps<KID, 0>(hook);
    ts();
    STAMP(KID, 2);
  }
};

DEV u32x4 xpose2(u32x2 a, u32x2 b) {
  auto x = __builtin_amdgcn_permlane16_swap(a.x, b.x, false, false);
  auto y = __builtin_amdgcn_permlane16_swap(a.y, b.y, false, false);
  return (u32x4){x[0], y[0], x[1], y[1]};
}
#define STAUX 0
#define SC1M 29
#define AUXK(k) (((SC1M >> (k)) & 1) ? 16 : 0)
template <int AUX> DEV void st16(void* base, long byte_off, u32x4 v) {
  if constexpr (AUX == 0) *(u32x4*)((char*)base + byte_off) = v;
  else {
    const __amdgpu_buffer_rsrc_t r = __builtin_amdgcn_make_buffer_rsrc(base, 0, 0x7fffffff, 0x00020000);
    __builtin_amdgcn_raw_buffer_store_b128(v, r, (int)byte_off, 0, AUX);
  }
}
DEV u32x2 phi4(f32x4 u) {
  const f32x4 a = u * 1.4426950408889634f;
  f32x4 e, m;
#pragma unroll
  for (int r = 0; r < 4; ++r) { e[r] = __builtin_amdgcn_fmed3f(__builtin_amdgcn_exp2f(a[r]), 0.f, 1.f); m[r] = __builtin_fmaxf(u[r], 1e-6f); }
  const f32x4 p = m + e;
  u32x2 w; w.x = pk2(p[0], p[1]); w.y = pk2(p[2], p[3]);
  return w;
}

using C1 = Cfg<128, 24, 2, 4, 4, 3, 4, 4, 64, 32, 8, 1, 0x1u>;
static_assert(C1::XS == 25 && C1::LDS_BYTES == 163840 && C1::H == 1 && C1::NMAIN == 25 && C1::P == 8, "K1 schedule");
__global__ __launch_bounds__(C1::NTHR, 2) void k1_qkv(const u16* __restrict__ x, const u16* __restrict__ wqkv,
                                                      u16* __restrict__ pqT, float* __restrict__ kvp,
                                                      const u16* __restrict__ wo, const u16* __restrict__ wg) {
  extern __shared__ __attribute__((aligned(16))) char smem[];
  LAS char* lds = (LAS char*)smem;
  const int bid = blockIdx.x, xcd = bid & 7, idx = bid >> 3;
  const int rb = (xcd & 1) * 8 + (idx & 7), hp = (xcd >> 1) * 4 + (idx >> 3);
  STAMP(0, 0);
  u32x4 ka = karg4();
  sched_fence();
  Loop<C1> L;
  auto bsrc = [&](int k, int c) -> unsigned { return (k * (3 * D_) + (c >> 3) * D_ + (c & 7) * 8) * 2; };
  L.init0(lds, D_ * 2, bsrc, -1);
  karg_wait(ka);
  L.init1(karg_ptr(ka, 0) + (long)rb * 128 * D_ * 2, karg_ptr(ka, 1) + hp * 128, 64L * 3 * D_ * 2);
  const int lane = L.lane, fr = lane & 15, g = lane >> 4, wm = L.wm, wn = L.wn, wid = L.wid;
  const char* tp;
  {
    const int f = (idx * 8 + wid) * 60 + (lane < 60 ? lane : 0);
    const bool isx = f < 6144;
    const int f2 = isx ? f : f - 6144, row = f2 / 24, c = f2 - row * 24;
    const unsigned to = isx ? ((xcd & 1) * 1024 + (xcd >> 1) * 256 + row) * 2048 + 512 + c * 64
                            : (256 + 2 * row + (xcd & 1)) * 6144 + (c >> 3) * 2048 + (xcd >> 1) * 512 + (c & 7) * 64;
    tp = (isx ? karg_ptr(ka, 0) : karg_ptr(ka, 1)) + to;
  }
  L.run<0>([&](auto sx) {
    if constexpr (decltype(sx)::value == 1000) {
      asm volatile("global_load_dword v255, %0, off" :: "v"(tp) : "memory", "v255");
    } else {
      const unsigned o1 = (idx * 32 + wid * 4 + (lane >> 4)) * 2048 + (xcd >> 2) * 1024 + (lane & 15) * 64;
      touchv(o1, wo);
      const unsigned o2 = ((bid * 8 + wid) * 56 + (lane < 56 ? lane : 0)) * 64;
      touchv(o2, wg);
    }
  });
  LAS char* ep = lds;
  LAS float* sR = (LAS float*)ep;
  LAS float* sC = (LAS float*)(ep + 1024);
  LAS char* sF = ep + 4096;
  {
    const float a = L.rs[0];
    const float b = (fr & 2) ? ((fr & 1) ? L.rq[3] : L.rq[2]) : ((fr & 1) ? L.rq[1] : L.rq[0]);
    if (g == (fr >> 2)) {
      float mean = a * (1.f / D_);
      float var = fmaxf(b * (1.f / D_) - mean * mean, 0.f);
      float rstd = rsqrtf(var + 1e-5f);
      *(LAS f32x2*)(sR + (wm * 64 + wn * 16 + fr) * 2) = (f32x2){rstd, -mean * rstd};
#pragma unroll
      for (int j = 0; j < 3; ++j) sC[wm * 192 + j * 64 + wn * 16 + fr] = L.cs[j][0];
    }
  }
  L.ts();
  lbar();
  L.ts();
  {
    const int ik = 64 + wn * 16 + fr;
    const float ck = sC[ik] + sC[192 + ik], cv = sC[64 + ik] + sC[256 + ik];
#pragma unroll
    for (int m = 0; m < 4; ++m) {
      const int am = m ^ wn, r0 = wm * 64 + am * 16 + 4 * g;
      const f32x4 u0 = *(const LAS f32x4*)(sR + r0 * 2), u1 = *(const LAS f32x4*)(sR + r0 * 2 + 4);
      const f32x4 sa = {u0[0], u0[2], u1[0], u1[2]}, sb = {u0[1], u0[3], u1[1], u1[3]};
      const f32x4 k = L.acc[m][1] * sa + (sb * ck + 1e-6f);
      const f32x4 v = L.acc[m][2] * sa + sb * cv;
      u32x2 a = phi4(k), b;
      b.x = pk2(v[0], v[1]); b.y = pk2(v[2], v[3]);
      LAS char* f = sF + (wn * 4 + wm * 2 + (am >> 1)) * 1024 + lane * 16 + (am & 1) * 8;
      *(LAS u32x2*)f = a;
      *(LAS u32x2*)(f + 16384) = b;
    }
  }
  L.ts();
  {
    const int ic = wn * 16 + 4 * g;
    const f32x4 c4 = *(const LAS f32x4*)(sC + ic) + *(const LAS f32x4*)(sC + 192 + ic);
    u32x2 w[4];
#pragma unroll
    for (int m = 0; m < 4; ++m) {
      const f32x2 st = *(const LAS f32x2*)(sR + (wm * 64 + (m ^ wn) * 16 + fr) * 2);
      w[m] = phi4(L.acc[m][0] * st.x + (c4 * st.y + 1e-6f));
    }
    const long pb = ((long)(hp * 4 + wn) * S_ + rb * 128 + wm * 64 + fr) * 16 + (g >> 1) * 8;
#pragma unroll
    for (int m = 0; m < 4; m += 2) st16<AUXK(0)>(pqT, (pb + (((m + (g & 1)) ^ wn) * 16) * 16) * 2, xpose2(w[m], w[m + 1]));
  }
  L.ts();
  lbar();
  L.ts();
  {
    f32x4 d = {0.f, 0.f, 0.f, 0.f}, z = d;
#pragma unroll
    for (int G = 0; G < 4; ++G) {
      const bf16x8 fk = *(const LAS bf16x8*)(sF + (wn * 4 + G) * 1024 + lane * 16);
      const bf16x8 fv = *(const LAS bf16x8*)(sF + 16384 + (((wn & 2) + wm) * 4 + G) * 1024 + lane * 16);
      d = mfma16(fk, fv, d);
      z = mfma16(fk, L.ones, z);
    }
    const long kp = ((long)(hp * 2 + (wn >> 1)) * 16 + rb) * KVP;
    st16<AUXK(0)>(kvp, (kp + ((wn & 1) * 2 + wm) * 256 + lane * 4) * 4, __builtin_bit_cast(u32x4, d));
    if (wm == 0 && fr == 0) st16<AUXK(0)>(kvp, (kp + 1024 + (wn & 1) * 16 + 4 * g) * 4, __builtin_bit_cast(u32x4, z));
  }
  L.ts();
  wait_vm<0>();
  L.ts();
  STAMP(0, 3);
  L.tsdump<0>();
}

__global__ __launch_bounds__(512) void k15_o(const u16* __restrict__ pqT, const float* __restrict__ kvp, u16* __restrict__ o,
                                             const u16* __restrict__ wg, const u16* __restrict__ wu, float* __restrict__ csp) {
  __shared__ __attribute__((aligned(16))) u16 kvT[33 * 40];
  __shared__ __attribute__((aligned(16))) float sCS[16 * 228];
  const int bid = blockIdx.x, head = bid & 31, rb = bid >> 5;
  const int tid = threadIdx.x, lane = tid & 63, wid = __builtin_amdgcn_readfirstlane(tid >> 6), fr = lane & 15, g = lane >> 4;
  STAMP(1, 0);
  f32x4 z0 = {0.f, 0.f, 0.f, 0.f}, z1 = z0;
  if (wid == 0) {
    const f32x4* p = (const f32x4*)(kvp + ((long)head * 16 + (lane >> 3)) * KVP + 1024) + (lane & 7);
    z0 = p[0]; z1 = p[8 * (KVP / 4)];
  }
  sched_fence();
  f32x4 v[8];
  {
    const f32x4* p = (const f32x4*)(kvp + ((long)head * 16 + (tid & 1) * 8) * KVP) + (tid >> 1);
#pragma unroll
    for (int b = 0; b < 8; ++b) v[b] = p[b * (KVP / 4)];
  }
  bf16x8 fq[2];
#pragma unroll
  for (int m = 0; m < 2; ++m)
    fq[m] = *(const bf16x8*)(pqT + ((long)(head * 2 + (g >> 1)) * S_ + rb * 256 + wid * 32 + m * 16 + fr) * 16 + (g & 1) * 8);
  const int xcd = bid & 7, idx = bid >> 3;
  const int rgp = (idx >> 3) * 2 + (xcd & 1), c8 = idx & 7, cgl = (xcd >> 1) * 4 + (c8 & 3);
  const int ct = tid < 448 ? tid : 447, r0 = ct / 28, cc = ct - r0 * 28;
  sched_fence();
  bar();
  sched_fence();
  u32x4 wv[8];
  {
    const u16* p = (c8 < 4 ? wg : wu) + (long)(rgp * 128 + r0) * DF_ + cgl * 224 + cc * 8;
#pragma unroll
    for (int i = 0; i < 8; ++i) wv[i] = *(const u32x4*)(p + (long)i * 16 * DF_);
  }
  sched_fence();
  {
    f32x4 s = ((v[0] + v[1]) + (v[2] + v[3])) + ((v[4] + v[5]) + (v[6] + v[7]));
#pragma unroll
    for (int r = 0; r < 4; ++r) s[r] += __shfl_xor(s[r], 1);
    const int it = tid >> 1, blk = it >> 6, l2 = it & 63;
    const int e = (blk & 1) * 16 + (l2 & 15), d = (blk >> 1) * 16 + (l2 >> 4) * 4 + (tid & 1) * 2;
    *(unsigned*)(kvT + e * 40 + d) = (tid & 1) ? pk2(s[2], s[3]) : pk2(s[0], s[1]);
  }
  if (wid == 0) {
    f32x4 z = z0 + z1;
#pragma unroll
    for (int r = 0; r < 4; ++r) { z[r] += __shfl_xor(z[r], 8); z[r] += __shfl_xor(z[r], 16); z[r] += __shfl_xor(z[r], 32); }
    if (lane < 8) { u32x2 w; w.x = pk2(z[0], z[1]); w.y = pk2(z[2], z[3]); *(u32x2*)(kvT + 32 * 40 + lane * 4) = w; }
  }
  STAMP(1, 1);
  lbar();
  bf16x8 fa[3];
#pragma unroll
  for (int t = 0; t < 2; ++t) fa[t] = *(const bf16x8*)(kvT + (t * 16 + fr) * 40 + g * 8);
  fa[2] = *(const bf16x8*)(kvT + 32 * 40 + g * 8);
  const f32x4 z4 = {0.f, 0.f, 0.f, 0.f};
#pragma unroll
  for (int m = 0; m < 2; ++m) {
    f32x4 den = mfma16(fa[2], fq[m], z4);
    float inv = 1.f / (den[0] + 1e-6f);
    const long row = (long)rb * 256 + wid * 32 + m * 16 + fr;
    u32x2 w[2];
#pragma unroll
    for (int t = 0; t < 2; ++t) {
      f32x4 n = mfma16(fa[t], fq[m], z4);
      w[t].x = pk2(n[0] * inv, n[1] * inv); w[t].y = pk2(n[2] * inv, n[3] * inv);
    }
    st16<AUXK(1)>(o, (row * D_ + head * 32 + (g & 1) * 16 + (g >> 1) * 8) * 2, xpose2(w[0], w[1]));
  }
  sched_fence();
  if (wid < 7) {
    f32x4 lo = {0.f, 0.f, 0.f, 0.f}, hi = lo;
#pragma unroll
    for (int i = 0; i < 8; ++i) {
      lo += (f32x4){bflo(wv[i][0]), bflo(wv[i][1]), bflo(wv[i][2]), bflo(wv[i][3])};
      hi += (f32x4){bfhi(wv[i][0]), bfhi(wv[i][1]), bfhi(wv[i][2]), bfhi(wv[i][3])};
    }
    *(f32x4*)(sCS + r0 * 228 + cc * 8) = (f32x4){lo[0], hi[0], lo[1], hi[1]};
    *(f32x4*)(sCS + r0 * 228 + cc * 8 + 4) = (f32x4){lo[2], hi[2], lo[3], hi[3]};
  }
  STAMP(1, 2);
  lbar();
  if (tid < 448) {
    const int c = tid >> 1, hf = tid & 1;
    float t = 0.f;
#pragma unroll
    for (int i = 0; i < 8; ++i) t += sCS[(hf * 8 + i) * 228 + c];
    t += __shfl_xor(t, 1);
    if (hf == 0) csp[(long)rgp * (2 * DF_) + (c8 < 4 ? 0 : DF_) + cgl * 224 + c] = t;
  }
  STAMP(1, 3);
}

using C2 = Cfg<128, 8, 2, 2, 4, 2, 4, 4, 64, 32, 8, 0, 0x3u>;
__global__ __launch_bounds__(C2::NTHR, 1) void k2_wo(const u16* __restrict__ o, const u16* __restrict__ wo, const u16* __restrict__ x,
                                                     u16* __restrict__ h, float* __restrict__ hstat) {
  extern __shared__ __attribute__((aligned(16))) char smem[];
  LAS char* lds = (LAS char*)smem;
  const int bid = blockIdx.x, xcd = bid & 7, idx = bid >> 3;
  const int rb = (xcd & 3) * 4 + (idx & 3), ct = (xcd >> 2) * 8 + (idx >> 2);
  STAMP(2, 0);
  u32x4 ka = karg4();
  sched_fence();
  Loop<C2> L;
  auto bsrc = [&](int k, int c) -> unsigned { return (k * D_ + c * 8) * 2; };
  L.init0(lds, D_ * 2, bsrc, 0);
  karg_wait(ka);
  L.init1(karg_ptr(ka, 0) + (long)rb * 128 * D_ * 2, karg_ptr(ka, 1) + ct * 128, 64L * D_ * 2);
  const int lane = L.lane, fr = lane & 15, g = lane >> 4, wm = L.wm, wn = L.wn;
  const long off0 = ((long)rb * 128 + wm * 64 + fr) * D_ + ct * 64 + wn * 32 + (g & 1) * 16 + (g >> 1) * 8;
  u32x4 xr[4];
  L.run<2>([&](auto) {
#pragma unroll
    for (int m = 0; m < 4; ++m) xr[m] = *(const u32x4*)(x + off0 + m * 16 * D_);
  });
  LAS float* sS = (LAS float*)lds;
#pragma unroll
  for (int m = 0; m < 4; ++m) {
    const int lr = wm * 64 + m * 16 + fr;
    float s = 0.f, q = 0.f;
    const u32x4 r = xpose2((u32x2){xr[m][0], xr[m][1]}, (u32x2){xr[m][2], xr[m][3]});
    u32x2 w[2];
#pragma unroll
    for (int j = 0; j < 2; ++j) {
      w[j].x = pk2(bflo(r[2 * j]) + L.acc[m][j][0], bfhi(r[2 * j]) + L.acc[m][j][1]);
      w[j].y = pk2(bflo(r[2 * j + 1]) + L.acc[m][j][2], bfhi(r[2 * j + 1]) + L.acc[m][j][3]);
      float f0 = bflo(w[j].x), f1 = bfhi(w[j].x), f2 = bflo(w[j].y), f3 = bfhi(w[j].y);
      s += (f0 + f1) + (f2 + f3);
      q += (f0 * f0 + f1 * f1) + (f2 * f2 + f3 * f3);
    }
    st16<AUXK(2)>(h, (off0 + m * 16 * D_) * 2, xpose2(w[0], w[1]));
    s += __shfl_xor(s, 16); s += __shfl_xor(s, 32);
    q += __shfl_xor(q, 16); q += __shfl_xor(q, 32);
    if (g == 0) *(LAS f32x2*)(sS + (wn * 128 + lr) * 2) = (f32x2){s, q};
  }
  lbar();
  if (threadIdx.x < 128) {
    const int r = threadIdx.x;
    f32x2 v = *(LAS f32x2*)(sS + r * 2) + *(LAS f32x2*)(sS + (128 + r) * 2);
    *(f32x2*)(hstat + (((long)rb * 128 + r) * 16 + ct) * 2) = v;
  }
  L.ts();
  STAMP(2, 3);
  L.tsdump<2>();
}

using C3 = Cfg<256, 28, 4, 2, 4, 7, 3, 4, 32, 32, 12, 2, 0x7Fu>;
constexpr int K3_LDS = C3::LDS_BYTES + 3072;
__global__ __launch_bounds__(C3::NTHR, 2) void k3_glu(const u16* __restrict__ h, const u16* __restrict__ wg, const u16* __restrict__ wu,
                                                      const float* __restrict__ hstat,
                                                      u16* __restrict__ act, const u16* __restrict__ wd, unsigned* __restrict__ xbuf) {
  extern __shared__ __attribute__((aligned(16))) char smem[];
  LAS char* lds = (LAS char*)smem;
  const int bid = blockIdx.x, xcd = bid & 7, idx = bid >> 3;
  const int rb = (xcd & 1) * 4 + (idx & 3), ct = (xcd >> 1) * 8 + (idx >> 2);
  const int tid = threadIdx.x;
  STAMP(3, 0);
  u32x8 ka = karg8();
  sched_fence();
  auto cmap = [](int c, bool& up) -> int {
    const int w = c >= 14, r = c - 14 * w;
    up = r >= 12 ? (r & 1) : r >= 6;
    return w * 7 + (r >= 12 ? 6 : r >= 6 ? r - 6 : r);
  };
  f32x4 hs[4];
  float cp[4];
  Loop<C3> L;
  L.ts();
  auto bsrc = [&](int k, int c) -> unsigned { return (k * DF_ + c * 8) * 2; };
  L.init0(lds, D_ * 2, bsrc, 0);
  L.ts();
  karg_wait(ka);
  L.ts();
  L.init1(karg_ptr(ka, 0) + (long)rb * 256 * D_ * 2, (L.wid >> 2 ? karg_ptr(ka, 2) : karg_ptr(ka, 1)) + ct * 224, 32L * DF_ * 2);
  const int lane = L.lane, fr = lane & 15, g = lane >> 4, wm = L.wm, wn = L.wn;
  LAS float* sC = (LAS float*)(lds + C3::LDS_BYTES);
  LAS float* sR = (LAS float*)(lds + C3::LDS_BYTES + 1024);
  const char* hsp = karg_ptr(ka, 3);
  unsigned ho = (rb * 256 + L.wid * 32) * 128 + lane * 16, co, to, sra, sca;
  f32x2 pp[4];
  float w8[8];
  {
    const int c = tid < 448 ? tid >> 1 : 0;
    bool up; const int ch = cmap(c >> 3, up);
    co = (S_ * 32 + (up ? DF_ : 0) + ct * 112 + ch * 8 + (c & 7) + (tid & 1) * (4 * 2 * DF_)) * 4;
    to = (((xcd & 1) * 4 + (xcd >> 1)) * 256 + idx * 8 + L.wid) * 3584 + (lane < 28 ? lane : 0) * 64;
    sca = (unsigned)(size_t)(sC + c);
    sra = (unsigned)(size_t)((lane & 4) ? sR + (L.wid * 32 + (lane & 3) * 8 + (lane >> 3)) * 2 : sC + 240);
    asm volatile("" : "+v"(ho), "+v"(co), "+v"(to), "+v"(sra), "+v"(sca));
  }
  L.run<3>([&](auto sx) {
    constexpr int sv = decltype(sx)::value;
    if constexpr (sv == SENTS * 100 + 12 || sv == SENTS * 100 + 26) stsent(bid * 16384 + tid * 16 + (sv == SENTS * 100 + 12 ? 0 : 8192), xbuf);
    constexpr int s0 = sv - SB * 100, su = s0 < 0 || s0 >= 500 ? -1 : s0 < 200 ? s0 + 2600 : s0 < 300 ? -1 : s0 + 2500;
    if constexpr (su >= 0) {
      if constexpr (su == 2602) ld4<0>(hs[0], ho, hsp);
      if constexpr (su == 2609) ld4<1024>(hs[1], ho, hsp);
      if constexpr (su == 2616) ld4<2048>(hs[2], ho, hsp);
      if constexpr (su == 2623) ld4<3072>(hs[3], ho, hsp);
      if constexpr (su == 2703) ld1(cp[0], co, hsp);
      if constexpr (su == 2708) ld1(cp[1], co, hsp + 1L * (2 * DF_ * 4));
      if constexpr (su == 2717) ld1(cp[2], co, hsp + 2L * (2 * DF_ * 4));
      if constexpr (su == 2722) ld1(cp[3], co, hsp + 3L * (2 * DF_ * 4));
      if constexpr (su == 2800) asm volatile("s_waitcnt vmcnt(8)" : "+v"(hs[0]), "+v"(hs[1]), "+v"(hs[2]), "+v"(hs[3]) :: "memory");
      if constexpr (su >= 2800 && su < 2804) {
        constexpr int i = su - 2800;
        pp[i] = (f32x2){hs[i][0], hs[i][1]} + (f32x2){hs[i][2], hs[i][3]};
        asm volatile("" : "+v"(pp[i]));
      }
      if constexpr (su >= 2804 && su < 2812) {
        constexpr int i = (su - 2804) >> 2, e = (su - 2804) & 1, gv = ((su - 2804) >> 1) & 1;
        w8[su - 2804] = fsel(lane & 1, pp[2 * i + 1 - gv][e], pp[2 * i + gv][e]);
        asm volatile("" : "+v"(w8[su - 2804]));
      }
      if constexpr (su >= 2812 && su < 2816) {
        constexpr int i = (su - 2812) >> 1, e = (su - 2812) & 1;
        pp[i][e] = w8[4 * i + e] + dpp<0xB1>(w8[4 * i + 2 + e]);
        asm volatile("" : "+v"(pp[i]));
      }
      if constexpr (su >= 2816 && su < 2820) {
        constexpr int e = (su - 2816) & 1, gv = (su - 2816) >> 1;
        w8[su - 2816] = fsel(lane & 2, pp[1 - gv][e], pp[gv][e]);
        asm volatile("" : "+v"(w8[su - 2816]));
      }
      if constexpr (su == 2820 || su == 2821) { w8[su - 2816] = w8[su - 2820] + dpp<0x4E>(w8[su - 2818]); asm volatile("" : "+v"(w8[su - 2816])); }
      if constexpr (su == 2823 || su == 2824) {
        w8[su - 2817] = (w8[su - 2819] + dpp<0x124>(w8[su - 2819])) * (1.f / D_); asm volatile("" : "+v"(w8[su - 2817]));
      }
      if constexpr (su == 2826) { w8[7] = fmaxf(w8[7] - w8[6] * w8[6], 0.f); asm volatile("" : "+v"(w8[7])); }
      if constexpr (su == 2900) *(LAS f32x2*)(size_t)sra = (f32x2){-w8[6], w8[7] + 1e-5f};
      if constexpr (su == 2902) {
        asm volatile("s_waitcnt vmcnt(8)" : "+v"(cp[0]), "+v"(cp[1]), "+v"(cp[2]), "+v"(cp[3]) :: "memory");
        cp[0] += cp[1]; asm volatile("" : "+v"(cp[0]));
      }
      if constexpr (su == 2903) { cp[2] += cp[3]; asm volatile("" : "+v"(cp[2])); }
      if constexpr (su == 2904) { cp[0] += cp[2]; asm volatile("" : "+v"(cp[0])); }
      if constexpr (su == 2906) { cp[0] += dpp<0xB1>(cp[0]); asm volatile("" : "+v"(cp[0])); }
      if constexpr (su == 2908) *(LAS float*)(size_t)sca = cp[0];
    }
    if constexpr (sv == TCH * 100) touchv(to, wd);
    if constexpr (sv == TCH2 * 100 + 1) touchv(to + 1792, wd);
  });
  static_assert(C3::XS == 28, "K3 schedule");
  {
    float nm[4];
#pragma unroll
    for (int m = 0; m < 4; ++m) nm[m] = sR[(wm * 64 + m * 16 + fr) * 2];
#pragma unroll
    for (int j = 0; j < 7; ++j) {
      const f32x4 c4 = *(const LAS f32x4*)(sC + (wn * 14 + 2 * j) * 8 + 4 * g);
#pragma unroll
      for (int m = 0; m < 4; ++m) L.acc[m][j] += c4 * nm[m];
    }
  }
  const int hi = lane >> 5;
  const int apo = ((rb * 2 + (wm >> 1)) * 448 + ct * 14 + wn * 7) * 2048 + ((wm & 1) * 64 + fr) * 16;
  constexpr float K1c = -2.f * 0.7978845608028654f * 1.4426950408889634f, K2c = K1c * 0.044715f;
  auto glu = [&](f32x4 a, f32x4 b, float q, float r1, float r3) -> u32x2 {
    const f32x4 z = a * ((a * a) * r3 + r1);
    f32x4 e;
#pragma unroll
    for (int r = 0; r < 4; ++r) e[r] = __builtin_amdgcn_exp2f(z[r]);
    e = e * q + q;
#pragma unroll
    for (int r = 0; r < 4; ++r) e[r] = __builtin_amdgcn_rcpf(e[r]);
    const f32x4 o = (a * b) * e;
    u32x2 w; w.x = pk2(o[0], o[1]); w.y = pk2(o[2], o[3]);
    return w;
  };
  u32x2 w6[2];
#pragma unroll
  for (int mp = 0; mp < 2; ++mp) {
    float q[2], r1[2], r3[2];
#pragma unroll
    for (int mm = 0; mm < 2; ++mm) {
      q[mm] = sR[(wm * 64 + (2 * mp + mm) * 16 + fr) * 2 + 1];
      const float rstd = __builtin_amdgcn_rsqf(q[mm]);
      r1[mm] = K1c * rstd; r3[mm] = K2c * rstd * rstd * rstd;
    }
#pragma unroll
    for (int j = 0; j < 3; ++j) {
      const u32x2 wa = glu(L.acc[2 * mp][j], L.acc[2 * mp][3 + j], q[0], r1[0], r3[0]);
      const u32x2 wb = glu(L.acc[2 * mp + 1][j], L.acc[2 * mp + 1][3 + j], q[1], r1[1], r3[1]);
      st16<AUXK(3)>(act, apo + (2 * j + (g >> 1)) * 2048 + (2 * mp + (g & 1)) * 256, xpose2(wa, wb));
    }
    f32x4 G, U;
#pragma unroll
    for (int r = 0; r < 4; ++r) {
      auto sw = __builtin_amdgcn_permlane32_swap(__float_as_uint(L.acc[2 * mp][6][r]), __float_as_uint(L.acc[2 * mp + 1][6][r]), false, false);
      G[r] = __uint_as_float(sw[0]); U[r] = __uint_as_float(sw[1]);
    }
    w6[mp] = glu(G, U, hi ? q[1] : q[0], hi ? r1[1] : r1[0], hi ? r3[1] : r3[0]);
  }
  st16<AUXK(3)>(act, apo + 6 * 2048 + (2 * (g & 1) + (g >> 1)) * 256, xpose2(w6[0], w6[1]));
  wait_vm<0>();
  L.ts();
  STAMP(3, 3);
  L.tsdump<3>();
}

using C4 = Cfg<128, 16, 2, 4, 4, 2, 4, 4, 64, 56, 8, 3, 0x3u>;
__global__ __launch_bounds__(C4::NTHR, 2) void k4_down(const u16* __restrict__ act, const u16* __restrict__ wd, const u16* __restrict__ h,
                                                       u16* __restrict__ out, unsigned* __restrict__ xbuf) {
  extern __shared__ __attribute__((aligned(16))) char smem[];
  LAS char* lds = (LAS char*)smem;
  const int bid = blockIdx.x, xcd = bid & 7, idx = bid >> 3;
  const int kh = xcd & 1, rb = (xcd >> 1) * 4 + (idx & 3), cb = idx >> 2;
  STAMP(4, 0);
  u32x4 ka = karg4();
  sched_fence();
  Loop<C4> L;
  auto bsrc = [&](int k, int c) -> unsigned { return (k * D_ + c * 8) * 2; };
  L.init0(lds, DF_ * 2, bsrc, 2 * kh);
  karg_wait(ka);
  L.init1(karg_ptr(ka, 0) + (long)(rb * 448 + kh * 224) * 2048, karg_ptr(ka, 1) + ((long)kh * 1792 * D_ + cb * 128) * 2, 64L * D_ * 2);
  const int lane = L.lane, fr = lane & 15, g = lane >> 4, wm = L.wm, wn = L.wn;
  const long off0 = ((long)rb * 128 + wm * 64 + kh * 32 + fr) * D_ + cb * 128 + wn * 32 + (g & 1) * 16 + (g >> 1) * 8;
  u32x4 xr[2];
  L.run<4>([&](auto) {
#pragma unroll
    for (int m = 0; m < 2; ++m) xr[m] = *(const u32x4*)(h + off0 + m * 16 * D_);
  });
  const __amdgpu_buffer_rsrc_t rx = __builtin_amdgcn_make_buffer_rsrc((void*)xbuf, 0, XBUF_BYTES, 0x00020000);
  const int xo = L.wid * 2048 + lane * 16;
#pragma unroll
  for (int i = 0; i < 2; ++i) {
    const f32x4 a = L.acc[2 + i][0], b = L.acc[2 + i][1];
    const u32x4 v = {pk2(a[0], a[1]), pk2(a[2], a[3]), pk2(b[0], b[1]), pk2(b[2], b[3])};
    __builtin_amdgcn_raw_buffer_store_b128(v, rx, bid * 16384 + xo + i * 1024, 0, 16);
  }
  u32x4 rv[2];
  for (int spin = 0; spin < (1 << 22); ++spin) {
    asm volatile("" ::: "memory");
    bool ok = true;
#pragma unroll
    for (int i = 0; i < 2; ++i) rv[i] = __builtin_amdgcn_raw_buffer_load_b128(rx, (bid ^ 1) * 16384 + xo + i * 1024, 0, 16);
#pragma unroll
    for (int i = 0; i < 2; ++i) ok = ok && rv[i][0] != SENT && rv[i][1] != SENT && rv[i][2] != SENT && rv[i][3] != SENT;
    if (__all(ok)) break;
  }
#pragma unroll
  for (int m = 0; m < 2; ++m) {
    const u32x4 r = xpose2((u32x2){xr[m][0], xr[m][1]}, (u32x2){xr[m][2], xr[m][3]});
    u32x2 w[2];
#pragma unroll
    for (int j = 0; j < 2; ++j) {
      const f32x4 a = L.acc[m][j];
      w[j].x = pk2(bflo(r[2 * j]) + (a[0] + bflo(rv[m][2 * j])), bfhi(r[2 * j]) + (a[1] + bfhi(rv[m][2 * j])));
      w[j].y = pk2(bflo(r[2 * j + 1]) + (a[2] + bflo(rv[m][2 * j + 1])), bfhi(r[2 * j + 1]) + (a[3] + bfhi(rv[m][2 * j + 1])));
    }
    st16<AUXK(4)>(out, (off0 + m * 16 * D_) * 2, xpose2(w[0], w[1]));
  }
  L.ts();
  STAMP(4, 3);
  L.tsdump<4>();
}

extern "C" void kernel_launch(void* const* d_in, const int* in_sizes, int n_in, void* d_out, int out_size, void* d_ws,
                              size_t ws_size, hipStream_t stream) {
  const u16* x = (const u16*)d_in[0];
  const u16* wqkv = (const u16*)d_in[1];
  const u16* wo = (const u16*)d_in[2];
  const u16* wg = (const u16*)d_in[4];
  const u16* wu = (const u16*)d_in[5];
  const u16* wd = (const u16*)d_in[6];
  u16* out = (u16*)d_out;
  char* ws = (char*)d_ws;
  u16* pq = (u16*)ws;                      ws += (size_t)S_ * D_ * 2;
  u16* o = (u16*)ws;                       ws += (size_t)S_ * D_ * 2;
  u16* h = (u16*)ws;                       ws += (size_t)S_ * D_ * 2;
  u16* act = (u16*)ws;                     ws += (size_t)S_ * DF_ * 2;
  float* kvp = (float*)ws;                 ws += (size_t)NH_ * 16 * KVP * 4;
  float* hstat = (float*)ws;               ws += (size_t)S_ * 16 * 2 * 4;
  float* csp = (float*)ws;                 ws += (size_t)8 * 2 * DF_ * 4;
  unsigned* xbuf = (unsigned*)ws;          ws += (size_t)XBUF_BYTES;
  (void)hipFuncSetAttribute((const void*)k1_qkv, hipFuncAttributeMaxDynamicSharedMemorySize, C1::LDS_BYTES);
  (void)hipFuncSetAttribute((const void*)k2_wo, hipFuncAttributeMaxDynamicSharedMemorySize, C2::LDS_BYTES);
  (void)hipFuncSetAttribute((const void*)k3_glu, hipFuncAttributeMaxDynamicSharedMemorySize, K3_LDS);
  (void)hipFuncSetAttribute((const void*)k4_down, hipFuncAttributeMaxDynamicSharedMemorySize, C4::LDS_BYTES);
  k1_qkv<<<256, C1::NTHR, C1::LDS_BYTES, stream>>>(x, wqkv, pq, kvp, wo, wg);
  k15_o<<<256, 512, 0, stream>>>(pq, kvp, o, wg, wu, csp);
  k2_wo<<<256, C2::NTHR, C2::LDS_BYTES, stream>>>(o, wo, x, h, hstat);
  k3_glu<<<256, C3::NTHR, K3_LDS, stream>>>(h, wg, wu, hstat, act, wd, xbuf);
  k4_down<<<256, C4::NTHR, C4::LDS_BYTES, stream>>>(act, wd, h, out, xbuf);
}
```

```cpp
#include <hip/hip_runtime.h>
#include <hip/hip_bf16.h>
#include <stdint.h>

typedef unsigned short u16;
typedef short bf16x8 __attribute__((ext_vector_type(8)));
typedef short s16x4 __attribute__((ext_vector_type(4)));
typedef float f32x4 __attribute__((ext_vector_type(4)));
typedef float f32x2 __attribute__((ext_vector_type(2)));
typedef unsigned u32x4 __attribute__((ext_vector_type(4)));
typedef unsigned u32x2 __attribute__((ext_vector_type(2)));
typedef __bf16 bf16x2_t __attribute__((ext_vector_type(2)));
#define LAS __attribute__((address_space(3)))
#define DEV __device__ __forceinline__
template <int V> struct IC { static constexpr int value = V; };

constexpr int S_ = 2048, D_ = 1024, DF_ = 3584, NH_ = 32;
constexpr int KVP = 1056;
constexpr unsigned SENT = 0xFFFFFFFFu;
constexpr int XBUF_BYTES = 256 * 16384;

DEV float bflo(unsigned v) { return __uint_as_float(v << 16); }
DEV float bfhi(unsigned v) { return __uint_as_float(v & 0xffff0000u); }
DEV unsigned pk2(float lo, float hi) {
  bf16x2_t r; r.x = (__bf16)lo; r.y = (__bf16)hi; return __builtin_bit_cast(unsigned, r);
}
template <int N> DEV void wait_vm() { asm volatile("s_waitcnt vmcnt(%0)" ::"n"(N) : "memory"); }
DEV void wait_lgkm0() { __builtin_amdgcn_s_waitcnt(0xC07F); }
DEV void bar() { __builtin_amdgcn_s_barrier(); }
DEV void lbar() { wait_lgkm0(); bar(); asm volatile("" ::: "memory"); }
DEV void sched_fence() { __builtin_amdgcn_sched_barrier(0); }
DEV void glds_v(const void* g, unsigned lds_addr) {
  asm volatile("s_mov_b32 m0, %1\n\ts_nop 0\n\tglobal_load_lds_dwordx4 %0, off" ::"v"(g), "s"(lds_addr) : "memory");
}
DEV void glds_vm(const void* g, unsigned lds_addr, unsigned long long mask) {
  asm volatile("s_mov_b32 m0, %1\n\ts_mov_b64 exec, %2\n\tglobal_load_lds_dwordx4 %0, off\n\ts_mov_b64 exec, -1"
               ::"v"(g), "s"(lds_addr), "s"(mask) : "memory");
}
DEV void glds_s(unsigned voff, const void* sbase, unsigned lds_addr) {
  asm volatile("s_mov_b32 m0, %2\n\ts_nop 0\n\tglobal_load_lds_dwordx4 %0, %1" ::"v"(voff), "s"(sbase), "s"(lds_addr) : "memory");
}
DEV void glds_sm(unsigned voff, const void* sbase, unsigned lds_addr, unsigned long long mask) {
  asm volatile("s_mov_b32 m0, %2\n\ts_mov_b64 exec, %3\n\tglobal_load_lds_dwordx4 %0, %1\n\ts_mov_b64 exec, -1"
               ::"v"(voff), "s"(sbase), "s"(lds_addr), "s"(mask) : "memory");
}
DEV void touch(const void* g, unsigned lds_addr) {
  asm volatile("s_mov_b32 m0, %1\n\ts_nop 0\n\tglobal_load_lds_dword %0, off" ::"v"(g), "s"(lds_addr) : "memory");
}
DEV void touchv(unsigned voff, const void* sbase) {
  asm volatile("global_load_dword v255, %0, %1" :: "v"(voff), "s"(sbase) : "memory", "v255");
}
typedef unsigned u32x8 __attribute__((ext_vector_type(8)));
DEV u32x4 karg4() { u32x4 r; asm volatile("s_load_dwordx4 %0, %1, 0x0" : "=s"(r) : "s"(__builtin_amdgcn_kernarg_segment_ptr())); return r; }
DEV u32x8 karg8() { u32x8 r; asm volatile("s_load_dwordx8 %0, %1, 0x0" : "=s"(r) : "s"(__builtin_amdgcn_kernarg_segment_ptr())); return r; }
template <class T> DEV void karg_wait(T& r) { asm volatile("s_waitcnt lgkmcnt(0)" : "+s"(r)); }
template <class T> DEV const char* karg_ptr(const T& r, int i) { return (const char*)(((unsigned long long)r[2 * i + 1] << 32) | r[2 * i]); }
template <int OFF> DEV void ld4(f32x4& d, unsigned voff, const void* sbase) {
  asm volatile("global_load_dwordx4 %0, %1, %2 offset:%3" : "=v"(d) : "v"(voff), "s"(sbase), "n"(OFF) : "memory");
}
DEV void ld1(float& d, unsigned voff, const void* sbase) {
  asm volatile("global_load_dword %0, %1, %2" : "=v"(d) : "v"(voff), "s"(sbase) : "memory");
}
template <int CTRL> DEV float dpp(float v) { return __int_as_float(__builtin_amdgcn_update_dpp(0, __float_as_int(v), CTRL, 0xF, 0xF, true)); }
#define SENTSC ""
DEV void stsent(unsigned voff, const void* sbase) {
  const u32x4 sv = {SENT, SENT, SENT, SENT};
  asm volatile("global_store_dwordx4 %0, %1, %2 " SENTSC "\n\ts_nop 1" :: "v"(voff), "v"(sv), "s"(sbase) : "memory");
}
DEV float fsel(bool c, float a, float b) { return c ? a : b; }
DEV f32x4 mfma16(bf16x8 a, bf16x8 b, f32x4 c) { return __builtin_amdgcn_mfma_f32_16x16x32_bf16(a, b, c, 0, 0, 0); }
DEV void mfma_acc(f32x4& c, bf16x8 a, bf16x8 b) {
  asm volatile("v_mfma_f32_16x16x32_bf16 %0, %1, %2, %0" : "+v"(c) : "v"(a), "v"(b));
}
DEV void mfma_z(f32x4& c, bf16x8 a, bf16x8 b) {
  asm volatile("v_mfma_f32_16x16x32_bf16 %0, %1, %2, 0" : "=&v"(c) : "v"(a), "v"(b));
}
template <bool Z> DEV void mfma_a(f32x4& c, bf16x8 a, bf16x8 b) { if constexpr (Z) mfma_z(c, a, b); else mfma_acc(c, a, b); }
DEV bf16x8 cat(s16x4 a, s16x4 b) { return __builtin_shufflevector(a, b, 0, 1, 2, 3, 4, 5, 6, 7); }
DEV s16x4 rdtr(unsigned a) { return __builtin_amdgcn_ds_read_tr16_b64_v4i16((LAS s16x4*)(size_t)a); }

#define FINEW 0
#define STAMP(kid, i)
#define SENTS 12
#define SB 5
#define TCH 28
#define TCH2 30
template <int BM_, int NC_, int WM_, int WN_, int MT_, int NT_, int RA_, int RB_, int KB_, int NS_, int P_, int KIND_, unsigned TRANS_>
struct Cfg {
  static constexpr int BM = BM_, NC = NC_, WM = WM_, WN = WN_, MT = MT_, NT = NT_, RA = RA_, RB = RB_, KB = KB_, NS = NS_, P = P_;
  static constexpr int KIND = KIND_;
  static constexpr unsigned TRANS = TRANS_;
  static constexpr bool STATS = KIND == 1, PERM = KIND == 1 || KIND == 3;
  static constexpr bool AFRAG = KIND == 3;
  static constexpr int NW = WM * WN, NTHR = NW * 64;
  static constexpr int ATILE = BM * 128, BSLOT = KIND_ == 2 ? 15360 : KB * NC * 16;
  static constexpr int BHF = KIND_ == 2 ? 4 * 224 : 4 * NC * 16;
  static constexpr int NA = BM * 8 / NTHR;
  static constexpr int CPW = KB * NC / NW;
  static constexpr int NB = (CPW + 63) / 64;
  static constexpr int NA1 = KB == 64 ? NA / 2 : (NA + 1) / 2, NB1 = KB == 64 ? (NB + 1) / 2 : NB;
  static constexpr int LDS_B0 = RA * ATILE, LDS_BYTES = RA * ATILE + RB * BSLOT;
  static constexpr int NTA = NS / 2;
#define RQN 7
#define RQD 8
  static constexpr int RQ1 = (MT * NT * RQN) / RQD;
  static_assert(BM * 8 % NTHR == 0, "A tile split");
  static_assert((KB * NC) % NW == 0, "B slot split");
  static_assert(P % 2 == 0 && (P / 2) % RA == 0 && (KB == 32 ? P % RB == 0 : (P / 2) % RB == 0), "period");
  static_assert(BM == WM * MT * 16, "M tiling");
  static_assert(NC * 8 == WN * NT * 16, "N tiling");
  DEV static constexpr int bswz(int k) {
    return NC == 28 ? 2 * ((k >> 3) & 1)
         : (NC % 16 == 8) ? 2 * (((k >> 1) & 1) | (((k >> 3) & 1) << 1))
                          : 2 * ((k & 3) | (((k >> 3) & 1) << 2));
  }
  static constexpr bool hasB(int t) {
    if (KB == 32) return t + RB >= 0 && t + RB < NS;
    int tile = (t & 1) ? ((t - 1) >> 1) + RB : ((t - 2) >> 1) + RB;
    return tile >= 0 && tile < NTA;
  }
  static constexpr bool hasA(int t) {
    int tile = (t & 1) ? ((t - 1) >> 1) + RA : ((t - 2) >> 1) + RA;
    return tile >= 0 && tile < NTA;
  }
  static constexpr int nB(int t) { return !hasB(t) ? 0 : KB == 32 ? NB : (t & 1) ? NB1 : NB - NB1; }
  static constexpr int nA(int t) { return !hasA(t) ? 0 : (t & 1) ? NA1 : NA - NA1; }
  static constexpr int T0 = -(2 * (RA > RB ? RA : RB) + 2);
  static constexpr int lastIssue() { int l = 0; for (int t = 0; t < NS; ++t) if (nA(t) + nB(t) > 0) l = t; return l; }
  static constexpr int XS = lastIssue() + 1;
  static constexpr int nX(int t) {
    if (KIND == 0) return t == XS ? 4 : 0;
    if (KIND == 1) return t == XS ? 2 : 0;
    if (KIND == 2) return (t == TCH ? 1 : 0) + (t == TCH2 ? 1 : 0) + ((t == SB || t == SB + 1) ? 4 : 0) + (t == SENTS ? 2 : 0);
    return t == XS ? 2 : 0;
  }
  static constexpr int hk(int t) { return KIND == 2 && ((t >= SB && t <= SB + 4) || t == SENTS || t == TCH || t == TCH2) ? 2 : nX(t) > 0 ? 1 : 0; }
  static constexpr int nXP(int t) { return KIND == 1 && t == 0 ? 1 : 0; }
  static constexpr int TPRE = KB == 32 ? -3 : -4, RATE = KB == 32 ? 2 : 4;
  static constexpr int vlast(int s) { int v = TPRE + RATE * (s + 1); return s < 0 ? TPRE : (v < s ? v : s); }
  static constexpr int nhead() { int s = 0; while (vlast(s - 1) != s - 1) ++s; return s; }
  static constexpr int H = nhead();
  static constexpr int TF = KB == 32 ? (-RB > -2 * RA + 2 ? -RB : -2 * RA + 2) : -2 * (RA < RB ? RA : RB) + 2;
  static constexpr int nfirst() { int n = 0; for (int t = T0; t <= TF; ++t) n += nA(t) + nB(t); return n; }
  static constexpr int npro() { int n = 0; for (int t = T0; t <= TPRE; ++t) n += nA(t) + nB(t); return n; }
  static constexpr int nmain() {
    if (KIND == 2) return H;
    int lim = 0;
    while (lim < NS && hasA(lim) && hasB(lim)) ++lim;
    return H + ((lim - H) / P) * P;
  }
  static constexpr int vm_at(int s) {
    int posA[64] = {}, posB[128] = {}, cntA[64] = {}, cntB[128] = {};
    int tot = 0;
    for (int r = -1; r < s || r < 0; ++r) {
      if (r >= 0) tot += nX(r);
      for (int t = (r < 0 ? T0 : vlast(r - 1) + 1); t <= vlast(r); ++t) {
        const int tl = (t & 1) ? ((t - 1) >> 1) : ((t - 2) >> 1);
        if (nB(t)) { tot += nB(t); const int i = KB == 32 ? t + RB : tl + RB; posB[i] = tot; cntB[i] += nB(t); }
        if (nA(t)) { tot += nA(t); posA[tl + RA] = tot; cntA[tl + RA] += nA(t); }
      }
      if (r >= 0) tot += nXP(r);
    }
    int nb = s + 1;
    if (nb >= NS) return 63;
    const int ib = KB == 32 ? nb : nb / 2;
    if (cntB[ib] != NB || cntA[nb / 2] != NA) return -1;
    int need = posB[ib] > posA[nb / 2] ? posB[ib] : posA[nb / 2];
    int v = tot - need;
    return v > 63 ? 63 : v;
  }
  static constexpr int NMAIN = nmain();
  static constexpr int vm_main(int u) {
    int v = 63;
    for (int s = H + u; s < NMAIN; s += P) { int x = vm_at(s); v = x < v ? x : v; }
    return v;
  }
};

template <class C> struct Loop {
  unsigned lbase;
  const char* abase;
  unsigned ga[C::NA];
  unsigned gbo[C::NB];
  const char* bbase;
  unsigned bdst;
  long bstep;
  int wid, lane, wm, wn;
  unsigned ra[2][C::PERM ? C::MT : 1];
  unsigned rb[C::NT];
  bf16x8 FA[2][C::MT];
  s16x4 FBh[2][C::NT][2];
  f32x4 acc[C::MT][C::NT];
  f32x4 rs, rq;
  f32x4 cs[3];
  bf16x8 ones;

  template <int SET, int I> DEV void readOp(int aslot, int ks, int boff) {
    if constexpr (I < C::MT) {
      unsigned a = C::PERM ? ra[ks][C::PERM ? I : 0] : ra[ks][0] + I * 2048;
      FA[SET][I] = *(const LAS bf16x8*)(size_t)(a + aslot * C::ATILE);
    } else {
      constexpr int j = (I - C::MT) / 2, hf = (I - C::MT) & 1;
      FBh[SET][j][hf] = rdtr(rb[j] + boff + hf * C::BHF);
    }
  }
  static constexpr int rord(int i) {
    if (i == 0) return 0;
    if (i <= 2 * C::NT) return C::MT + i - 1;
    return i - 2 * C::NT;
  }
  template <int SET> DEV bf16x8 FB(int j) const { return cat(FBh[SET][j][0], FBh[SET][j][1]); }
  static constexpr int NMM = C::MT * C::NT, NR = C::MT + 2 * C::NT;
  static constexpr int nm(int U, int CP) { return NMM + (C::STATS ? ((U & 1) == CP ? 5 : 2) : 0); }
  template <int SET, int Q, bool Z> DEV void mmaOp() {
    if constexpr (Q < NMM) {
      constexpr int m = Q / C::NT, j = Q % C::NT;
      if constexpr ((C::TRANS >> j) & 1) mfma_a<Z>(acc[m][j], FB<SET>(j), FA[SET][m]);
      else mfma_a<Z>(acc[m][j], FA[SET][m], FB<SET>(j));
    } else if constexpr (Q == NMM) {
      mfma_a<Z>(rs, ones, FA[SET][0]);
    } else if constexpr (Q == NMM + 1) {
      mfma_a<Z>(rq, FA[SET][0], FA[SET][0]);
    } else {
      mfma_acc(cs[Q - NMM - 2], ones, FB<SET>(Q - NMM - 2));
    }
  }
  template <int PAR, bool IB, int I> DEV void gldsOp(int bslot, int aslot) {
    constexpr int nb = !IB ? 0 : C::KB == 32 ? C::NB : PAR ? C::NB1 : C::NB - C::NB1;
    if constexpr (I < nb) {
      constexpr int i = (C::KB == 64 && !PAR) ? C::NB1 + I : I;
      constexpr int rem = C::CPW - 64 * i;
      const unsigned dst = bdst + bslot * C::BSLOT + i * 1024;
      if constexpr (rem >= 64) glds_s(gbo[i], bbase, dst);
      else glds_sm(gbo[i], bbase, dst, (1ull << (rem & 63)) - 1ull);
      if constexpr (i == C::NB - 1) { bbase += bstep; asm volatile("" : "+s"(bbase)); }
    } else {
      constexpr int i = (PAR ? 0 : C::NA1) + (I - nb);
      glds_s(ga[i], abase, lbase + aslot * C::ATILE + (wid * C::NA + i) * 1024);
      if constexpr (i == C::NA - 1) { abase += C::AFRAG ? C::ATILE : 128; asm volatile("" : "+s"(abase)); }
    }
  }
  static constexpr int spread(int N, int Q0, int Q1, int Q) { return Q <= Q0 ? 0 : Q >= Q1 ? N : (N * (Q - Q0) + (Q1 - Q0) - 1) / (Q1 - Q0); }
  template <int SET, int LO, int HI> DEV void readRange(int aslot, int ks, int boff) {
    if constexpr (LO < HI) { readOp<SET, rord(LO)>(aslot, ks, boff); readRange<SET, LO + 1, HI>(aslot, ks, boff); }
  }
  static constexpr int wslotA(int U) { return (((U & 1) ? (U - 1) / 2 : (U - 2 + 2 * C::P) / 2)) % C::RA; }
  static constexpr int wslotB(int U) { return C::KB == 32 ? U % C::RB : (((U & 1) ? (U - 1) / 2 : (U - 2 + 2 * C::P) / 2)) % C::RB; }
  static constexpr int rslotA(int U1) { return (U1 / 2) % C::RA; }
  static constexpr int roffB(int U1) { return C::KB == 32 ? (U1 % C::RB) * C::BSLOT : ((U1 / 2) % C::RB) * C::BSLOT + (U1 & 1) * 32 * C::NC * 16; }
  static constexpr int nops(int V, int VHI, bool MAIN) {
    int n = 0;
    for (int v = V; v <= VHI; ++v)
      n += MAIN ? ((C::KB == 32 ? C::NB : (v & 1) ? C::NB1 : C::NB - C::NB1) + ((v & 1) ? C::NA1 : C::NA - C::NA1)) : C::nB(v) + C::nA(v);
    return n;
  }
  template <int V, int VHI, bool MAIN, int I> DEV void vop() {
    if constexpr (V <= VHI) {
      constexpr int n = nops(V, V, MAIN), U = ((V % C::P) + C::P) % C::P;
      if constexpr (I < n) gldsOp<U & 1, MAIN || C::hasB(V), I>(wslotB(U), wslotA(U));
      else vop<V + 1, VHI, MAIN, I - n>();
    }
  }
  template <int V, int VHI, bool MAIN, int LO, int HI> DEV void vopRange() {
    if constexpr (LO < HI) { vop<V, VHI, MAIN, LO>(); vopRange<V, VHI, MAIN, LO + 1, HI>(); }
  }
  template <int CP, int U, int V, int VHI, bool MAIN, bool RN, int Q, int HS, class H> DEV void body(H& hook) {
    constexpr int NM = nm(U, CP);
    if constexpr (Q < NM) {
      constexpr int PAR = U & 1;
      constexpr int NG = nops(V, VHI, MAIN);
      constexpr int RQ = C::RQ1 + (NM - NMM);
      mmaOp<PAR, Q, !MAIN && V == C::TPRE + 1>();
      vopRange<V, VHI, MAIN, spread(NG, 0, NM, Q), spread(NG, 0, NM, Q + 1)>();
      if constexpr (RN) readRange<PAR ^ 1, spread(NR, 0, RQ, Q), spread(NR, 0, RQ, Q + 1)>(rslotA(U + 1), PAR ^ 1, roffB(U + 1));
      if constexpr (HS >= 0) hook(IC<HS + Q>{});
      sched_fence();
      body<CP, U, V, VHI, MAIN, RN, Q + 1, HS>(hook);
    }
  }
  template <int CP, int S, int VM, bool MAIN, class H> DEV void step(H& hook) {
    constexpr int U = S % C::P;
    constexpr bool RN = MAIN || S + 1 < C::NS;
    if constexpr (RN && (C::KB == 32 || (U & 1))) {
      static_assert(VM >= 0, "schedule"); wait_vm<VM>(); bar();
    }
    sched_fence();
    constexpr int HM = MAIN ? 0 : C::hk(S);
    if constexpr (HM == 1) { hook(IC<S>{}); sched_fence(); }
    if constexpr (HM == 3) { if (wid >= C::NW / 2) hook(IC<S>{}); sched_fence(); }
    body<CP, U, C::vlast(S - 1) + 1, C::vlast(S), MAIN, RN, 0, HM == 2 ? S * 100 : -1>(hook);
    if constexpr (HM == 3) { if (wid < C::NW / 2) hook(IC<S>{}); sched_fence(); }
    if constexpr (C::nXP(S) > 0) { hook(IC<MAIN ? 2000 : 1000>{}); sched_fence(); }
    if constexpr (C::KB == 32 || !(U & 1)) wait_lgkm0();
    sched_fence();
  }
  template <int CP, int U, class H> DEV void mainSteps(H& hook) {
    if constexpr (U < C::P) { step<CP, C::H + U, C::vm_main(U), true>(hook); mainSteps<CP, U + 1>(hook); }
  }
  template <int CP, int Sx, int SE, class H> DEV void seqSteps(H& hook) {
    if constexpr (Sx < SE) { step<CP, Sx, C::vm_at(Sx), false>(hook); seqSteps<CP, Sx + 1, SE>(hook); }
  }
  template <int KID, int CP, class H> DEV void allSteps(H& hook) {
    pinAcc();
    sched_fence();
    ts();
    wait_vm<C::vm_at(-1)>();
    ts();
    bar();
    sched_fence();
    ts();
    readRange<0, 0, NR>(0, 0, 0);
    wait_lgkm0();
    sched_fence();
    STAMP(KID, 1);
    ts();
    pinAcc();
    asm volatile("s_nop 7" ::: "memory");
    sched_fence();
    seqSteps<CP, 0, C::H>(hook);
    for (int it = 0; it < (C::NMAIN - C::H) / C::P; ++it) mainSteps<CP, 0>(hook);
    seqSteps<CP, C::NMAIN, C::NS>(hook);
    asm volatile("s_nop 7\n\ts_nop 7\n\ts_nop 7" ::: "memory");
    sched_fence();
  }
  DEV void ts() {
  }
  template <int KID> DEV void tsdump() {
  }
  template <class BF> DEV void init0(LAS char* l, int lda, BF bsrc, int mperm) {
    lbase = (unsigned)(size_t)l;
    const int tid = threadIdx.x;
    wid = __builtin_amdgcn_readfirstlane(tid >> 6); lane = tid & 63;
    wm = wid / C::WN; wn = wid % C::WN;
    if (mperm < 0) mperm = wn;
#pragma unroll
    for (int i = 0; i < C::NA; ++i) {
      int q = (wid * C::NA + i) * 64 + lane, row = q >> 3, p = q & 7, c = p ^ ((row >> 1) & 7);
      ga[i] = C::AFRAG ? q * 16 : row * lda + c * 16;
      asm volatile("" : "+v"(ga[i]));
    }
#pragma unroll
    for (int i = 0; i < C::NB; ++i) {
      if constexpr (C::KIND == 2) {
        int q = i * 64 + lane;
        if (q >= 112) q = 111;
        const int k = q / 14, c = q - k * 14;
        gbo[i] = bsrc((wid & 3) * 8 + k, c);
      } else {
        const int q = wid * C::CPW + i * 64 + lane;
        const int k = q / C::NC, p = q % C::NC, idx = p ^ C::bswz(k);
        gbo[i] = bsrc(k, idx);
      }
      asm volatile("" : "+v"(gbo[i]));
    }
    bdst = lbase + C::LDS_B0 + (C::KIND == 2 ? wid * 1920 + (wid >> 2) * 16 : wid * C::CPW * 16);
    mp = mperm;
    sched_fence();
  }
  int mp;
  DEV void init2() {
    const int mperm = mp;
    const int fr = lane & 15, g = lane >> 4;
#pragma unroll
    for (int ks = 0; ks < 2; ++ks)
#pragma unroll
      for (int m = 0; m < (C::PERM ? C::MT : 1); ++m) {
        ra[ks][m] = C::AFRAG ? lbase + (4 * ks + g) * (C::BM * 16) + ((wm * C::MT + (C::PERM ? (m ^ mperm) : m)) * 16 + fr) * 16
                             : lbase + ((wm * C::MT + (C::PERM ? (m ^ mperm) : m)) * 16 + fr) * 128 + (((4 * ks + g) ^ (fr >> 1)) * 16);
        asm volatile("" : "+v"(ra[ks][m]));
      }
    const int q4 = fr >> 2, p4 = fr & 3, k = 8 * g + q4;
#pragma unroll
    for (int j = 0; j < C::NT; ++j) {
      if constexpr (C::KIND == 2) {
        const int mat = j < 3 ? 0 : j < 6 ? 1 : (p4 >> 1);
        const int ch = wn * 7 + (j < 6 ? 2 * (j % 3) + (p4 >> 1) : 6);
        rb[j] = lbase + C::LDS_B0 + (mat * 4 + g) * 1920 + mat * 16 + q4 * 224 + ch * 16 + 8 * (p4 & 1);
      } else {
        int idx;
        if (C::KIND == 1) idx = j * 8 + wn * 2 + (p4 >> 1);
        else idx = 2 * (wn * C::NT + j) + (p4 >> 1);
        rb[j] = lbase + C::LDS_B0 + k * C::NC * 16 + ((idx ^ C::bswz(k)) * 16) + 8 * (p4 & 1);
      }
      asm volatile("" : "+v"(rb[j]));
    }
    sched_fence();
  }
  DEV void init1(const char* Abase, const char* Bbase, long bstep_) {
    bstep = bstep_;
    abase = Abase; bbase = Bbase;
    vopRange<C::T0, C::TPRE, false, 0, C::nfirst()>();
    sched_fence();
    bar();
    sched_fence();
    vopRange<C::T0, C::TPRE, false, C::nfirst(), C::npro()>();
    sched_fence();
    init2();
    if constexpr (C::STATS) {
      u32x4 o4 = {0x3F803F80u, 0x3F803F80u, 0x3F803F80u, 0x3F803F80u};
      asm volatile("" : "+v"(o4));
      ones = __builtin_bit_cast(bf16x8, o4);
    }
#pragma unroll
    for (int j = 0; j < 3; ++j) cs[j] = (f32x4){0.f, 0.f, 0.f, 0.f};
  }
  DEV void pinAcc() {
    if constexpr (C::STATS) asm volatile("" : "+v"(cs[0]), "+v"(cs[1]), "+v"(cs[2]));
  }
  template <int KID, class H> DEV void run(H hook) {
    if (C::STATS && wm) allSteps<KID, 1>(hook);
    else allSteps<KID, 0>(hook);
    ts();
    STAMP(KID, 2);
  }
};

DEV u32x4 xpose2(u32x2 a, u32x2 b) {
  auto x = __builtin_amdgcn_permlane16_swap(a.x, b.x, false, false);
  auto y = __builtin_amdgcn_permlane16_swap(a.y, b.y, false, false);
  return (u32x4){x[0], y[0], x[1], y[1]};
}
#define STAUX 0
#define SC1M 29
#define AUXK(k) (((SC1M >> (k)) & 1) ? 16 : 0)
template <int AUX> DEV void st16(void* base, long byte_off, u32x4 v) {
  if constexpr (AUX == 0) *(u32x4*)((char*)base + byte_off) = v;
  else {
    const __amdgpu_buffer_rsrc_t r = __builtin_amdgcn_make_buffer_rsrc(base, 0, 0x7fffffff, 0x00020000);
    __builtin_amdgcn_raw_buffer_store_b128(v, r, (int)byte_off, 0, AUX);
  }
}
DEV u32x2 phi4(f32x4 u) {
  const f32x4 a = u * 1.4426950408889634f;
  f32x4 e, m;
#pragma unroll
  for (int r = 0; r < 4; ++r) { e[r] = __builtin_amdgcn_fmed3f(__builtin_amdgcn_exp2f(a[r]), 0.f, 1.f); m[r] = __builtin_fmaxf(u[r], 1e-6f); }
  const f32x4 p = m + e;
  u32x2 w; w.x = pk2(p[0], p[1]); w.y = pk2(p[2], p[3]);
  return w;
}

using C1 = Cfg<128, 24, 2, 4, 4, 3, 4, 4, 64, 32, 8, 1, 0x1u>;
static_assert(C1::XS == 25 && C1::LDS_BYTES == 163840 && C1::H == 1 && C1::NMAIN == 25 && C1::P == 8, "K1 schedule");
__global__ __launch_bounds__(C1::NTHR, 2) void k1_qkv(const u16* __restrict__ x, const u16* __restrict__ wqkv,
                                                      u16* __restrict__ pqT, float* __restrict__ kvp,
                                                      const u16* __restrict__ wo, const u16* __restrict__ wg) {
  extern __shared__ __attribute__((aligned(16))) char smem[];
  LAS char* lds = (LAS char*)smem;
  const int bid = blockIdx.x, xcd = bid & 7, idx = bid >> 3;
  const int rb = (xcd & 1) * 8 + (idx & 7), hp = (xcd >> 1) * 4 + (idx >> 3);
  STAMP(0, 0);
  u32x4 ka = karg4();
  sched_fence();
  Loop<C1> L;
  auto bsrc = [&](int k, int c) -> unsigned { return (k * (3 * D_) + (c >> 3) * D_ + (c & 7) * 8) * 2; };
  L.init0(lds, D_ * 2, bsrc, -1);
  karg_wait(ka);
  L.init1(karg_ptr(ka, 0) + (long)rb * 128 * D_ * 2, karg_ptr(ka, 1) + hp * 128, 64L * 3 * D_ * 2);
  const int lane = L.lane, fr = lane & 15, g = lane >> 4, wm = L.wm, wn = L.wn, wid = L.wid;
  {
    const char* pc0; int csz;
    asm volatile("s_getpc_b64 %0\nk1_code_here:\n\ts_mov_b32 %1, k4_code_end-k1_code_here" : "=s"(pc0), "=s"(csz));
    const int f = ((idx * 8 + wid) * 4 + (lane & 3)) * 64;
    touchv(f < csz ? f : 0, pc0);
  }
  const char* tp;
  {
    const int f = (idx * 8 + wid) * 60 + (lane < 60 ? lane : 0);
    const bool isx = f < 6144;
    const int f2 = isx ? f : f - 6144, row = f2 / 24, c = f2 - row * 24;
    const unsigned to = isx ? ((xcd & 1) * 1024 + (xcd >> 1) * 256 + row) * 2048 + 512 + c * 64
                            : (256 + 2 * row + (xcd & 1)) * 6144 + (c >> 3) * 2048 + (xcd >> 1) * 512 + (c & 7) * 64;
    tp = (isx ? karg_ptr(ka, 0) : karg_ptr(ka, 1)) + to;
  }
  L.run<0>([&](auto sx) {
    if constexpr (decltype(sx)::value == 1000) {
      asm volatile("global_load_dword v255, %0, off" :: "v"(tp) : "memory", "v255");
    } else {
      const unsigned o1 = (idx * 32 + wid * 4 + (lane >> 4)) * 2048 + (xcd >> 2) * 1024 + (lane & 15) * 64;
      touchv(o1, wo);
      const unsigned o2 = ((bid * 8 + wid) * 56 + (lane < 56 ? lane : 0)) * 64;
      touchv(o2, wg);
    }
  });
  LAS char* ep = lds;
  LAS float* sR = (LAS float*)ep;
  LAS float* sC = (LAS float*)(ep + 1024);
  LAS char* sF = ep + 4096;
  {
    const float a = L.rs[0];
    const float b = (fr & 2) ? ((fr & 1) ? L.rq[3] : L.rq[2]) : ((fr & 1) ? L.rq[1] : L.rq[0]);
    if (g == (fr >> 2)) {
      float mean = a * (1.f / D_);
      float var = fmaxf(b * (1.f / D_) - mean * mean, 0.f);
      float rstd = rsqrtf(var + 1e-5f);
      *(LAS f32x2*)(sR + (wm * 64 + wn * 16 + fr) * 2) = (f32x2){rstd, -mean * rstd};
#pragma unroll
      for (int j = 0; j < 3; ++j) sC[wm * 192 + j * 64 + wn * 16 + fr] = L.cs[j][0];
    }
  }
  L.ts();
  lbar();
  L.ts();
  {
    const int ik = 64 + wn * 16 + fr;
    const float ck = sC[ik] + sC[192 + ik], cv = sC[64 + ik] + sC[256 + ik];
#pragma unroll
    for (int m = 0; m < 4; ++m) {
      const int am = m ^ wn, r0 = wm * 64 + am * 16 + 4 * g;
      const f32x4 u0 = *(const LAS f32x4*)(sR + r0 * 2), u1 = *(const LAS f32x4*)(sR + r0 * 2 + 4);
      const f32x4 sa = {u0[0], u0[2], u1[0], u1[2]}, sb = {u0[1], u0[3], u1[1], u1[3]};
      const f32x4 k = L.acc[m][1] * sa + (sb * ck + 1e-6f);
      const f32x4 v = L.acc[m][2] * sa + sb * cv;
      u32x2 a = phi4(k), b;
      b.x = pk2(v[0], v[1]); b.y = pk2(v[2], v[3]);
      LAS char* f = sF + (wn * 4 + wm * 2 + (am >> 1)) * 1024 + lane * 16 + (am & 1) * 8;
      *(LAS u32x2*)f = a;
      *(LAS u32x2*)(f + 16384) = b;
    }
  }
  L.ts();
  {
    const int ic = wn * 16 + 4 * g;
    const f32x4 c4 = *(const LAS f32x4*)(sC + ic) + *(const LAS f32x4*)(sC + 192 + ic);
    u32x2 w[4];
#pragma unroll
    for (int m = 0; m < 4; ++m) {
      const f32x2 st = *(const LAS f32x2*)(sR + (wm * 64 + (m ^ wn) * 16 + fr) * 2);
      w[m] = phi4(L.acc[m][0] * st.x + (c4 * st.y + 1e-6f));
    }
    const long pb = ((long)(hp * 4 + wn) * S_ + rb * 128 + wm * 64 + fr) * 16 + (g >> 1) * 8;
#pragma unroll
    for (int m = 0; m < 4; m += 2) st16<AUXK(0)>(pqT, (pb + (((m + (g & 1)) ^ wn) * 16) * 16) * 2, xpose2(w[m], w[m + 1]));
  }
  L.ts();
  lbar();
  L.ts();
  {
    f32x4 d = {0.f, 0.f, 0.f, 0.f}, z = d;
#pragma unroll
    for (int G = 0; G < 4; ++G) {
      const bf16x8 fk = *(const LAS bf16x8*)(sF + (wn * 4 + G) * 1024 + lane * 16);
      const bf16x8 fv = *(const LAS bf16x8*)(sF + 16384 + (((wn & 2) + wm) * 4 + G) * 1024 + lane * 16);
      d = mfma16(fk, fv, d);
      z = mfma16(fk, L.ones, z);
    }
    const long kp = ((long)(hp * 2 + (wn >> 1)) * 16 + rb) * KVP;
    st16<AUXK(0)>(kvp, (kp + ((wn & 1) * 2 + wm) * 256 + lane * 4) * 4, __builtin_bit_cast(u32x4, d));
    if (wm == 0 && fr == 0) st16<AUXK(0)>(kvp, (kp + 1024 + (wn & 1) * 16 + 4 * g) * 4, __builtin_bit_cast(u32x4, z));
  }
  L.ts();
  wait_vm<0>();
  L.ts();
  STAMP(0, 3);
  L.tsdump<0>();
}

__global__ __launch_bounds__(512) void k15_o(const u16* __restrict__ pqT, const float* __restrict__ kvp, u16* __restrict__ o,
                                             const u16* __restrict__ wg, const u16* __restrict__ wu, float* __restrict__ csp) {
  __shared__ __attribute__((aligned(16))) u16 kvT[33 * 40];
  __shared__ __attribute__((aligned(16))) float sCS[16 * 228];
  const int bid = blockIdx.x, head = bid & 31, rb = bid >> 5;
  const int tid = threadIdx.x, lane = tid & 63, wid = __builtin_amdgcn_readfirstlane(tid >> 6), fr = lane & 15, g = lane >> 4;
  STAMP(1, 0);
  f32x4 z0 = {0.f, 0.f, 0.f, 0.f}, z1 = z0;
  if (wid == 0) {
    const f32x4* p = (const f32x4*)(kvp + ((long)head * 16 + (lane >> 3)) * KVP + 1024) + (lane & 7);
    z0 = p[0]; z1 = p[8 * (KVP / 4)];
  }
  sched_fence();
  f32x4 v[8];
  {
    const f32x4* p = (const f32x4*)(kvp + ((long)head * 16 + (tid & 1) * 8) * KVP) + (tid >> 1);
#pragma unroll
    for (int b = 0; b < 8; ++b) v[b] = p[b * (KVP / 4)];
  }
  bf16x8 fq[2];
#pragma unroll
  for (int m = 0; m < 2; ++m)
    fq[m] = *(const bf16x8*)(pqT + ((long)(head * 2 + (g >> 1)) * S_ + rb * 256 + wid * 32 + m * 16 + fr) * 16 + (g & 1) * 8);
  const int xcd = bid & 7, idx = bid >> 3;
  const int rgp = (idx >> 3) * 2 + (xcd & 1), c8 = idx & 7, cgl = (xcd >> 1) * 4 + (c8 & 3);
  const int ct = tid < 448 ? tid : 447, r0 = ct / 28, cc = ct - r0 * 28;
  sched_fence();
  bar();
  sched_fence();
  u32x4 wv[8];
  {
    const u16* p = (c8 < 4 ? wg : wu) + (long)(rgp * 128 + r0) * DF_ + cgl * 224 + cc * 8;
#pragma unroll
    for (int i = 0; i < 8; ++i) wv[i] = *(const u32x4*)(p + (long)i * 16 * DF_);
  }
  sched_fence();
  {
    f32x4 s = ((v[0] + v[1]) + (v[2] + v[3])) + ((v[4] + v[5]) + (v[6] + v[7]));
#pragma unroll
    for (int r = 0; r < 4; ++r) s[r] += __shfl_xor(s[r], 1);
    const int it = tid >> 1, blk = it >> 6, l2 = it & 63;
    const int e = (blk & 1) * 16 + (l2 & 15), d = (blk >> 1) * 16 + (l2 >> 4) * 4 + (tid & 1) * 2;
    *(unsigned*)(kvT + e * 40 + d) = (tid & 1) ? pk2(s[2], s[3]) : pk2(s[0], s[1]);
  }
  if (wid == 0) {
    f32x4 z = z0 + z1;
#pragma unroll
    for (int r = 0; r < 4; ++r) { z[r] += __shfl_xor(z[r], 8); z[r] += __shfl_xor(z[r], 16); z[r] += __shfl_xor(z[r], 32); }
    if (lane < 8) { u32x2 w; w.x = pk2(z[0], z[1]); w.y = pk2(z[2], z[3]); *(u32x2*)(kvT + 32 * 40 + lane * 4) = w; }
  }
  STAMP(1, 1);
  lbar();
  bf16x8 fa[3];
#pragma unroll
  for (int t = 0; t < 2; ++t) fa[t] = *(const bf16x8*)(kvT + (t * 16 + fr) * 40 + g * 8);
  fa[2] = *(const bf16x8*)(kvT + 32 * 40 + g * 8);
  const f32x4 z4 = {0.f, 0.f, 0.f, 0.f};
#pragma unroll
  for (int m = 0; m < 2; ++m) {
    f32x4 den = mfma16(fa[2], fq[m], z4);
    float inv = 1.f / (den[0] + 1e-6f);
    const long row = (long)rb * 256 + wid * 32 + m * 16 + fr;
    u32x2 w[2];
#pragma unroll
    for (int t = 0; t < 2; ++t) {
      f32x4 n = mfma16(fa[t], fq[m], z4);
      w[t].x = pk2(n[0] * inv, n[1] * inv); w[t].y = pk2(n[2] * inv, n[3] * inv);
    }
    st16<AUXK(1)>(o, (row * D_ + head * 32 + (g & 1) * 16 + (g >> 1) * 8) * 2, xpose2(w[0], w[1]));
  }
  sched_fence();
  if (wid < 7) {
    f32x4 lo = {0.f, 0.f, 0.f, 0.f}, hi = lo;
#pragma unroll
    for (int i = 0; i < 8; ++i) {
      lo += (f32x4){bflo(wv[i][0]), bflo(wv[i][1]), bflo(wv[i][2]), bflo(wv[i][3])};
      hi += (f32x4){bfhi(wv[i][0]), bfhi(wv[i][1]), bfhi(wv[i][2]), bfhi(wv[i][3])};
    }
    *(f32x4*)(sCS + r0 * 228 + cc * 8) = (f32x4){lo[0], hi[0], lo[1], hi[1]};
    *(f32x4*)(sCS + r0 * 228 + cc * 8 + 4) = (f32x4){lo[2], hi[2], lo[3], hi[3]};
  }
  STAMP(1, 2);
  lbar();
  if (tid < 448) {
    const int c = tid >> 1, hf = tid & 1;
    float t = 0.f;
#pragma unroll
    for (int i = 0; i < 8; ++i) t += sCS[(hf * 8 + i) * 228 + c];
    t += __shfl_xor(t, 1);
    if (hf == 0) csp[(long)rgp * (2 * DF_) + (c8 < 4 ? 0 : DF_) + cgl * 224 + c] = t;
  }
  STAMP(1, 3);
}

using C2 = Cfg<128, 8, 2, 2, 4, 2, 4, 4, 64, 32, 8, 0, 0x3u>;
__global__ __launch_bounds__(C2::NTHR, 1) void k2_wo(const u16* __restrict__ o, const u16* __restrict__ wo, const u16* __restrict__ x,
                                                     u16* __restrict__ h, float* __restrict__ hstat) {
  extern __shared__ __attribute__((aligned(16))) char smem[];
  LAS char* lds = (LAS char*)smem;
  const int bid = blockIdx.x, xcd = bid & 7, idx = bid >> 3;
  const int rb = (xcd & 3) * 4 + (idx & 3), ct = (xcd >> 2) * 8 + (idx >> 2);
  STAMP(2, 0);
  u32x4 ka = karg4();
  sched_fence();
  Loop<C2> L;
  auto bsrc = [&](int k, int c) -> unsigned { return (k * D_ + c * 8) * 2; };
  L.init0(lds, D_ * 2, bsrc, 0);
  karg_wait(ka);
  L.init1(karg_ptr(ka, 0) + (long)rb * 128 * D_ * 2, karg_ptr(ka, 1) + ct * 128, 64L * D_ * 2);
  const int lane = L.lane, fr = lane & 15, g = lane >> 4, wm = L.wm, wn = L.wn;
  const long off0 = ((long)rb * 128 + wm * 64 + fr) * D_ + ct * 64 + wn * 32 + (g & 1) * 16 + (g >> 1) * 8;
  u32x4 xr[4];
  L.run<2>([&](auto) {
#pragma unroll
    for (int m = 0; m < 4; ++m) xr[m] = *(const u32x4*)(x + off0 + m * 16 * D_);
  });
  LAS float* sS = (LAS float*)lds;
#pragma unroll
  for (int m = 0; m < 4; ++m) {
    const int lr = wm * 64 + m * 16 + fr;
    float s = 0.f, q = 0.f;
    const u32x4 r = xpose2((u32x2){xr[m][0], xr[m][1]}, (u32x2){xr[m][2], xr[m][3]});
    u32x2 w[2];
#pragma unroll
    for (int j = 0; j < 2; ++j) {
      w[j].x = pk2(bflo(r[2 * j]) + L.acc[m][j][0], bfhi(r[2 * j]) + L.acc[m][j][1]);
      w[j].y = pk2(bflo(r[2 * j + 1]) + L.acc[m][j][2], bfhi(r[2 * j + 1]) + L.acc[m][j][3]);
      float f0 = bflo(w[j].x), f1 = bfhi(w[j].x), f2 = bflo(w[j].y), f3 = bfhi(w[j].y);
      s += (f0 + f1) + (f2 + f3);
      q += (f0 * f0 + f1 * f1) + (f2 * f2 + f3 * f3);
    }
    st16<AUXK(2)>(h, (off0 + m * 16 * D_) * 2, xpose2(w[0], w[1]));
    s += __shfl_xor(s, 16); s += __shfl_xor(s, 32);
    q += __shfl_xor(q, 16); q += __shfl_xor(q, 32);
    if (g == 0) *(LAS f32x2*)(sS + (wn * 128 + lr) * 2) = (f32x2){s, q};
  }
  lbar();
  if (threadIdx.x < 128) {
    const int r = threadIdx.x;
    f32x2 v = *(LAS f32x2*)(sS + r * 2) + *(LAS f32x2*)(sS + (128 + r) * 2);
    *(f32x2*)(hstat + (((long)rb * 128 + r) * 16 + ct) * 2) = v;
  }
  L.ts();
  STAMP(2, 3);
  L.tsdump<2>();
}

using C3 = Cfg<256, 28, 4, 2, 4, 7, 3, 4, 32, 32, 12, 2, 0x7Fu>;
constexpr int K3_LDS = C3::LDS_BYTES + 3072;
__global__ __launch_bounds__(C3::NTHR, 2) void k3_glu(const u16* __restrict__ h, const u16* __restrict__ wg, const u16* __restrict__ wu,
                                                      const float* __restrict__ hstat,
                                                      u16* __restrict__ act, const u16* __restrict__ wd, unsigned* __restrict__ xbuf) {
  extern __shared__ __attribute__((aligned(16))) char smem[];
  LAS char* lds = (LAS char*)smem;
  const int bid = blockIdx.x, xcd = bid & 7, idx = bid >> 3;
  const int rb = (xcd & 1) * 4 + (idx & 3), ct = (xcd >> 1) * 8 + (idx >> 2);
  const int tid = threadIdx.x;
  STAMP(3, 0);
  u32x8 ka = karg8();
  sched_fence();
  auto cmap = [](int c, bool& up) -> int {
    const int w = c >= 14, r = c - 14 * w;
    up = r >= 12 ? (r & 1) : r >= 6;
    return w * 7 + (r >= 12 ? 6 : r >= 6 ? r - 6 : r);
  };
  f32x4 hs[4];
  float cp[4];
  Loop<C3> L;
  L.ts();
  auto bsrc = [&](int k, int c) -> unsigned { return (k * DF_ + c * 8) * 2; };
  L.init0(lds, D_ * 2, bsrc, 0);
  L.ts();
  karg_wait(ka);
  L.ts();
  L.init1(karg_ptr(ka, 0) + (long)rb * 256 * D_ * 2, (L.wid >> 2 ? karg_ptr(ka, 2) : karg_ptr(ka, 1)) + ct * 224, 32L * DF_ * 2);
  const int lane = L.lane, fr = lane & 15, g = lane >> 4, wm = L.wm, wn = L.wn;
  LAS float* sC = (LAS float*)(lds + C3::LDS_BYTES);
  LAS float* sR = (LAS float*)(lds + C3::LDS_BYTES + 1024);
  const char* hsp = karg_ptr(ka, 3);
  unsigned ho = (rb * 256 + L.wid * 32) * 128 + lane * 16, co, to, sra, sca;
  f32x2 pp[4];
  float w8[8];
  {
    const int c = tid < 448 ? tid >> 1 : 0;
    bool up; const int ch = cmap(c >> 3, up);
    co = (S_ * 32 + (up ? DF_ : 0) + ct * 112 + ch * 8 + (c & 7) + (tid & 1) * (4 * 2 * DF_)) * 4;
    to = (((xcd & 1) * 4 + (xcd >> 1)) * 256 + idx * 8 + L.wid) * 3584 + (lane < 28 ? lane : 0) * 64;
    sca = (unsigned)(size_t)(sC + c);
    sra = (unsigned)(size_t)((lane & 4) ? sR + (L.wid * 32 + (lane & 3) * 8 + (lane >> 3)) * 2 : sC + 240);
    asm volatile("" : "+v"(ho), "+v"(co), "+v"(to), "+v"(sra), "+v"(sca));
  }
  L.run<3>([&](auto sx) {
    constexpr int sv = decltype(sx)::value;
    if constexpr (sv == SENTS * 100 + 12 || sv == SENTS * 100 + 26) stsent(bid * 16384 + tid * 16 + (sv == SENTS * 100 + 12 ? 0 : 8192), xbuf);
    constexpr int s0 = sv - SB * 100, su = s0 < 0 || s0 >= 500 ? -1 : s0 < 200 ? s0 + 2600 : s0 < 300 ? -1 : s0 + 2500;
    if constexpr (su >= 0) {
      if constexpr (su == 2602) ld4<0>(hs[0], ho, hsp);
      if constexpr (su == 2609) ld4<1024>(hs[1], ho, hsp);
      if constexpr (su == 2616) ld4<2048>(hs[2], ho, hsp);
      if constexpr (su == 2623) ld4<3072>(hs[3], ho, hsp);
      if constexpr (su == 2703) ld1(cp[0], co, hsp);
      if constexpr (su == 2708) ld1(cp[1], co, hsp + 1L * (2 * DF_ * 4));
      if constexpr (su == 2717) ld1(cp[2], co, hsp + 2L * (2 * DF_ * 4));
      if constexpr (su == 2722) ld1(cp[3], co, hsp + 3L * (2 * DF_ * 4));
      if constexpr (su == 2800) asm volatile("s_waitcnt vmcnt(8)" : "+v"(hs[0]), "+v"(hs[1]), "+v"(hs[2]), "+v"(hs[3]) :: "memory");
      if constexpr (su >= 2800 && su < 2804) {
        constexpr int i = su - 2800;
        pp[i] = (f32x2){hs[i][0], hs[i][1]} + (f32x2){hs[i][2], hs[i][3]};
        asm volatile("" : "+v"(pp[i]));
      }
      if constexpr (su >= 2804 && su < 2812) {
        constexpr int i = (su - 2804) >> 2, e = (su - 2804) & 1, gv = ((su - 2804) >> 1) & 1;
        w8[su - 2804] = fsel(lane & 1, pp[2 * i + 1 - gv][e], pp[2 * i + gv][e]);
        asm volatile("" : "+v"(w8[su - 2804]));
      }
      if constexpr (su >= 2812 && su < 2816) {
        constexpr int i = (su - 2812) >> 1, e = (su - 2812) & 1;
        pp[i][e] = w8[4 * i + e] + dpp<0xB1>(w8[4 * i + 2 + e]);
        asm volatile("" : "+v"(pp[i]));
      }
      if constexpr (su >= 2816 && su < 2820) {
        constexpr int e = (su - 2816) & 1, gv = (su - 2816) >> 1;
        w8[su - 2816] = fsel(lane & 2, pp[1 - gv][e], pp[gv][e]);
        asm volatile("" : "+v"(w8[su - 2816]));
      }
      if constexpr (su == 2820 || su == 2821) { w8[su - 2816] = w8[su - 2820] + dpp<0x4E>(w8[su - 2818]); asm volatile("" : "+v"(w8[su - 2816])); }
      if constexpr (su == 2823 || su == 2824) {
        w8[su - 2817] = (w8[su - 2819] + dpp<0x124>(w8[su - 2819])) * (1.f / D_); asm volatile("" : "+v"(w8[su - 2817]));
      }
      if constexpr (su == 2826) { w8[7] = fmaxf(w8[7] - w8[6] * w8[6], 0.f); asm volatile("" : "+v"(w8[7])); }
      if constexpr (su == 2900) *(LAS f32x2*)(size_t)sra = (f32x2){-w8[6], w8[7] + 1e-5f};
      if constexpr (su == 2902) {
        asm volatile("s_waitcnt vmcnt(8)" : "+v"(cp[0]), "+v"(cp[1]), "+v"(cp[2]), "+v"(cp[3]) :: "memory");
        cp[0] += cp[1]; asm volatile("" : "+v"(cp[0]));
      }
      if constexpr (su == 2903) { cp[2] += cp[3]; asm volatile("" : "+v"(cp[2])); }
      if constexpr (su == 2904) { cp[0] += cp[2]; asm volatile("" : "+v"(cp[0])); }
      if constexpr (su == 2906) { cp[0] += dpp<0xB1>(cp[0]); asm volatile("" : "+v"(cp[0])); }
      if constexpr (su == 2908) *(LAS float*)(size_t)sca = cp[0];
    }
    if constexpr (sv == TCH * 100) touchv(to, wd);
    if constexpr (sv == TCH2 * 100 + 1) touchv(to + 1792, wd);
  });
  static_assert(C3::XS == 28, "K3 schedule");
  {
    float nm[4];
#pragma unroll
    for (int m = 0; m < 4; ++m) nm[m] = sR[(wm * 64 + m * 16 + fr) * 2];
#pragma unroll
    for (int j = 0; j < 7; ++j) {
      const f32x4 c4 = *(const LAS f32x4*)(sC + (wn * 14 + 2 * j) * 8 + 4 * g);
#pragma unroll
      for (int m = 0; m < 4; ++m) L.acc[m][j] += c4 * nm[m];
    }
  }
  const int hi = lane >> 5;
  const int apo = ((rb * 2 + (wm >> 1)) * 448 + ct * 14 + wn * 7) * 2048 + ((wm & 1) * 64 + fr) * 16;
  constexpr float K1c = -2.f * 0.7978845608028654f * 1.4426950408889634f, K2c = K1c * 0.044715f;
  auto glu = [&](f32x4 a, f32x4 b, float q, float r1, float r3) -> u32x2 {
    const f32x4 z = a * ((a * a) * r3 + r1);
    f32x4 e;
#pragma unroll
    for (int r = 0; r < 4; ++r) e[r] = __builtin_amdgcn_exp2f(z[r]);
    e = e * q + q;
#pragma unroll
    for (int r = 0; r < 4; ++r) e[r] = __builtin_amdgcn_rcpf(e[r]);
    const f32x4 o = (a * b) * e;
    u32x2 w; w.x = pk2(o[0], o[1]); w.y = pk2(o[2], o[3]);
    return w;
  };
  u32x2 w6[2];
#pragma unroll
  for (int mp = 0; mp < 2; ++mp) {
    float q[2], r1[2], r3[2];
#pragma unroll
    for (int mm = 0; mm < 2; ++mm) {
      q[mm] = sR[(wm * 64 + (2 * mp + mm) * 16 + fr) * 2 + 1];
      const float rstd = __builtin_amdgcn_rsqf(q[mm]);
      r1[mm] = K1c * rstd; r3[mm] = K2c * rstd * rstd * rstd;
    }
#pragma unroll
    for (int j = 0; j < 3; ++j) {
      const u32x2 wa = glu(L.acc[2 * mp][j], L.acc[2 * mp][3 + j], q[0], r1[0], r3[0]);
      const u32x2 wb = glu(L.acc[2 * mp + 1][j], L.acc[2 * mp + 1][3 + j], q[1], r1[1], r3[1]);
      st16<AUXK(3)>(act, apo + (2 * j + (g >> 1)) * 2048 + (2 * mp + (g & 1)) * 256, xpose2(wa, wb));
    }
    f32x4 G, U;
#pragma unroll
    for (int r = 0; r < 4; ++r) {
      auto sw = __builtin_amdgcn_permlane32_swap(__float_as_uint(L.acc[2 * mp][6][r]), __float_as_uint(L.acc[2 * mp + 1][6][r]), false, false);
      G[r] = __uint_as_float(sw[0]); U[r] = __uint_as_float(sw[1]);
    }
    w6[mp] = glu(G, U, hi ? q[1] : q[0], hi ? r1[1] : r1[0], hi ? r3[1] : r3[0]);
  }
  st16<AUXK(3)>(act, apo + 6 * 2048 + (2 * (g & 1) + (g >> 1)) * 256, xpose2(w6[0], w6[1]));
  wait_vm<0>();
  L.ts();
  STAMP(3, 3);
  L.tsdump<3>();
}

using C4 = Cfg<128, 16, 2, 4, 4, 2, 4, 4, 64, 56, 8, 3, 0x3u>;
__global__ __launch_bounds__(C4::NTHR, 2) void k4_down(const u16* __restrict__ act, const u16* __restrict__ wd, const u16* __restrict__ h,
                                                       u16* __restrict__ out, unsigned* __restrict__ xbuf) {
  extern __shared__ __attribute__((aligned(16))) char smem[];
  LAS char* lds = (LAS char*)smem;
  const int bid = blockIdx.x, xcd = bid & 7, idx = bid >> 3;
  const int kh = xcd & 1, rb = (xcd >> 1) * 4 + (idx & 3), cb = idx >> 2;
  STAMP(4, 0);
  u32x4 ka = karg4();
  sched_fence();
  Loop<C4> L;
  auto bsrc = [&](int k, int c) -> unsigned { return (k * D_ + c * 8) * 2; };
  L.init0(lds, DF_ * 2, bsrc, 2 * kh);
  karg_wait(ka);
  L.init1(karg_ptr(ka, 0) + (long)(rb * 448 + kh * 224) * 2048, karg_ptr(ka, 1) + ((long)kh * 1792 * D_ + cb * 128) * 2, 64L * D_ * 2);
  const int lane = L.lane, fr = lane & 15, g = lane >> 4, wm = L.wm, wn = L.wn;
  const long off0 = ((long)rb * 128 + wm * 64 + kh * 32 + fr) * D_ + cb * 128 + wn * 32 + (g & 1) * 16 + (g >> 1) * 8;
  u32x4 xr[2];
  L.run<4>([&](auto) {
#pragma unroll
    for (int m = 0; m < 2; ++m) xr[m] = *(const u32x4*)(h + off0 + m * 16 * D_);
  });
  const __amdgpu_buffer_rsrc_t rx = __builtin_amdgcn_make_buffer_rsrc((void*)xbuf, 0, XBUF_BYTES, 0x00020000);
  const int xo = L.wid * 2048 + lane * 16;
#pragma unroll
  for (int i = 0; i < 2; ++i) {
    const f32x4 a = L.acc[2 + i][0], b = L.acc[2 + i][1];
    const u32x4 v = {pk2(a[0], a[1]), pk2(a[2], a[3]), pk2(b[0], b[1]), pk2(b[2], b[3])};
    __builtin_amdgcn_raw_buffer_store_b128(v, rx, bid * 16384 + xo + i * 1024, 0, 16);
  }
  u32x4 rv[2];
  for (int spin = 0; spin < (1 << 22); ++spin) {
    asm volatile("" ::: "memory");
    bool ok = true;
#pragma unroll
    for (int i = 0; i < 2; ++i) rv[i] = __builtin_amdgcn_raw_buffer_load_b128(rx, (bid ^ 1) * 16384 + xo + i * 1024, 0, 16);
#pragma unroll
    for (int i = 0; i < 2; ++i) ok = ok && rv[i][0] != SENT && rv[i][1] != SENT && rv[i][2] != SENT && rv[i][3] != SENT;
    if (__all(ok)) break;
  }
#pragma unroll
  for (int m = 0; m < 2; ++m) {
    const u32x4 r = xpose2((u32x2){xr[m][0], xr[m][1]}, (u32x2){xr[m][2], xr[m][3]});
    u32x2 w[2];
#pragma unroll
    for (int j = 0; j < 2; ++j) {
      const f32x4 a = L.acc[m][j];
      w[j].x = pk2(bflo(r[2 * j]) + (a[0] + bflo(rv[m][2 * j])), bfhi(r[2 * j]) + (a[1] + bfhi(rv[m][2 * j])));
      w[j].y = pk2(bflo(r[2 * j + 1]) + (a[2] + bflo(rv[m][2 * j + 1])), bfhi(r[2 * j + 1]) + (a[3] + bfhi(rv[m][2 * j + 1])));
    }
    st16<AUXK(4)>(out, (off0 + m * 16 * D_) * 2, xpose2(w[0], w[1]));
  }
  L.ts();
  STAMP(4, 3);
  L.tsdump<4>();
  asm volatile("k4_code_end:");
}

extern "C" void kernel_launch(void* const* d_in, const int* in_sizes, int n_in, void* d_out, int out_size, void* d_ws,
                              size_t ws_size, hipStream_t stream) {
  const u16* x = (const u16*)d_in[0];
  const u16* wqkv = (const u16*)d_in[1];
  const u16* wo = (const u16*)d_in[2];
  const u16* wg = (const u16*)d_in[4];
  const u16* wu = (const u16*)d_in[5];
  const u16* wd = (const u16*)d_in[6];
  u16* out = (u16*)d_out;
  char* ws = (char*)d_ws;
  u16* pq = (u16*)ws;                      ws += (size_t)S_ * D_ * 2;
  u16* o = (u16*)ws;                       ws += (size_t)S_ * D_ * 2;
  u16* h = (u16*)ws;                       ws += (size_t)S_ * D_ * 2;
  u16* act = (u16*)ws;                     ws += (size_t)S_ * DF_ * 2;
  float* kvp = (float*)ws;                 ws += (size_t)NH_ * 16 * KVP * 4;
  float* hstat = (float*)ws;               ws += (size_t)S_ * 16 * 2 * 4;
  float* csp = (float*)ws;                 ws += (size_t)8 * 2 * DF_ * 4;
  unsigned* xbuf = (unsigned*)ws;          ws += (size_t)XBUF_BYTES;
  (void)hipFuncSetAttribute((const void*)k1_qkv, hipFuncAttributeMaxDynamicSharedMemorySize, C1::LDS_BYTES);
  (void)hipFuncSetAttribute((const void*)k2_wo, hipFuncAttributeMaxDynamicSharedMemorySize, C2::LDS_BYTES);
  (void)hipFuncSetAttribute((const void*)k3_glu, hipFuncAttributeMaxDynamicSharedMemorySize, K3_LDS);
  (void)hipFuncSetAttribute((const void*)k4_down, hipFuncAttributeMaxDynamicSharedMemorySize, C4::LDS_BYTES);
  k1_qkv<<<256, C1::NTHR, C1::LDS_BYTES, stream>>>(x, wqkv, pq, kvp, wo, wg);
  k15_o<<<256, 512, 0, stream>>>(pq, kvp, o, wg, wu, csp);
  k2_wo<<<256, C2::NTHR, C2::LDS_BYTES, stream>>>(o, wo, x, h, hstat);
  k3_glu<<<256, C3::NTHR, K3_LDS, stream>>>(h, wg, wu, hstat, act, wd, xbuf);
  k4_down<<<256, C4::NTHR, C4::LDS_BYTES, stream>>>(act, wd, h, out, xbuf);
}
```

```cpp
#include <hip/hip_runtime.h>
#include <hip/hip_bf16.h>
#include <stdint.h>

typedef unsigned short u16;
typedef short bf16x8 __attribute__((ext_vector_type(8)));
typedef short s16x4 __attribute__((ext_vector_type(4)));
typedef float f32x4 __attribute__((ext_vector_type(4)));
typedef float f32x2 __attribute__((ext_vector_type(2)));
typedef unsigned u32x4 __attribute__((ext_vector_type(4)));
typedef unsigned u32x2 __attribute__((ext_vector_type(2)));
typedef __bf16 bf16x2_t __attribute__((ext_vector_type(2)));
#define LAS __attribute__((address_space(3)))
#define DEV __device__ __forceinline__
template <int V> struct IC { static constexpr int value = V; };

constexpr int S_ = 2048, D_ = 1024, DF_ = 3584, NH_ = 32;
constexpr int KVP = 1056;
constexpr unsigned SENT = 0xFFFFFFFFu;
constexpr int XBUF_BYTES = 256 * 16384;

DEV float bflo(unsigned v) { return __uint_as_float(v << 16); }
DEV float bfhi(unsigned v) { return __uint_as_float(v & 0xffff0000u); }
DEV unsigned pk2(float lo, float hi) {
  bf16x2_t r; r.x = (__bf16)lo; r.y = (__bf16)hi; return __builtin_bit_cast(unsigned, r);
}
template <int N> DEV void wait_vm() { asm volatile("s_waitcnt vmcnt(%0)" ::"n"(N) : "memory"); }
DEV void wait_lgkm0() { __builtin_amdgcn_s_waitcnt(0xC07F); }
DEV void bar() { __builtin_amdgcn_s_barrier(); }
DEV void lbar() { wait_lgkm0(); bar(); asm volatile("" ::: "memory"); }
DEV void sched_fence() { __builtin_amdgcn_sched_barrier(0); }
DEV void glds_v(const void* g, unsigned lds_addr) {
  asm volatile("s_mov_b32 m0, %1\n\ts_nop 0\n\tglobal_load_lds_dwordx4 %0, off" ::"v"(g), "s"(lds_addr) : "memory");
}
DEV void glds_vm(const void* g, unsigned lds_addr, unsigned long long mask) {
  asm volatile("s_mov_b32 m0, %1\n\ts_mov_b64 exec, %2\n\tglobal_load_lds_dwordx4 %0, off\n\ts_mov_b64 exec, -1"
               ::"v"(g), "s"(lds_addr), "s"(mask) : "memory");
}
DEV void glds_s(unsigned voff, const void* sbase, unsigned lds_addr) {
  asm volatile("s_mov_b32 m0, %2\n\ts_nop 0\n\tglobal_load_lds_dwordx4 %0, %1" ::"v"(voff), "s"(sbase), "s"(lds_addr) : "memory");
}
DEV void glds_sm(unsigned voff, const void* sbase, unsigned lds_addr, unsigned long long mask) {
  asm volatile("s_mov_b32 m0, %2\n\ts_mov_b64 exec, %3\n\tglobal_load_lds_dwordx4 %0, %1\n\ts_mov_b64 exec, -1"
               ::"v"(voff), "s"(sbase), "s"(lds_addr), "s"(mask) : "memory");
}
DEV void touch(const void* g, unsigned lds_addr) {
  asm volatile("s_mov_b32 m0, %1\n\ts_nop 0\n\tglobal_load_lds_dword %0, off" ::"v"(g), "s"(lds_addr) : "memory");
}
DEV void touchv(unsigned voff, const void* sbase) {
  asm volatile("global_load_dword v255, %0, %1" :: "v"(voff), "s"(sbase) : "memory", "v255");
}
typedef unsigned u32x8 __attribute__((ext_vector_type(8)));
DEV u32x4 karg4() { u32x4 r; asm volatile("s_load_dwordx4 %0, %1, 0x0" : "=s"(r) : "s"(__builtin_amdgcn_kernarg_segment_ptr())); return r; }
DEV u32x8 karg8() { u32x8 r; asm volatile("s_load_dwordx8 %0, %1, 0x0" : "=s"(r) : "s"(__builtin_amdgcn_kernarg_segment_ptr())); return r; }
template <class T> DEV void karg_wait(T& r) { asm volatile("s_waitcnt lgkmcnt(0)" : "+s"(r)); }
template <class T> DEV const char* karg_ptr(const T& r, int i) { return (const char*)(((unsigned long long)r[2 * i + 1] << 32) | r[2 * i]); }
template <int OFF> DEV void ld4(f32x4& d, unsigned voff, const void* sbase) {
  asm volatile("global_load_dwordx4 %0, %1, %2 offset:%3" : "=v"(d) : "v"(voff), "s"(sbase), "n"(OFF) : "memory");
}
DEV void ld1(float& d, unsigned voff, const void* sbase) {
  asm volatile("global_load_dword %0, %1, %2" : "=v"(d) : "v"(voff), "s"(sbase) : "memory");
}
template <int CTRL> DEV float dpp(float v) { return __int_as_float(__builtin_amdgcn_update_dpp(0, __float_as_int(v), CTRL, 0xF, 0xF, true)); }
#define SENTSC ""
DEV void stsent(unsigned voff, const void* sbase) {
  const u32x4 sv = {SENT, SENT, SENT, SENT};
  asm volatile("global_store_dwordx4 %0, %1, %2 " SENTSC "\n\ts_nop 1" :: "v"(voff), "v"(sv), "s"(sbase) : "memory");
}
DEV float fsel(bool c, float a, float b) { return c ? a : b; }
DEV float fadd1(float a, float b) { float r; asm volatile("v_add_f32 %0, %1, %2" : "=v"(r) : "v"(a), "v"(b)); return r; }
DEV f32x4 mfma16(bf16x8 a, bf16x8 b, f32x4 c) { return __builtin_amdgcn_mfma_f32_16x16x32_bf16(a, b, c, 0, 0, 0); }
DEV void mfma_acc(f32x4& c, bf16x8 a, bf16x8 b) {
  asm volatile("v_mfma_f32_16x16x32_bf16 %0, %1, %2, %0" : "+v"(c) : "v"(a), "v"(b));
}
DEV void mfma_z(f32x4& c, bf16x8 a, bf16x8 b) {
  asm volatile("v_mfma_f32_16x16x32_bf16 %0, %1, %2, 0" : "=&v"(c) : "v"(a), "v"(b));
}
template <bool Z> DEV void mfma_a(f32x4& c, bf16x8 a, bf16x8 b) { if constexpr (Z) mfma_z(c, a, b); else mfma_acc(c, a, b); }
DEV bf16x8 cat(s16x4 a, s16x4 b) { return __builtin_shufflevector(a, b, 0, 1, 2, 3, 4, 5, 6, 7); }
DEV s16x4 rdtr(unsigned a) { return __builtin_amdgcn_ds_read_tr16_b64_v4i16((LAS s16x4*)(size_t)a); }

#define FINEW 0
#define STAMP(kid, i)
#define SENTS 12
#define SB 5
#define TCH 28
#define TCH2 30
template <int BM_, int NC_, int WM_, int WN_, int MT_, int NT_, int RA_, int RB_, int KB_, int NS_, int P_, int KIND_, unsigned TRANS_>
struct Cfg {
  static constexpr int BM = BM_, NC = NC_, WM = WM_, WN = WN_, MT = MT_, NT = NT_, RA = RA_, RB = RB_, KB = KB_, NS = NS_, P = P_;
  static constexpr int KIND = KIND_;
  static constexpr unsigned TRANS = TRANS_;
  static constexpr bool STATS = KIND == 1, PERM = KIND == 1 || KIND == 3;
  static constexpr bool AFRAG = KIND == 3 || KIND == 0;
  static constexpr int NW = WM * WN, NTHR = NW * 64;
  static constexpr int ATILE = BM * 128, BSLOT = KIND_ == 2 ? 15360 : KB * NC * 16;
  static constexpr int BHF = KIND_ == 2 ? 4 * 224 : 4 * NC * 16;
  static constexpr int NA = BM * 8 / NTHR;
  static constexpr int CPW = KB * NC / NW;
  static constexpr int NB = (CPW + 63) / 64;
  static constexpr int NA1 = KB == 64 ? NA / 2 : (NA + 1) / 2, NB1 = KB == 64 ? (NB + 1) / 2 : NB;
  static constexpr int LDS_B0 = RA * ATILE, LDS_BYTES = RA * ATILE + RB * BSLOT;
  static constexpr int NTA = NS / 2;
#define RQN 7
#define RQD 8
  static constexpr int RQ1 = (MT * NT * RQN) / RQD;
  static_assert(BM * 8 % NTHR == 0, "A tile split");
  static_assert((KB * NC) % NW == 0, "B slot split");
  static_assert(P % 2 == 0 && (P / 2) % RA == 0 && (KB == 32 ? P % RB == 0 : (P / 2) % RB == 0), "period");
  static_assert(BM == WM * MT * 16, "M tiling");
  static_assert(NC * 8 == WN * NT * 16, "N tiling");
  DEV static constexpr int bswz(int k) {
    return NC == 28 ? 2 * ((k >> 3) & 1)
         : (NC % 16 == 8) ? 2 * (((k >> 1) & 1) | (((k >> 3) & 1) << 1))
                          : 2 * ((k & 3) | (((k >> 3) & 1) << 2));
  }
  static constexpr bool hasB(int t) {
    if (KB == 32) return t + RB >= 0 && t + RB < NS;
    int tile = (t & 1) ? ((t - 1) >> 1) + RB : ((t - 2) >> 1) + RB;
    return tile >= 0 && tile < NTA;
  }
  static constexpr bool hasA(int t) {
    int tile = (t & 1) ? ((t - 1) >> 1) + RA : ((t - 2) >> 1) + RA;
    return tile >= 0 && tile < NTA;
  }
  static constexpr int nB(int t) { return !hasB(t) ? 0 : KB == 32 ? NB : (t & 1) ? NB1 : NB - NB1; }
  static constexpr int nA(int t) { return !hasA(t) ? 0 : (t & 1) ? NA1 : NA - NA1; }
  static constexpr int T0 = -(2 * (RA > RB ? RA : RB) + 2);
  static constexpr int lastIssue() { int l = 0; for (int t = 0; t < NS; ++t) if (nA(t) + nB(t) > 0) l = t; return l; }
  static constexpr int XS = lastIssue() + 1;
  static constexpr int nX(int t) {
    if (KIND == 0) return t == XS ? 4 : 0;
    if (KIND == 1) return t == 8 ? 2 : 0;
    if (KIND == 2) return (t == TCH ? 1 : 0) + (t == TCH2 ? 1 : 0) + (t == SB ? 4 : t == SB + 1 ? 2 : 0) + (t == SENTS ? 2 : 0);
    return t == XS ? 2 : 0;
  }
  static constexpr int hk(int t) {
    if (KIND == 1) return nX(t) > 0 ? 2 : 0;
    return KIND == 2 && ((t >= SB && t <= SB + 4) || t == SENTS || t == TCH || t == TCH2) ? 2 : nX(t) > 0 ? 1 : 0;
  }
  static constexpr int between(int a, int b) { int n = 0; for (int t = a + 1; t < b; ++t) n += nA(t) + nB(t) + nX(t); return n; }
  static constexpr int TPRE = KB == 32 ? -3 : -4, RATE = KB == 32 ? 2 : 4;
  static constexpr int vlast(int s) { int v = TPRE + RATE * (s + 1); return s < 0 ? TPRE : (v < s ? v : s); }
  static constexpr int nhead() { int s = KIND == 1 ? 9 : 0; while (vlast(s - 1) != s - 1) ++s; return s; }
  static constexpr int H = nhead();
  static constexpr int TF = KB == 32 ? (-RB > -2 * RA + 2 ? -RB : -2 * RA + 2) : -2 * (RA < RB ? RA : RB) + 2;
  static constexpr int nfirst() { int n = 0; for (int t = T0; t <= TF; ++t) n += nA(t) + nB(t); return n; }
  static constexpr int npro() { int n = 0; for (int t = T0; t <= TPRE; ++t) n += nA(t) + nB(t); return n; }
  static constexpr int npro(int) { int n = 0; for (int t = TPRE + 1; t < 0; ++t) n += nA(t) + nB(t); return n; }
  static constexpr int nmain() {
    if (KIND == 2) return H;
    int lim = 0;
    while (lim < NS && hasA(lim) && hasB(lim)) ++lim;
    return H + ((lim - H) / P) * P;
  }
  static constexpr int vm_at(int s) {
    int posA[64] = {}, posB[128] = {}, cntA[64] = {}, cntB[128] = {};
    int tot = 0;
    for (int r = -1; r < s || r < 0; ++r) {
      if (r >= 0) tot += nX(r);
      for (int t = (r < 0 ? T0 : vlast(r - 1) + 1); t <= vlast(r); ++t) {
        const int tl = (t & 1) ? ((t - 1) >> 1) : ((t - 2) >> 1);
        if (nB(t)) { tot += nB(t); const int i = KB == 32 ? t + RB : tl + RB; posB[i] = tot; cntB[i] += nB(t); }
        if (nA(t)) { tot += nA(t); posA[tl + RA] = tot; cntA[tl + RA] += nA(t); }
      }
    }
    int nb = s + 1;
    if (nb >= NS) return 63;
    const int ib = KB == 32 ? nb : nb / 2;
    if (cntB[ib] != NB || cntA[nb / 2] != NA) return -1;
    int need = posB[ib] > posA[nb / 2] ? posB[ib] : posA[nb / 2];
    int v = tot - need;
    return v > 63 ? 63 : v;
  }
  static constexpr int NMAIN = nmain();
  static constexpr int vm_main(int u) {
    int v = 63;
    for (int s = H + u; s < NMAIN; s += P) { int x = vm_at(s); v = x < v ? x : v; }
    return v;
  }
};

template <class C> struct Loop {
  unsigned lbase;
  const char* abase;
  unsigned ga[C::NA];
  unsigned gbo[C::NB];
  const char* bbase;
  unsigned bdst;
  long bstep;
  const char *a0, *b0;
  unsigned aoff, boff;
  int sp;
  int wid, lane, wm, wn;
  unsigned ra[2][C::PERM ? C::MT : 1];
  unsigned rb[C::NT];
  bf16x8 FA[2][C::MT];
  s16x4 FBh[2][C::NT][2];
  f32x4 acc[C::MT][C::NT];
  f32x4 rs, rq;
  f32x4 cs[3];
  bf16x8 ones;

  template <int SET, int I> DEV void readOp(int aslot, int ks, int boff) {
    if constexpr (I < C::MT) {
      unsigned a = C::PERM ? ra[ks][C::PERM ? I : 0] : ra[ks][0] + I * (C::AFRAG ? 256 : 2048);
      FA[SET][I] = *(const LAS bf16x8*)(size_t)(a + aslot * C::ATILE);
    } else {
      constexpr int j = (I - C::MT) / 2, hf = (I - C::MT) & 1;
      FBh[SET][j][hf] = rdtr(rb[j] + boff + hf * C::BHF);
    }
  }
  static constexpr int rord(int i) {
    if (i == 0) return 0;
    if (i <= 2 * C::NT) return C::MT + i - 1;
    return i - 2 * C::NT;
  }
  template <int SET> DEV bf16x8 FB(int j) const { return cat(FBh[SET][j][0], FBh[SET][j][1]); }
  static constexpr int NMM = C::MT * C::NT, NR = C::MT + 2 * C::NT;
  template <int SET, int Q, bool Z> DEV void mmaOp() {
    if constexpr (Q < NMM) {
      constexpr int m = Q / C::NT, j = Q % C::NT;
      if constexpr ((C::TRANS >> j) & 1) mfma_a<Z>(acc[m][j], FB<SET>(j), FA[SET][m]);
      else mfma_a<Z>(acc[m][j], FA[SET][m], FB<SET>(j));
    } else if constexpr (Q == NMM) {
      mfma_a<Z>(rs, ones, FA[SET][0]);
    } else if constexpr (Q == NMM + 1) {
      mfma_a<Z>(rq, FA[SET][0], FA[SET][0]);
    } else {
      mfma_a<Z>(cs[Q - NMM - 2], ones, FB<SET>(Q - NMM - 2));
    }
  }
  template <int PAR, bool IB, int I> DEV void gldsOp(int bslot, int aslot) {
    constexpr int nb = !IB ? 0 : C::KB == 32 ? C::NB : PAR ? C::NB1 : C::NB - C::NB1;
    if constexpr (I < nb) {
      constexpr int i = (C::KB == 64 && !PAR) ? C::NB1 + I : I;
      constexpr int rem = C::CPW - 64 * i;
      const unsigned dst = bdst + bslot * C::BSLOT + i * 1024;
      if constexpr (rem >= 64) glds_s(gbo[i], bbase, dst);
      else glds_sm(gbo[i], bbase, dst, (1ull << (rem & 63)) - 1ull);
      if constexpr (i == C::NB - 1) {
        if constexpr (C::STATS) {
          boff += (unsigned)bstep; boff = boff == 16u * (unsigned)bstep ? 0u : boff; bbase = b0 + boff;
          asm volatile("" : "+s"(boff));
        } else bbase += bstep;
        asm volatile("" : "+s"(bbase));
      }
    } else {
      constexpr int i = (PAR ? 0 : C::NA1) + (I - nb);
      glds_s(ga[i], abase, lbase + aslot * C::ATILE + (wid * C::NA + i) * 1024);
      if constexpr (i == C::NA - 1) {
        if constexpr (C::STATS) { aoff = (aoff + 128u) & 2047u; abase = a0 + aoff; asm volatile("" : "+s"(aoff)); }
        else abase += C::AFRAG ? C::ATILE : 128;
        asm volatile("" : "+s"(abase));
      }
    }
  }
  static constexpr int spread(int N, int Q0, int Q1, int Q) { return Q <= Q0 ? 0 : Q >= Q1 ? N : (N * (Q - Q0) + (Q1 - Q0) - 1) / (Q1 - Q0); }
  template <int SET, int LO, int HI> DEV void readRange(int aslot, int ks, int boff) {
    if constexpr (LO < HI) { readOp<SET, rord(LO)>(aslot, ks, boff); readRange<SET, LO + 1, HI>(aslot, ks, boff); }
  }
  static constexpr int wslotA(int U) { return (((U & 1) ? (U - 1) / 2 : (U - 2 + 2 * C::P) / 2)) % C::RA; }
  static constexpr int wslotB(int U) { return C::KB == 32 ? U % C::RB : (((U & 1) ? (U - 1) / 2 : (U - 2 + 2 * C::P) / 2)) % C::RB; }
  static constexpr int rslotA(int U1) { return (U1 / 2) % C::RA; }
  static constexpr int roffB(int U1) { return C::KB == 32 ? (U1 % C::RB) * C::BSLOT : ((U1 / 2) % C::RB) * C::BSLOT + (U1 & 1) * 32 * C::NC * 16; }
  static constexpr int nops(int V, int VHI, bool MAIN) {
    int n = 0;
    for (int v = V; v <= VHI; ++v)
      n += MAIN ? ((C::KB == 32 ? C::NB : (v & 1) ? C::NB1 : C::NB - C::NB1) + ((v & 1) ? C::NA1 : C::NA - C::NA1)) : C::nB(v) + C::nA(v);
    return n;
  }
  template <int V, int VHI, bool MAIN, int I> DEV void vop() {
    if constexpr (V <= VHI) {
      constexpr int n = nops(V, V, MAIN), U = ((V % C::P) + C::P) % C::P;
      if constexpr (I < n) gldsOp<U & 1, MAIN || C::hasB(V), I>(wslotB(U), wslotA(U));
      else vop<V + 1, VHI, MAIN, I - n>();
    }
  }
  template <int V, int VHI, bool MAIN, int LO, int HI> DEV void vopRange() {
    if constexpr (LO < HI) { vop<V, VHI, MAIN, LO>(); vopRange<V, VHI, MAIN, LO + 1, HI>(); }
  }
  template <int CP, int U, int V, int VHI, bool MAIN, bool RN, int Q, int HS, class H> DEV void body(H& hook) {
    constexpr int NM = NMM + CP;
    if constexpr (Q < NM) {
      constexpr int PAR = U & 1;
      constexpr int NG = nops(V, VHI, MAIN);
      constexpr int RQ = C::RQ1 + (NM - NMM);
      mmaOp<PAR, Q, !MAIN && V == C::TPRE + 1>();
      vopRange<V, VHI, MAIN, spread(NG, 0, NM, Q), spread(NG, 0, NM, Q + 1)>();
      if constexpr (RN) readRange<PAR ^ 1, spread(NR, 0, RQ, Q), spread(NR, 0, RQ, Q + 1)>(rslotA(U + 1), PAR ^ 1, roffB(U + 1));
      if constexpr (HS >= 0) hook(IC<HS + Q>{});
      sched_fence();
      body<CP, U, V, VHI, MAIN, RN, Q + 1, HS>(hook);
    }
  }
  template <int CP, int S, int VM, bool MAIN, class H> DEV void step(H& hook) {
    constexpr int U = S % C::P;
    constexpr bool RN = MAIN || S + 1 < C::NS;
    if constexpr (RN && (C::KB == 32 || (U & 1))) {
      static_assert(VM >= 0, "schedule"); wait_vm<VM>(); bar();
    }
    sched_fence();
    constexpr int HM = MAIN ? 0 : C::hk(S);
    if constexpr (HM == 1) { hook(IC<S>{}); sched_fence(); }
    if constexpr (HM == 3) { if (wid >= C::NW / 2) hook(IC<S>{}); sched_fence(); }
    body<0, U, C::vlast(S - 1) + 1, C::vlast(S), MAIN, RN, 0, HM == 2 ? S * 100 : -1>(hook);
    if constexpr (C::STATS && !MAIN && S < 8) {
      if (sp == S / 2) {
        mmaOp<U & 1, NMM, false>(); mmaOp<U & 1, NMM + 1, false>(); mmaOp<U & 1, NMM + 2, false>();
        mmaOp<U & 1, NMM + 3, false>(); mmaOp<U & 1, NMM + 4, false>();
      }
      sched_fence();
    }
    if constexpr (HM == 3) { if (wid < C::NW / 2) hook(IC<S>{}); sched_fence(); }
    if constexpr (C::KB == 32 || !(U & 1)) wait_lgkm0();
    sched_fence();
  }
  template <int CP, int U, class H> DEV void mainSteps(H& hook) {
    if constexpr (U < C::P) { step<CP, C::H + U, C::vm_main(U), true>(hook); mainSteps<CP, U + 1>(hook); }
  }
  template <int CP, int Sx, int SE, class H> DEV void seqSteps(H& hook) {
    if constexpr (Sx < SE) { step<CP, Sx, C::vm_at(Sx), false>(hook); seqSteps<CP, Sx + 1, SE>(hook); }
  }
  template <int KID, int CP, class H> DEV void allSteps(H& hook) {
    sched_fence();
    ts();
    wait_vm<C::vm_at(-1)>();
    ts();
    bar();
    sched_fence();
    ts();
    readRange<0, 0, NR>(0, 0, 0);
    wait_lgkm0();
    sched_fence();
    STAMP(KID, 1);
    ts();
    asm volatile("s_nop 7" ::: "memory");
    sched_fence();
    seqSteps<CP, 0, C::H>(hook);
    for (int it = 0; it < (C::NMAIN - C::H) / C::P; ++it) mainSteps<CP, 0>(hook);
    seqSteps<CP, C::NMAIN, C::NS>(hook);
    asm volatile("s_nop 7\n\ts_nop 7\n\ts_nop 7" ::: "memory");
    sched_fence();
  }
  DEV void ts() {
  }
  template <int KID> DEV void tsdump() {
  }
  template <class BF> DEV void init0(LAS char* l, int lda, BF bsrc, int mperm) {
    lbase = (unsigned)(size_t)l;
    const int tid = threadIdx.x;
    wid = __builtin_amdgcn_readfirstlane(tid >> 6); lane = tid & 63;
    wm = wid / C::WN; wn = wid % C::WN;
    if (mperm < 0) mperm = wn;
#pragma unroll
    for (int i = 0; i < C::NA; ++i) {
      int q = (wid * C::NA + i) * 64 + lane, row = q >> 3, p = q & 7, c = p ^ ((row >> 1) & 7);
      ga[i] = C::AFRAG ? q * 16 : row * lda + c * 16;
      asm volatile("" : "+v"(ga[i]));
    }
#pragma unroll
    for (int i = 0; i < C::NB; ++i) {
      if constexpr (C::KIND == 2) {
        int q = i * 64 + lane;
        if (q >= 112) q = 111;
        const int k = q / 14, c = q - k * 14;
        gbo[i] = bsrc((wid & 3) * 8 + k, c);
      } else {
        const int q = wid * C::CPW + i * 64 + lane;
        const int k = q / C::NC, p = q % C::NC, idx = p ^ C::bswz(k);
        gbo[i] = bsrc(k, idx);
      }
      asm volatile("" : "+v"(gbo[i]));
    }
    bdst = lbase + C::LDS_B0 + (C::KIND == 2 ? wid * 1920 + (wid >> 2) * 16 : wid * C::CPW * 16);
    mp = mperm;
    sched_fence();
  }
  int mp;
  DEV void init2() {
    const int mperm = mp;
    const int fr = lane & 15, g = lane >> 4;
#pragma unroll
    for (int ks = 0; ks < 2; ++ks)
#pragma unroll
      for (int m = 0; m < (C::PERM ? C::MT : 1); ++m) {
        ra[ks][m] = C::AFRAG ? lbase + (4 * ks + g) * (C::BM * 16) + ((wm * C::MT + (C::PERM ? (m ^ mperm) : m)) * 16 + fr) * 16
                             : lbase + ((wm * C::MT + (C::PERM ? (m ^ mperm) : m)) * 16 + fr) * 128 + (((4 * ks + g) ^ (fr >> 1)) * 16);
        asm volatile("" : "+v"(ra[ks][m]));
      }
    const int q4 = fr >> 2, p4 = fr & 3, k = 8 * g + q4;
#pragma unroll
    for (int j = 0; j < C::NT; ++j) {
      if constexpr (C::KIND == 2) {
        const int mat = j < 3 ? 0 : j < 6 ? 1 : (p4 >> 1);
        const int ch = wn * 7 + (j < 6 ? 2 * (j % 3) + (p4 >> 1) : 6);
        rb[j] = lbase + C::LDS_B0 + (mat * 4 + g) * 1920 + mat * 16 + q4 * 224 + ch * 16 + 8 * (p4 & 1);
      } else {
        int idx;
        if (C::KIND == 1) idx = j * 8 + wn * 2 + (p4 >> 1);
        else idx = 2 * (wn * C::NT + j) + (p4 >> 1);
        rb[j] = lbase + C::LDS_B0 + k * C::NC * 16 + ((idx ^ C::bswz(k)) * 16) + 8 * (p4 & 1);
      }
      asm volatile("" : "+v"(rb[j]));
    }
    sched_fence();
  }
  DEV void init1(const char* Abase, const char* Bbase, long bstep_, int t0 = 0) {
    bstep = bstep_;
    abase = Abase; bbase = Bbase;
    if constexpr (C::STATS) {
      a0 = Abase; b0 = Bbase; aoff = t0 * 128; boff = t0 * (unsigned)bstep_;
      abase = a0 + aoff; bbase = b0 + boff;
    }
    vopRange<C::T0, C::TPRE, false, 0, C::nfirst()>();
    sched_fence();
    bar();
    sched_fence();
    vopRange<C::T0, C::TPRE, false, C::nfirst(), C::npro()>();
    sched_fence();
    init2();
    if constexpr (C::STATS) {
      u32x4 o4 = {0x3F803F80u, 0x3F803F80u, 0x3F803F80u, 0x3F803F80u};
      asm volatile("" : "+v"(o4));
      ones = __builtin_bit_cast(bf16x8, o4);
      rs = rq = cs[0] = cs[1] = cs[2] = (f32x4){0.f, 0.f, 0.f, 0.f};
      asm volatile("" : "+v"(rs), "+v"(rq), "+v"(cs[0]), "+v"(cs[1]), "+v"(cs[2]));
    }
  }
  template <int KID, class H> DEV void run(H hook) {
    allSteps<KID, 0>(hook);
    ts();
    STAMP(KID, 2);
  }
};

DEV u32x4 xpose2(u32x2 a, u32x2 b) {
  auto x = __builtin_amdgcn_permlane16_swap(a.x, b.x, false, false);
  auto y = __builtin_amdgcn_permlane16_swap(a.y, b.y, false, false);
  return (u32x4){x[0], y[0], x[1], y[1]};
}
#define STAUX 0
#define SC1M 29
#define AUXK(k) (((SC1M >> (k)) & 1) ? 16 : 0)
template <int AUX> DEV void st16(void* base, long byte_off, u32x4 v) {
  if constexpr (AUX == 0) *(u32x4*)((char*)base + byte_off) = v;
  else {
    const __amdgpu_buffer_rsrc_t r = __builtin_amdgcn_make_buffer_rsrc(base, 0, 0x7fffffff, 0x00020000);
    __builtin_amdgcn_raw_buffer_store_b128(v, r, (int)byte_off, 0, AUX);
  }
}
DEV u32x2 phi4(f32x4 u) {
  const f32x4 a = u * 1.4426950408889634f;
  f32x4 e, m;
#pragma unroll
  for (int r = 0; r < 4; ++r) { e[r] = __builtin_amdgcn_fmed3f(__builtin_amdgcn_exp2f(a[r]), 0.f, 1.f); m[r] = __builtin_fmaxf(u[r], 1e-6f); }
  const f32x4 p = m + e;
  u32x2 w; w.x = pk2(p[0], p[1]); w.y = pk2(p[2], p[3]);
  return w;
}

using C1 = Cfg<128, 24, 2, 4, 4, 3, 4, 4, 64, 32, 8, 1, 0x1u>;
static_assert(C1::XS == 25 && C1::LDS_BYTES == 163840, "K1 schedule");
constexpr int IB2 = 16 * 528, IBX = IB2 + 4224 + 128;
constexpr int AUX_I = 0, AUX_T = AUX_I + 256 * IBX, AUX_F = AUX_T + 4096, AUX_R = AUX_F + 65536, AUX_C = AUX_R + 262144,
              WS_O = AUX_C + 262144, WS_HS = WS_O + S_ * D_ * 2, WS_CS = WS_HS + S_ * 128,
              AUX_END = WS_CS + 4 * 2 * DF_ * 4, WS_H = AUX_END, WS_ACT = WS_H + S_ * D_ * 2, WS_X = WS_ACT + S_ * DF_ * 2;
static_assert(WS_O % 4096 == 0 && WS_H % 4096 == 0, "alignment");
constexpr int OOB = 0x7FFFFFF0;
DEV void ld2c(u32x2& d, unsigned voff, const void* sbase) {
  asm volatile("global_load_dwordx2 %0, %1, %2 sc1" : "=v"(d) : "v"(voff), "s"(sbase) : "memory");
}
template <int OFF> DEV void ld4c(f32x4& d, unsigned voff, const void* sbase) {
  asm volatile("global_load_dwordx4 %0, %1, %2 offset:%3 sc1" : "=v"(d) : "v"(voff), "s"(sbase), "n"(OFF) : "memory");
}
DEV void touchd(unsigned& d, unsigned voff, const void* sbase) {
  asm volatile("global_load_dword %0, %1, %2" : "+v"(d) : "v"(voff), "s"(sbase) : "memory");
}
DEV bool pend(u32x4 v) { return __any(max(max(v[0], v[1]), max(v[2], v[3])) == SENT); }
#define SL0 0
#define SL1 16
#define SL2 4
#define BK1 18
struct Svc {
  f32x4 q[28], c[4], sr, sc;
  u32x4 sel;
  unsigned wo_, ro, co;
  const char *cbase, *aux;
  template <int N> DEV void issue() {
    if constexpr (N < 32) {
      constexpr int p = N >> 1;
      ld4c<(p & 3) * 1024>(q[N % 28], (N & 1) ? co : ro, aux + (p >> 2) * 4096);
    } else {
      ld4<0>(q[N % 28], wo_, cbase + (long)(N - 32) * (4 * DF_ * 2));
    }
  }
  template <int N> DEV void use() {
    asm volatile("" : "+v"(q[N % 28]));
    if constexpr (N < 32) {
      f32x4& t = (N & 1) ? sc : sr;
#pragma unroll
      for (int r = 0; r < 4; ++r) t[r] = fadd1(t[r], q[N % 28][r]);
    } else {
      mfma_a<(N < 36)>(c[N & 3], __builtin_bit_cast(bf16x8, sel), __builtin_bit_cast(bf16x8, q[N % 28]));
    }
  }
  template <int A, int B> DEV void issues() { if constexpr (A < B) { issue<A>(); issues<A + 1, B>(); } }
  template <int A, int B> DEV void uses() { if constexpr (A < B) { use<A>(); uses<A + 1, B>(); } }
  template <int A, int B, int ISS> DEV void take() { if constexpr (A < B) { wait_vm<ISS - B>(); uses<A, B>(); sched_fence(); } }
  template <int A> DEV void roll() {
    if constexpr (A < 68) { take<A, A + 4, A + 28>(); issues<A + 28, A + 32>(); sched_fence(); roll<A + 4>(); }
  }
};
DEV void bars(int n) { for (int i = 0; i < n; ++i) bar(); sched_fence(); }
__global__ __launch_bounds__(C1::NTHR + 64) void k1_qkv(const u16* __restrict__ x, const u16* __restrict__ wqkv,
                                                        char* __restrict__ ws, const u16* __restrict__ wo,
                                                        const u16* __restrict__ wg, const u16* __restrict__ wu) {
  extern __shared__ __attribute__((aligned(16))) char smem[];
  LAS char* lds = (LAS char*)smem;
  const int bid = blockIdx.x, xcd = bid & 7, idx = bid >> 3;
  const int rb = (xcd & 1) * 8 + (idx & 7), hp = (xcd >> 1) * 4 + (idx >> 3);
  STAMP(0, 0);
  asm volatile("k1_code_begin:");
  LAS char* ep = lds;
  LAS float* sRa = (LAS float*)ep;
  LAS float* sRb = (LAS float*)(ep + 512);
  LAS float* sC = (LAS float*)(ep + 1024);
  LAS char* sF = ep + 4096;
  LAS char* sQ = lds + C1::LDS_B0;
  LAS char* sK = sQ + 16384;
  const int ib = AUX_I + (rb * 16 + hp) * IBX;
  if (__builtin_expect(__builtin_amdgcn_readfirstlane(threadIdx.x) >= C1::NTHR, 0)) {
#define STS()
    bar();
    const int lane = threadIdx.x & 63, fr = lane & 15, g = lane >> 4;
    const char* aux = ws;
    const __amdgpu_buffer_rsrc_t rx = __builtin_amdgcn_make_buffer_rsrc((void*)aux, 0, AUX_END, 0x00020000);
    const int pj = lane & 31, pc = pj < 16 ? rb * 16 + pj : (pj - 16) * 16 + hp;
    u32x2 tv, fl;
    unsigned td = 0;
    ld2c(tv, AUX_T + pc * 8, aux);
    const u32x4 s4 = {SENT, SENT, SENT, SENT};
    __builtin_amdgcn_raw_buffer_store_b128(s4, rx, AUX_R + (rb * 16 + hp) * 1024 + lane * 16, 0, 16);
    __builtin_amdgcn_raw_buffer_store_b128(s4, rx, AUX_C + (hp * 16 + rb) * 1024 + lane * 16, 0, 16);
#pragma unroll
    for (int i = 0; i < 13; ++i) __builtin_amdgcn_raw_buffer_store_b128(s4, rx, (i < 12 || lane < 24) ? ib + lane * 16 : OOB, i * 1024, 16);
    {
      const char* pc0; int back, csz;
      asm volatile("s_getpc_b64 %0\nk1_code_here:\n\ts_mov_b32 %1, k1_code_here-k1_code_begin\n\ts_mov_b32 %2, k4_code_end-k1_code_begin"
                   : "=s"(pc0), "=s"(back), "=s"(csz));
      const int f = (idx * 32 + pj) * 64;
      touchd(td, f < csz ? f : 0, pc0 - back);
    }
    Svc V;
    const int cu = (xcd & 1) * 28 + (idx < 28 ? idx : idx - 4), cg = cu >> 2, ccol = (xcd >> 1) * 896 + (cg < 7 ? cg : cg - 7) * 128;
    V.cbase = (const char*)(cg < 7 ? wg : wu) + ((long)(cu & 3) * 256 * DF_ + ccol) * 2;
    V.aux = aux;
    V.wo_ = g * (DF_ * 2) + fr * 16;
    V.ro = AUX_R + rb * 16384 + lane * 16;
    V.co = AUX_C + hp * 16384 + (lane < 48 ? lane : lane - 16) * 16;
#pragma unroll
    for (int j = 0; j < 4; ++j) V.sel[j] = fr == 2 * j ? 0x3F80u : fr == 2 * j + 1 ? 0x3F800000u : 0u;
    V.sr = V.sc = (f32x4){0.f, 0.f, 0.f, 0.f};
    asm volatile("" : "+v"(V.sel));
    sched_fence();
    bar();
    sched_fence();
    asm volatile("s_waitcnt vmcnt(0)" : "+v"(tv) :: "memory");
    tv.x = tv.x * 0x9E3779B1u + 0x7F4A7C15u;
    tv.y = tv.y * 0x85EBCA6Bu + 0xC2B2AE35u;
    __builtin_amdgcn_raw_buffer_store_b64(tv, rx, lane < 32 ? AUX_F + (pc * 32 + (pj < 16 ? hp : 16 + rb)) * 8 : OOB, 0, 16);
    const unsigned e0 = __builtin_amdgcn_readlane(tv.x, hp), e1 = __builtin_amdgcn_readlane(tv.y, hp);
    const unsigned lo = AUX_F + ((rb * 16 + hp) * 32 + pj) * 8;
    sched_fence();
    bars(5);
    ld2c(fl, lo, aux);
    bars(2);
    asm volatile("s_waitcnt vmcnt(0)" : "+v"(fl) :: "memory");
    if (!__all(fl.x == e0 && fl.y == e1)) {
      for (int spin = 0; spin < (1 << 18); ++spin) {
        ld2c(fl, lo, aux);
        asm volatile("s_waitcnt vmcnt(0)" : "+v"(fl) :: "memory");
        if (__all(fl.x == e0 && fl.y == e1)) break;
      }
    }
    sched_fence();
    bars(1); V.issues<0, 8>();
    bars(1); V.issues<8, 16>();
    bars(1); V.issues<16, 24>(); V.take<0, 8, 24>();
    bars(1); V.issues<24, 32>(); V.take<8, 16, 32>();
    bars(1); V.take<16, 24, 32>();
    bars(1); STS(); V.take<24, 32, 32>();
    if (__any(__builtin_isunordered(V.sr[0], V.sr[1]) || __builtin_isunordered(V.sr[2], V.sr[3]) ||
              __builtin_isunordered(V.sc[0], V.sc[1]) || __builtin_isunordered(V.sc[2], V.sc[3]))) {
      V.sr = V.sc = (f32x4){0.f, 0.f, 0.f, 0.f};
      for (int hf = 0; hf < 2; ++hf) {
        u32x4 a[16];
        for (int spin = 0; spin < (1 << 17); ++spin) {
          asm volatile("" ::: "memory");
#pragma unroll
          for (int p = 0; p < 16; ++p) a[p] = __builtin_amdgcn_raw_buffer_load_b128(rx, ((p & 1) ? V.co : V.ro) + hf * 8192, (p >> 1) * 1024, 16);
          u32x4 mx = a[0];
#pragma unroll
          for (int p = 1; p < 16; ++p) mx = __builtin_elementwise_max(mx, a[p]);
          if (!pend(mx)) break;
        }
#pragma unroll
        for (int p = 0; p < 16; p += 2) { V.sr += __builtin_bit_cast(f32x4, a[p]); V.sc += __builtin_bit_cast(f32x4, a[p + 1]); }
      }
    }
    {
      f32x2 ra, rm;
#pragma unroll
      for (int r = 0; r < 2; ++r) {
        const float nm = V.sr[2 * r] * (-1.f / D_);
        ra[r] = __builtin_amdgcn_rsqf(fmaxf(V.sr[2 * r + 1] * (1.f / D_) - nm * nm, 0.f) + 1e-5f);
        rm[r] = nm * ra[r];
      }
      *(LAS f32x2*)(sRa + 2 * lane) = ra;
      *(LAS f32x2*)(sRb + 2 * lane) = rm;
      *(LAS f32x4*)(sC + 4 * lane) = V.sc;
    }
    sched_fence();
#pragma unroll
    for (int i = 0; i < 8; ++i) touchd(td, (idx * 32 + i * 4 + g) * 2048 + (xcd >> 2) * 1024 + fr * 64, wo);
    STS();
    V.issues<32, 32 + BK1>();
    STS();
    lbar();
    sched_fence();
    STS();
    V.issues<32 + BK1, 60>();
    STS();
    bars(1);
    STS();
    V.roll<32>();
    STS();
    bars(1);
    STS();
    V.take<68, 96, 96>();
    STS();
    {
      asm volatile("s_nop 7\n\ts_nop 7\n\ts_nop 7" : "+v"(V.c[0]), "+v"(V.c[1]), "+v"(V.c[2]), "+v"(V.c[3]));
      const f32x4 t = (V.c[0] + V.c[1]) + (V.c[2] + V.c[3]);
      __builtin_amdgcn_raw_buffer_store_b128(__builtin_bit_cast(u32x4, t), rx,
          lane < (idx < 28 ? 32 : 0) ? WS_CS + ((cu & 3) * (2 * DF_) + (cg < 7 ? 0 : DF_) + ccol + fr * 8 + g * 4) * 4 : OOB, 0, AUXK(0));
      __builtin_amdgcn_raw_buffer_store_b64((u32x2){e0, e1}, rx, lane == 0 ? AUX_T + (rb * 16 + hp) * 8 : OOB, 0, 16);
    }
    asm volatile("" : "+v"(td));
    if (SL0) __builtin_amdgcn_s_sleep(SL0);
    {
      u32x4 v[9];
      const int io = ib + (lane >> 5) * 528 + pj * 16, zo = ib + fr * 528 + 512;
      unsigned pm = 0x1FF;
      for (int spin = 0; spin < (1 << 18) && pm; ++spin) {
        asm volatile("" ::: "memory");
#pragma unroll
        for (int i = 0; i < 9; ++i) if (pm >> i & 1) v[i] = __builtin_amdgcn_raw_buffer_load_b128(rx, i < 8 ? io : zo, i < 8 ? i * 1056 : 0, 16);
#pragma unroll
        for (int i = 0; i < 9; ++i) if ((pm >> i & 1) && !pend(v[i])) pm &= ~(1u << i);
        if (SL2 && pm) __builtin_amdgcn_s_sleep(SL2);
      }
      STS();
#define F4(i) __builtin_bit_cast(f32x4, v[i])
      const f32x4 t = ((F4(0) + F4(1)) + (F4(2) + F4(3))) + ((F4(4) + F4(5)) + (F4(6) + F4(7)));
      auto a = __builtin_amdgcn_permlane32_swap(__float_as_uint(t[0]), __float_as_uint(t[2]), false, false);
      auto b = __builtin_amdgcn_permlane32_swap(__float_as_uint(t[1]), __float_as_uint(t[3]), false, false);
      const unsigned w = pk2(__uint_as_float(a[0]) + __uint_as_float(a[1]), __uint_as_float(b[0]) + __uint_as_float(b[1]));
      auto c = __builtin_amdgcn_permlane32_swap(w, w, false, false);
      const int oo = AUX_I + ((lane >> 5) * 16 + hp) * IBX + IB2 + rb * 256 + pj * 8;
#pragma unroll
      for (int i = 0; i < 8; ++i) __builtin_amdgcn_raw_buffer_store_b64((u32x2){c[0], c[1]}, rx, oo, i * 32 * IBX, 16);
      f32x4 z = F4(8);
#undef F4
#pragma unroll
      for (int r = 0; r < 4; ++r) { z[r] += dpp<0x128>(z[r]); z[r] += dpp<0x124>(z[r]); z[r] += dpp<0x4E>(z[r]); z[r] += dpp<0xB1>(z[r]); }
      __builtin_amdgcn_raw_buffer_store_b64((u32x2){pk2(z[0], z[1]), pk2(z[2], z[3])}, rx,
                                            lane < 16 ? AUX_I + (lane * 16 + hp) * IBX + IB2 + 4096 + rb * 8 : OOB, 0, 16);
    }
    STS();
    if (SL1) __builtin_amdgcn_s_sleep(SL1);
    {
      u32x4 v[5];
      unsigned pm = 0x1F;
      for (int spin = 0; spin < (1 << 18) && pm; ++spin) {
        asm volatile("" ::: "memory");
#pragma unroll
        for (int i = 0; i < 5; ++i) if (pm >> i & 1) v[i] = __builtin_amdgcn_raw_buffer_load_b128(rx, ib + IB2 + (i < 4 ? lane : lane & 7) * 16, i * 1024, 16);
#pragma unroll
        for (int i = 0; i < 5; ++i) if ((pm >> i & 1) && !pend(v[i])) pm &= ~(1u << i);
        if (SL2 && pm) __builtin_amdgcn_s_sleep(SL2);
      }
      STS();
#pragma unroll
      for (int i = 0; i < 5; ++i) *(LAS u32x4*)(sK + i * 1024 + (i < 4 ? lane : lane & 7) * 16) = v[i];
    }
    lbar();
    return;
  }
  u32x8 ka = karg8();
  sched_fence();
  Loop<C1> L;
  auto bsrc = [&](int k, int c) -> unsigned { return (k * (3 * D_) + (c >> 3) * D_ + (c & 7) * 8) * 2; };
  L.init0(lds, D_ * 2, bsrc, -1);
  karg_wait(ka);
  L.sp = (rb + hp) & 3;
  L.init1(karg_ptr(ka, 0) + (long)rb * 128 * D_ * 2, karg_ptr(ka, 1) + hp * 128, 64L * 3 * D_ * 2, (((rb >> 2) + (hp >> 2)) & 3) * 4);
  const int lane = L.lane, fr = lane & 15, g = lane >> 4, wm = L.wm, wn = L.wn, wid = L.wid, pj = lane & 31;
  const char* aux = karg_ptr(ka, 2);
  const __amdgpu_buffer_rsrc_t rx = __builtin_amdgcn_make_buffer_rsrc((void*)aux, 0, AUX_END, 0x00020000);
  int ro, co;
  {
    ro = AUX_R + ((rb * 16 + hp) * 128 + wm * 64 + wn * 16 + fr) * 8;
    co = AUX_C + ((hp * 16 + rb) * 256 + g * 64 + wn * 16 + fr) * 4;
    asm volatile("" : "+v"(ro), "+v"(co));
    ro = g == (fr >> 2) ? ro : OOB;
    co = g < (wm ? 0 : 3) ? co : OOB;
    asm volatile("" : "+v"(ro), "+v"(co));
  }
  float w0, w1;
  L.run<0>([&](auto sx) {
    constexpr int sv = decltype(sx)::value;
    if constexpr (sv == 804) { w0 = fsel(fr & 2, fsel(fr & 1, L.rq[3], L.rq[2]), fsel(fr & 1, L.rq[1], L.rq[0])); asm volatile("" : "+v"(w0) : "v"(L.rs)); }
    if constexpr (sv == 806) __builtin_amdgcn_raw_buffer_store_b64((u32x2){__float_as_uint(L.rs[0]), __float_as_uint(w0)}, rx, ro, 0, 16);
    if constexpr (sv == 808) { w1 = fsel(g == 0, L.cs[0][0], fsel(g == 1, L.cs[1][0], L.cs[2][0])); asm volatile("" : "+v"(w1) : "v"(L.cs[0]), "v"(L.cs[1]), "v"(L.cs[2])); }
    if constexpr (sv == 810) __builtin_amdgcn_raw_buffer_store_b32(__float_as_uint(w1), rx, co, 0, 16);
  });
  {
    const int ik = 64 + wn * 16 + fr;
    const float ck = sC[ik], cv = sC[64 + ik];
#pragma unroll
    for (int m = 0; m < 4; ++m) {
      const int am = m ^ wn, r0 = wm * 64 + am * 16 + 4 * g;
      const f32x4 sa = *(const LAS f32x4*)(sRa + r0), sb = *(const LAS f32x4*)(sRb + r0);
      const f32x4 k = L.acc[m][1] * sa + (sb * ck + 1e-6f);
      const f32x4 v = L.acc[m][2] * sa + sb * cv;
      u32x2 a = phi4(k), b;
      b.x = pk2(v[0], v[1]); b.y = pk2(v[2], v[3]);
      LAS char* f = sF + (wn * 4 + wm * 2 + (am >> 1)) * 1024 + lane * 16 + (am & 1) * 8;
      *(LAS u32x2*)f = a;
      *(LAS u32x2*)(f + 16384) = b;
    }
  }
  L.ts();
  lbar();
  L.ts();
  {
    f32x4 d = {0.f, 0.f, 0.f, 0.f}, z = d;
#pragma unroll
    for (int G = 0; G < 4; ++G) {
      const bf16x8 fk = *(const LAS bf16x8*)(sF + (wn * 4 + G) * 1024 + lane * 16);
      const bf16x8 fv = *(const LAS bf16x8*)(sF + 16384 + (((wn & 2) + wm) * 4 + G) * 1024 + lane * 16);
      d = mfma16(fk, fv, d);
      z = mfma16(fk, L.ones, z);
    }
    int zo = AUX_I + ((wn * 4 + g) * 16 + hp) * IBX + rb * 528 + 512;
    asm volatile("" : "+v"(zo));
    __builtin_amdgcn_raw_buffer_store_b128(__builtin_bit_cast(u32x4, d), rx, AUX_I + ((2 * wid + (lane >> 5)) * 16 + hp) * IBX + rb * 528 + pj * 16, 0, 16);
    __builtin_amdgcn_raw_buffer_store_b128(__builtin_bit_cast(u32x4, z), rx, fr < (wm ? 0 : 1) ? zo : OOB, 0, 16);
  }
  sched_fence();
  L.ts();
  {
    const int ic = wn * 16 + 4 * g;
    const f32x4 c4 = *(const LAS f32x4*)(sC + ic);
#pragma unroll
    for (int m = 0; m < 4; ++m) {
      const int r = wm * 64 + (m ^ wn) * 16 + fr;
      *(LAS u32x2*)(sQ + ((wn >> 1) * 8 + wm * 4 + (m ^ wn)) * 1024 + lane * 16 + (wn & 1) * 8) = phi4(L.acc[m][0] * sRa[r] + (c4 * sRb[r] + 1e-6f));
    }
  }
  sched_fence();
  L.ts();
  {
    lbar();
    L.ts();
    bf16x8 fq[2];
#pragma unroll
    for (int m = 0; m < 2; ++m) fq[m] = *(const LAS bf16x8*)(sQ + (wid * 2 + m) * 1024 + lane * 16);
    u32x4 fa[3];
#pragma unroll
    for (int t = 0; t < 3; ++t) {
      LAS char* p = t < 2 ? sK + (t * 4 + wm * 2) * 512 + lane * 8 : sK + 4096 + (wm * 8 + g) * 8;
      const u32x2 lo = *(const LAS u32x2*)p, hi = *(const LAS u32x2*)(p + (t < 2 ? 512 : 32));
      fa[t] = (u32x4){lo.x, lo.y, hi.x, hi.y};
    }
    const f32x4 z4 = {0.f, 0.f, 0.f, 0.f};
#pragma unroll
    for (int m = 0; m < 2; ++m) {
      const f32x4 den = mfma16(__builtin_bit_cast(bf16x8, fa[2]), fq[m], z4);
      const float inv = __builtin_amdgcn_rcpf(den[0] + 1e-6f);
      u32x2 w[2];
#pragma unroll
      for (int t = 0; t < 2; ++t) {
        const f32x4 n = mfma16(__builtin_bit_cast(bf16x8, fa[t]), fq[m], z4) * inv;
        w[t].x = pk2(n[0], n[1]); w[t].y = pk2(n[2], n[3]);
      }
      __builtin_amdgcn_raw_buffer_store_b128(xpose2(w[0], w[1]), rx,
          WS_O + ((rb * 128 + (hp * 2 + wm) * 4 + (g & 1) * 2 + (g >> 1)) * 128 + (wn * 2 + m) * 16 + fr) * 16, 0, AUXK(0));
    }
  }
  L.ts();
  wait_vm<0>();
  L.ts();
  STAMP(0, 3);
  L.tsdump<0>();
}

using C2 = Cfg<128, 8, 2, 2, 4, 2, 4, 4, 64, 32, 8, 0, 0x3u>;
__global__ __launch_bounds__(C2::NTHR, 1) void k2_wo(const u16* __restrict__ o, const u16* __restrict__ wo, const u16* __restrict__ x,
                                                     u16* __restrict__ h, float* __restrict__ hstat) {
  extern __shared__ __attribute__((aligned(16))) char smem[];
  LAS char* lds = (LAS char*)smem;
  const int bid = blockIdx.x, xcd = bid & 7, idx = bid >> 3;
  const int rb = (xcd & 3) * 4 + (idx & 3), ct = (xcd >> 2) * 8 + (idx >> 2);
  STAMP(2, 0);
  u32x4 ka = karg4();
  sched_fence();
  Loop<C2> L;
  auto bsrc = [&](int k, int c) -> unsigned { return (k * D_ + c * 8) * 2; };
  L.init0(lds, D_ * 2, bsrc, 0);
  karg_wait(ka);
  L.init1(karg_ptr(ka, 0) + (long)rb * 128 * D_ * 2, karg_ptr(ka, 1) + ct * 128, 64L * D_ * 2);
  const int lane = L.lane, fr = lane & 15, g = lane >> 4, wm = L.wm, wn = L.wn;
  const long off0 = ((long)rb * 128 + wm * 64 + fr) * D_ + ct * 64 + wn * 32 + (g & 1) * 16 + (g >> 1) * 8;
  u32x4 xr[4];
  L.run<2>([&](auto) {
#pragma unroll
    for (int m = 0; m < 4; ++m) xr[m] = *(const u32x4*)(x + off0 + m * 16 * D_);
  });
  LAS float* sS = (LAS float*)lds;
#pragma unroll
  for (int m = 0; m < 4; ++m) {
    const int lr = wm * 64 + m * 16 + fr;
    float s = 0.f, q = 0.f;
    const u32x4 r = xpose2((u32x2){xr[m][0], xr[m][1]}, (u32x2){xr[m][2], xr[m][3]});
    u32x2 w[2];
#pragma unroll
    for (int j = 0; j < 2; ++j) {
      w[j].x = pk2(bflo(r[2 * j]) + L.acc[m][j][0], bfhi(r[2 * j]) + L.acc[m][j][1]);
      w[j].y = pk2(bflo(r[2 * j + 1]) + L.acc[m][j][2], bfhi(r[2 * j + 1]) + L.acc[m][j][3]);
      float f0 = bflo(w[j].x), f1 = bfhi(w[j].x), f2 = bflo(w[j].y), f3 = bfhi(w[j].y);
      s += (f0 + f1) + (f2 + f3);
      q += (f0 * f0 + f1 * f1) + (f2 * f2 + f3 * f3);
    }
    st16<AUXK(2)>(h, (off0 + m * 16 * D_) * 2, xpose2(w[0], w[1]));
    s += __shfl_xor(s, 16); s += __shfl_xor(s, 32);
    q += __shfl_xor(q, 16); q += __shfl_xor(q, 32);
    if (g == 0) *(LAS f32x2*)(sS + (wn * 128 + lr) * 2) = (f32x2){s, q};
  }
  lbar();
  if (threadIdx.x < 128) {
    const int r = threadIdx.x;
    f32x2 v = *(LAS f32x2*)(sS + r * 2) + *(LAS f32x2*)(sS + (128 + r) * 2);
    *(f32x2*)(hstat + (((long)rb * 128 + r) * 16 + ct) * 2) = v;
  }
  L.ts();
  STAMP(2, 3);
  L.tsdump<2>();
}

using C3 = Cfg<256, 28, 4, 2, 4, 7, 3, 4, 32, 32, 12, 2, 0x7Fu>;
constexpr int K3_LDS = C3::LDS_BYTES + 3072;
__global__ __launch_bounds__(C3::NTHR, 2) void k3_glu(const u16* __restrict__ h, const u16* __restrict__ wg, const u16* __restrict__ wu,
                                                      const float* __restrict__ hstat,
                                                      u16* __restrict__ act, const u16* __restrict__ wd, unsigned* __restrict__ xbuf) {
  extern __shared__ __attribute__((aligned(16))) char smem[];
  LAS char* lds = (LAS char*)smem;
  const int bid = blockIdx.x, xcd = bid & 7, idx = bid >> 3;
  const int rb = (xcd & 1) * 4 + (idx & 3), ct = (xcd >> 1) * 8 + (idx >> 2);
  const int tid = threadIdx.x;
  STAMP(3, 0);
  u32x8 ka = karg8();
  sched_fence();
  auto cmap = [](int c, bool& up) -> int {
    const int w = c >= 14, r = c - 14 * w;
    up = r >= 12 ? (r & 1) : r >= 6;
    return w * 7 + (r >= 12 ? 6 : r >= 6 ? r - 6 : r);
  };
  f32x4 hs[4];
  float cp[2];
  Loop<C3> L;
  L.ts();
  auto bsrc = [&](int k, int c) -> unsigned { return (k * DF_ + c * 8) * 2; };
  L.init0(lds, D_ * 2, bsrc, 0);
  L.ts();
  karg_wait(ka);
  L.ts();
  L.init1(karg_ptr(ka, 0) + (long)rb * 256 * D_ * 2, (L.wid >> 2 ? karg_ptr(ka, 2) : karg_ptr(ka, 1)) + ct * 224, 32L * DF_ * 2);
  const int lane = L.lane, fr = lane & 15, g = lane >> 4, wm = L.wm, wn = L.wn;
  LAS float* sC = (LAS float*)(lds + C3::LDS_BYTES);
  LAS float* sR = (LAS float*)(lds + C3::LDS_BYTES + 1024);
  const char* hsp = karg_ptr(ka, 3);
  unsigned ho = (rb * 256 + L.wid * 32) * 128 + lane * 16, co, to, sra, sca;
  f32x2 pp[4];
  float w8[8];
  {
    const int c = tid < 448 ? tid >> 1 : 0;
    bool up; const int ch = cmap(c >> 3, up);
    co = (S_ * 32 + (up ? DF_ : 0) + ct * 112 + ch * 8 + (c & 7) + (tid & 1) * (2 * 2 * DF_)) * 4;
    to = (((xcd & 1) * 4 + (xcd >> 1)) * 256 + idx * 8 + L.wid) * 3584 + (lane < 28 ? lane : 0) * 64;
    sca = (unsigned)(size_t)(sC + c);
    sra = (unsigned)(size_t)((lane & 4) ? sR + (L.wid * 32 + (lane & 3) * 8 + (lane >> 3)) * 2 : sC + 240);
    asm volatile("" : "+v"(ho), "+v"(co), "+v"(to), "+v"(sra), "+v"(sca));
  }
  L.run<3>([&](auto sx) {
    constexpr int sv = decltype(sx)::value;
    if constexpr (sv == SENTS * 100 + 12 || sv == SENTS * 100 + 26) stsent(bid * 16384 + tid * 16 + (sv == SENTS * 100 + 12 ? 0 : 8192), xbuf);
    constexpr int s0 = sv - SB * 100, su = s0 < 0 || s0 >= 500 ? -1 : s0 < 200 ? s0 + 2600 : s0 < 300 ? -1 : s0 + 2500;
    if constexpr (su >= 0) {
      if constexpr (su == 2602) ld4<0>(hs[0], ho, hsp);
      if constexpr (su == 2609) ld4<1024>(hs[1], ho, hsp);
      if constexpr (su == 2616) ld4<2048>(hs[2], ho, hsp);
      if constexpr (su == 2623) ld4<3072>(hs[3], ho, hsp);
      if constexpr (su == 2703) ld1(cp[0], co, hsp);
      if constexpr (su == 2717) ld1(cp[1], co, hsp + 1L * (2 * DF_ * 4));
      if constexpr (su == 2800) asm volatile("s_waitcnt vmcnt(8)" : "+v"(hs[0]), "+v"(hs[1]), "+v"(hs[2]), "+v"(hs[3]) :: "memory");
      if constexpr (su >= 2800 && su < 2804) {
        constexpr int i = su - 2800;
        pp[i] = (f32x2){hs[i][0], hs[i][1]} + (f32x2){hs[i][2], hs[i][3]};
        asm volatile("" : "+v"(pp[i]));
      }
      if constexpr (su >= 2804 && su < 2812) {
        constexpr int i = (su - 2804) >> 2, e = (su - 2804) & 1, gv = ((su - 2804) >> 1) & 1;
        w8[su - 2804] = fsel(lane & 1, pp[2 * i + 1 - gv][e], pp[2 * i + gv][e]);
        asm volatile("" : "+v"(w8[su - 2804]));
      }
      if constexpr (su >= 2812 && su < 2816) {
        constexpr int i = (su - 2812) >> 1, e = (su - 2812) & 1;
        pp[i][e] = w8[4 * i + e] + dpp<0xB1>(w8[4 * i + 2 + e]);
        asm volatile("" : "+v"(pp[i]));
      }
      if constexpr (su >= 2816 && su < 2820) {
        constexpr int e = (su - 2816) & 1, gv = (su - 2816) >> 1;
        w8[su - 2816] = fsel(lane & 2, pp[1 - gv][e], pp[gv][e]);
        asm volatile("" : "+v"(w8[su - 2816]));
      }
      if constexpr (su == 2820 || su == 2821) { w8[su - 2816] = w8[su - 2820] + dpp<0x4E>(w8[su - 2818]); asm volatile("" : "+v"(w8[su - 2816])); }
      if constexpr (su == 2823 || su == 2824) {
        w8[su - 2817] = (w8[su - 2819] + dpp<0x124>(w8[su - 2819])) * (1.f / D_); asm volatile("" : "+v"(w8[su - 2817]));
      }
      if constexpr (su == 2826) { w8[7] = fmaxf(w8[7] - w8[6] * w8[6], 0.f); asm volatile("" : "+v"(w8[7])); }
      if constexpr (su == 2900) *(LAS f32x2*)(size_t)sra = (f32x2){-w8[6], w8[7] + 1e-5f};
      if constexpr (su == 2902) {
        asm volatile("s_waitcnt vmcnt(8)" : "+v"(cp[0]), "+v"(cp[1]) :: "memory");
        cp[0] += cp[1]; asm volatile("" : "+v"(cp[0]));
      }
      if constexpr (su == 2906) { cp[0] += dpp<0xB1>(cp[0]); asm volatile("" : "+v"(cp[0])); }
      if constexpr (su == 2908) *(LAS float*)(size_t)sca = cp[0];
    }
    if constexpr (sv == TCH * 100) touchv(to, wd);
    if constexpr (sv == TCH2 * 100 + 1) touchv(to + 1792, wd);
  });
  static_assert(C3::XS == 28, "K3 schedule");
  {
    float nm[4];
#pragma unroll
    for (int m = 0; m < 4; ++m) nm[m] = sR[(wm * 64 + m * 16 + fr) * 2];
#pragma unroll
    for (int j = 0; j < 7; ++j) {
      const f32x4 c4 = *(const LAS f32x4*)(sC + (wn * 14 + 2 * j) * 8 + 4 * g);
#pragma unroll
      for (int m = 0; m < 4; ++m) L.acc[m][j] += c4 * nm[m];
    }
  }
  const int hi = lane >> 5;
  const int apo = ((rb * 2 + (wm >> 1)) * 448 + ct * 14 + wn * 7) * 2048 + ((wm & 1) * 64 + fr) * 16;
  constexpr float K1c = -2.f * 0.7978845608028654f * 1.4426950408889634f, K2c = K1c * 0.044715f;
  auto glu = [&](f32x4 a, f32x4 b, float q, float r1, float r3) -> u32x2 {
    const f32x4 z = a * ((a * a) * r3 + r1);
    f32x4 e;
#pragma unroll
    for (int r = 0; r < 4; ++r) e[r] = __builtin_amdgcn_exp2f(z[r]);
    e = e * q + q;
#pragma unroll
    for (int r = 0; r < 4; ++r) e[r] = __builtin_amdgcn_rcpf(e[r]);
    const f32x4 o = (a * b) * e;
    u32x2 w; w.x = pk2(o[0], o[1]); w.y = pk2(o[2], o[3]);
    return w;
  };
  u32x2 w6[2];
#pragma unroll
  for (int mp = 0; mp < 2; ++mp) {
    float q[2], r1[2], r3[2];
#pragma unroll
    for (int mm = 0; mm < 2; ++mm) {
      q[mm] = sR[(wm * 64 + (2 * mp + mm) * 16 + fr) * 2 + 1];
      const float rstd = __builtin_amdgcn_rsqf(q[mm]);
      r1[mm] = K1c * rstd; r3[mm] = K2c * rstd * rstd * rstd;
    }
#pragma unroll
    for (int j = 0; j < 3; ++j) {
      const u32x2 wa = glu(L.acc[2 * mp][j], L.acc[2 * mp][3 + j], q[0], r1[0], r3[0]);
      const u32x2 wb = glu(L.acc[2 * mp + 1][j], L.acc[2 * mp + 1][3 + j], q[1], r1[1], r3[1]);
      st16<AUXK(3)>(act, apo + (2 * j + (g >> 1)) * 2048 + (2 * mp + (g & 1)) * 256, xpose2(wa, wb));
    }
    f32x4 G, U;
#pragma unroll
    for (int r = 0; r < 4; ++r) {
      auto sw = __builtin_amdgcn_permlane32_swap(__float_as_uint(L.acc[2 * mp][6][r]), __float_as_uint(L.acc[2 * mp + 1][6][r]), false, false);
      G[r] = __uint_as_float(sw[0]); U[r] = __uint_as_float(sw[1]);
    }
    w6[mp] = glu(G, U, hi ? q[1] : q[0], hi ? r1[1] : r1[0], hi ? r3[1] : r3[0]);
  }
  st16<AUXK(3)>(act, apo + 6 * 2048 + (2 * (g & 1) + (g >> 1)) * 256, xpose2(w6[0], w6[1]));
  wait_vm<0>();
  L.ts();
  STAMP(3, 3);
  L.tsdump<3>();
}

using C4 = Cfg<128, 16, 2, 4, 4, 2, 4, 4, 64, 56, 8, 3, 0x3u>;
__global__ __launch_bounds__(C4::NTHR, 2) void k4_down(const u16* __restrict__ act, const u16* __restrict__ wd, const u16* __restrict__ h,
                                                       u16* __restrict__ out, unsigned* __restrict__ xbuf) {
  extern __shared__ __attribute__((aligned(16))) char smem[];
  LAS char* lds = (LAS char*)smem;
  const int bid = blockIdx.x, xcd = bid & 7, idx = bid >> 3;
  const int kh = xcd & 1, rb = (xcd >> 1) * 4 + (idx & 3), cb = idx >> 2;
  STAMP(4, 0);
  u32x4 ka = karg4();
  sched_fence();
  Loop<C4> L;
  auto bsrc = [&](int k, int c) -> unsigned { return (k * D_ + c * 8) * 2; };
  L.init0(lds, DF_ * 2, bsrc, 2 * kh);
  karg_wait(ka);
  L.init1(karg_ptr(ka, 0) + (long)(rb * 448 + kh * 224) * 2048, karg_ptr(ka, 1) + ((long)kh * 1792 * D_ + cb * 128) * 2, 64L * D_ * 2);
  const int lane = L.lane, fr = lane & 15, g = lane >> 4, wm = L.wm, wn = L.wn;
  const long off0 = ((long)rb * 128 + wm * 64 + kh * 32 + fr) * D_ + cb * 128 + wn * 32 + (g & 1) * 16 + (g >> 1) * 8;
  u32x4 xr[2];
  L.run<4>([&](auto) {
#pragma unroll
    for (int m = 0; m < 2; ++m) xr[m] = *(const u32x4*)(h + off0 + m * 16 * D_);
  });
  const __amdgpu_buffer_rsrc_t rx = __builtin_amdgcn_make_buffer_rsrc((void*)xbuf, 0, XBUF_BYTES, 0x00020000);
  const int xo = L.wid * 2048 + lane * 16;
#pragma unroll
  for (int i = 0; i < 2; ++i) {
    const f32x4 a = L.acc[2 + i][0], b = L.acc[2 + i][1];
    const u32x4 v = {pk2(a[0], a[1]), pk2(a[2], a[3]), pk2(b[0], b[1]), pk2(b[2], b[3])};
    __builtin_amdgcn_raw_buffer_store_b128(v, rx, bid * 16384 + xo + i * 1024, 0, 16);
  }
  u32x4 rv[2];
  for (int spin = 0; spin < (1 << 22); ++spin) {
    asm volatile("" ::: "memory");
    bool ok = true;
#pragma unroll
    for (int i = 0; i < 2; ++i) rv[i] = __builtin_amdgcn_raw_buffer_load_b128(rx, (bid ^ 1) * 16384 + xo + i * 1024, 0, 16);
#pragma unroll
    for (int i = 0; i < 2; ++i) ok = ok && rv[i][0] != SENT && rv[i][1] != SENT && rv[i][2] != SENT && rv[i][3] != SENT;
    if (__all(ok)) break;
  }
#pragma unroll
  for (int m = 0; m < 2; ++m) {
    const u32x4 r = xpose2((u32x2){xr[m][0], xr[m][1]}, (u32x2){xr[m][2], xr[m][3]});
    u32x2 w[2];
#pragma unroll
    for (int j = 0; j < 2; ++j) {
      const f32x4 a = L.acc[m][j];
      w[j].x = pk2(bflo(r[2 * j]) + (a[0] + bflo(rv[m][2 * j])), bfhi(r[2 * j]) + (a[1] + bfhi(rv[m][2 * j])));
      w[j].y = pk2(bflo(r[2 * j + 1]) + (a[2] + bflo(rv[m][2 * j + 1])), bfhi(r[2 * j + 1]) + (a[3] + bfhi(rv[m][2 * j + 1])));
    }
    st16<AUXK(4)>(out, (off0 + m * 16 * D_) * 2, xpose2(w[0], w[1]));
  }
  L.ts();
  STAMP(4, 3);
  L.tsdump<4>();
  asm volatile("k4_code_end:");
}

extern "C" void kernel_launch(void* const* d_in, const int* in_sizes, int n_in, void* d_out, int out_size, void* d_ws,
                              size_t ws_size, hipStream_t stream) {
  const u16* x = (const u16*)d_in[0];
  const u16* wqkv = (const u16*)d_in[1];
  const u16* wo = (const u16*)d_in[2];
  const u16* wg = (const u16*)d_in[4];
  const u16* wu = (const u16*)d_in[5];
  const u16* wd = (const u16*)d_in[6];
  u16* out = (u16*)d_out;
  char* ws = (char*)d_ws;
  u16* o = (u16*)(ws + WS_O);
  u16* h = (u16*)(ws + WS_H);
  u16* act = (u16*)(ws + WS_ACT);
  float* hstat = (float*)(ws + WS_HS);
  unsigned* xbuf = (unsigned*)(ws + WS_X);
  (void)hipFuncSetAttribute((const void*)k1_qkv, hipFuncAttributeMaxDynamicSharedMemorySize, C1::LDS_BYTES);
  (void)hipFuncSetAttribute((const void*)k2_wo, hipFuncAttributeMaxDynamicSharedMemorySize, C2::LDS_BYTES);
  (void)hipFuncSetAttribute((const void*)k3_glu, hipFuncAttributeMaxDynamicSharedMemorySize, K3_LDS);
  (void)hipFuncSetAttribute((const void*)k4_down, hipFuncAttributeMaxDynamicSharedMemorySize, C4::LDS_BYTES);
  k1_qkv<<<256, C1::NTHR + 64, C1::LDS_BYTES, stream>>>(x, wqkv, ws, wo, wg, wu);
  k2_wo<<<256, C2::NTHR, C2::LDS_BYTES, stream>>>(o, wo, x, h, hstat);
  k3_glu<<<256, C3::NTHR, K3_LDS, stream>>>(h, wg, wu, hstat, act, wd, xbuf);
  k4_down<<<256, C4::NTHR, C4::LDS_BYTES, stream>>>(act, wd, h, out, xbuf);
}
```

```cpp
#include <hip/hip_runtime.h>
#include <hip/hip_bf16.h>
#include <stdint.h>

typedef unsigned short u16;
typedef short bf16x8 __attribute__((ext_vector_type(8)));
typedef short s16x4 __attribute__((ext_vector_type(4)));
typedef float f32x4 __attribute__((ext_vector_type(4)));
typedef float f32x2 __attribute__((ext_vector_type(2)));
typedef unsigned u32x4 __attribute__((ext_vector_type(4)));
typedef unsigned u32x2 __attribute__((ext_vector_type(2)));
typedef __bf16 bf16x2_t __attribute__((ext_vector_type(2)));
#define LAS __attribute__((address_space(3)))
#define DEV __device__ __forceinline__
template <int V> struct IC { static constexpr int value = V; };

constexpr int S_ = 2048, D_ = 1024, DF_ = 3584, NH_ = 32;
constexpr int KVP = 1056;
constexpr unsigned SENT = 0xFFFFFFFFu;
constexpr int XBUF_BYTES = 256 * 16384;

DEV float bflo(unsigned v) { return __uint_as_float(v << 16); }
DEV float bfhi(unsigned v) { return __uint_as_float(v & 0xffff0000u); }
DEV unsigned pk2(float lo, float hi) {
  bf16x2_t r; r.x = (__bf16)lo; r.y = (__bf16)hi; return __builtin_bit_cast(unsigned, r);
}
template <int N> DEV void wait_vm() { asm volatile("s_waitcnt vmcnt(%0)" ::"n"(N) : "memory"); }
DEV void wait_lgkm0() { __builtin_amdgcn_s_waitcnt(0xC07F); }
DEV void bar() { __builtin_amdgcn_s_barrier(); }
DEV void lbar() { wait_lgkm0(); bar(); asm volatile("" ::: "memory"); }
DEV void sched_fence() { __builtin_amdgcn_sched_barrier(0); }
DEV void glds_v(const void* g, unsigned lds_addr) {
  asm volatile("s_mov_b32 m0, %1\n\ts_nop 0\n\tglobal_load_lds_dwordx4 %0, off" ::"v"(g), "s"(lds_addr) : "memory");
}
DEV void glds_vm(const void* g, unsigned lds_addr, unsigned long long mask) {
  asm volatile("s_mov_b32 m0, %1\n\ts_mov_b64 exec, %2\n\tglobal_load_lds_dwordx4 %0, off\n\ts_mov_b64 exec, -1"
               ::"v"(g), "s"(lds_addr), "s"(mask) : "memory");
}
DEV void glds_s(unsigned voff, const void* sbase, unsigned lds_addr) {
  asm volatile("s_mov_b32 m0, %2\n\ts_nop 0\n\tglobal_load_lds_dwordx4 %0, %1" ::"v"(voff), "s"(sbase), "s"(lds_addr) : "memory");
}
DEV void glds_sm(unsigned voff, const void* sbase, unsigned lds_addr, unsigned long long mask) {
  asm volatile("s_mov_b32 m0, %2\n\ts_mov_b64 exec, %3\n\tglobal_load_lds_dwordx4 %0, %1\n\ts_mov_b64 exec, -1"
               ::"v"(voff), "s"(sbase), "s"(lds_addr), "s"(mask) : "memory");
}
DEV void touch(const void* g, unsigned lds_addr) {
  asm volatile("s_mov_b32 m0, %1\n\ts_nop 0\n\tglobal_load_lds_dword %0, off" ::"v"(g), "s"(lds_addr) : "memory");
}
DEV void touchv(unsigned voff, const void* sbase) {
  asm volatile("global_load_dword v255, %0, %1" :: "v"(voff), "s"(sbase) : "memory", "v255");
}
typedef unsigned u32x8 __attribute__((ext_vector_type(8)));
DEV u32x4 karg4() { u32x4 r; asm volatile("s_load_dwordx4 %0, %1, 0x0" : "=s"(r) : "s"(__builtin_amdgcn_kernarg_segment_ptr())); return r; }
DEV u32x8 karg8() { u32x8 r; asm volatile("s_load_dwordx8 %0, %1, 0x0" : "=s"(r) : "s"(__builtin_amdgcn_kernarg_segment_ptr())); return r; }
template <class T> DEV void karg_wait(T& r) { asm volatile("s_waitcnt lgkmcnt(0)" : "+s"(r)); }
template <class T> DEV const char* karg_ptr(const T& r, int i) { return (const char*)(((unsigned long long)r[2 * i + 1] << 32) | r[2 * i]); }
template <int OFF> DEV void ld4(f32x4& d, unsigned voff, const void* sbase) {
  asm volatile("global_load_dwordx4 %0, %1, %2 offset:%3" : "=v"(d) : "v"(voff), "s"(sbase), "n"(OFF) : "memory");
}
DEV void ld1(float& d, unsigned voff, const void* sbase) {
  asm volatile("global_load_dword %0, %1, %2" : "=v"(d) : "v"(voff), "s"(sbase) : "memory");
}
template <int CTRL> DEV float dpp(float v) { return __int_as_float(__builtin_amdgcn_update_dpp(0, __float_as_int(v), CTRL, 0xF, 0xF, true)); }
#define SENTSC "sc1"
DEV void stsent(unsigned voff, const void* sbase) {
  const u32x4 sv = {SENT, SENT, SENT, SENT};
  asm volatile("global_store_dwordx4 %0, %1, %2 " SENTSC "\n\ts_nop 1" :: "v"(voff), "v"(sv), "s"(sbase) : "memory");
}
DEV float fsel(bool c, float a, float b) { return c ? a : b; }
DEV float fadd1(float a, float b) { float r; asm volatile("v_add_f32 %0, %1, %2" : "=v"(r) : "v"(a), "v"(b)); return r; }
DEV f32x4 mfma16(bf16x8 a, bf16x8 b, f32x4 c) { return __builtin_amdgcn_mfma_f32_16x16x32_bf16(a, b, c, 0, 0, 0); }
DEV void mfma_acc(f32x4& c, bf16x8 a, bf16x8 b) {
  asm volatile("v_mfma_f32_16x16x32_bf16 %0, %1, %2, %0" : "+v"(c) : "v"(a), "v"(b));
}
DEV void mfma_z(f32x4& c, bf16x8 a, bf16x8 b) {
  asm volatile("v_mfma_f32_16x16x32_bf16 %0, %1, %2, 0" : "=&v"(c) : "v"(a), "v"(b));
}
template <bool Z> DEV void mfma_a(f32x4& c, bf16x8 a, bf16x8 b) { if constexpr (Z) mfma_z(c, a, b); else mfma_acc(c, a, b); }
DEV bf16x8 cat(s16x4 a, s16x4 b) { return __builtin_shufflevector(a, b, 0, 1, 2, 3, 4, 5, 6, 7); }
DEV s16x4 rdtr(unsigned a) { return __builtin_amdgcn_ds_read_tr16_b64_v4i16((LAS s16x4*)(size_t)a); }

#define FINEW 0
#define STAMP(kid, i)
#define SENTS 12
#define SB 5
#define TCH 28
#define INVS 99
#define INVQ 4
#define TCH2 30
template <int BM_, int NC_, int WM_, int WN_, int MT_, int NT_, int RA_, int RB_, int KB_, int NS_, int P_, int KIND_, unsigned TRANS_>
struct Cfg {
  static constexpr int BM = BM_, NC = NC_, WM = WM_, WN = WN_, MT = MT_, NT = NT_, RA = RA_, RB = RB_, KB = KB_, NS = NS_, P = P_;
  static constexpr int KIND = KIND_;
  static constexpr unsigned TRANS = TRANS_;
  static constexpr bool STATS = KIND == 1, PERM = KIND == 1 || KIND == 3;
  static constexpr bool AFRAG = KIND == 3 || KIND == 0;
  static constexpr int NW = WM * WN, NTHR = NW * 64;
  static constexpr int ATILE = BM * 128, BSLOT = KIND_ == 2 ? 15360 : KB * NC * 16;
  static constexpr int BHF = KIND_ == 2 ? 4 * 224 : 4 * NC * 16;
  static constexpr int NA = BM * 8 / NTHR;
  static constexpr int CPW = KB * NC / NW;
  static constexpr int NB = (CPW + 63) / 64;
  static constexpr int NA1 = KB == 64 ? NA / 2 : (NA + 1) / 2, NB1 = KB == 64 ? (NB + 1) / 2 : NB;
  static constexpr int LDS_B0 = RA * ATILE, LDS_BYTES = RA * ATILE + RB * BSLOT;
  static constexpr int NTA = NS / 2;
#define RQN 7
#define RQD 8
  static constexpr int RQ1 = (MT * NT * RQN) / RQD;
  static_assert(BM * 8 % NTHR == 0, "A tile split");
  static_assert((KB * NC) % NW == 0, "B slot split");
  static_assert(P % 2 == 0 && (P / 2) % RA == 0 && (KB == 32 ? P % RB == 0 : (P / 2) % RB == 0), "period");
  static_assert(BM == WM * MT * 16, "M tiling");
  static_assert(NC * 8 == WN * NT * 16, "N tiling");
  DEV static constexpr int bswz(int k) {
    return NC == 28 ? 2 * ((k >> 3) & 1)
         : (NC % 16 == 8) ? 2 * (((k >> 1) & 1) | (((k >> 3) & 1) << 1))
                          : 2 * ((k & 3) | (((k >> 3) & 1) << 2));
  }
  static constexpr bool hasB(int t) {
    if (KB == 32) return t + RB >= 0 && t + RB < NS;
    int tile = (t & 1) ? ((t - 1) >> 1) + RB : ((t - 2) >> 1) + RB;
    return tile >= 0 && tile < NTA;
  }
  static constexpr bool hasA(int t) {
    int tile = (t & 1) ? ((t - 1) >> 1) + RA : ((t - 2) >> 1) + RA;
    return tile >= 0 && tile < NTA;
  }
  static constexpr int nB(int t) { return !hasB(t) ? 0 : KB == 32 ? NB : (t & 1) ? NB1 : NB - NB1; }
  static constexpr int nA(int t) { return !hasA(t) ? 0 : (t & 1) ? NA1 : NA - NA1; }
  static constexpr int T0 = -(2 * (RA > RB ? RA : RB) + 2);
  static constexpr int lastIssue() { int l = 0; for (int t = 0; t < NS; ++t) if (nA(t) + nB(t) > 0) l = t; return l; }
  static constexpr int XS = lastIssue() + 1;
  static constexpr int nX(int t) {
    if (KIND == 0) return t == XS ? 4 : 0;
    if (KIND == 1) return t == 8 ? 2 : 0;
    if (KIND == 2) return (t == TCH ? 1 : 0) + (t == TCH2 ? 1 : 0) + (t == INVS ? 1 : 0) + (t == SB ? 4 : t == SB + 1 ? 2 : 0) + (t == SENTS ? 2 : 0);
    return t == XS ? 2 : 0;
  }
  static constexpr int hk(int t) {
    if (KIND == 1) return nX(t) > 0 ? 2 : 0;
    return KIND == 2 && ((t >= SB && t <= SB + 4) || t == SENTS || t == TCH || t == TCH2 || t == INVS) ? 2 : nX(t) > 0 ? 1 : 0;
  }
  static constexpr int between(int a, int b) { int n = 0; for (int t = a + 1; t < b; ++t) n += nA(t) + nB(t) + nX(t); return n; }
  static constexpr int TPRE = KB == 32 ? -3 : -4, RATE = KB == 32 ? 2 : 4;
  static constexpr int vlast(int s) { int v = TPRE + RATE * (s + 1); return s < 0 ? TPRE : (v < s ? v : s); }
  static constexpr int nhead() { int s = KIND == 1 ? 9 : 0; while (vlast(s - 1) != s - 1) ++s; return s; }
  static constexpr int H = nhead();
  static constexpr int TF = KB == 32 ? (-RB > -2 * RA + 2 ? -RB : -2 * RA + 2) : -2 * (RA < RB ? RA : RB) + 2;
  static constexpr int nfirst() { int n = 0; for (int t = T0; t <= TF; ++t) n += nA(t) + nB(t); return n; }
  static constexpr int npro() { int n = 0; for (int t = T0; t <= TPRE; ++t) n += nA(t) + nB(t); return n; }
  static constexpr int npro(int) { int n = 0; for (int t = TPRE + 1; t < 0; ++t) n += nA(t) + nB(t); return n; }
  static constexpr int nmain() {
    if (KIND == 2) return H;
    int lim = 0;
    while (lim < NS && hasA(lim) && hasB(lim)) ++lim;
    return H + ((lim - H) / P) * P;
  }
  static constexpr int vm_at(int s) {
    int posA[64] = {}, posB[128] = {}, cntA[64] = {}, cntB[128] = {};
    int tot = 0;
    for (int r = -1; r < s || r < 0; ++r) {
      if (r >= 0) tot += nX(r);
      for (int t = (r < 0 ? T0 : vlast(r - 1) + 1); t <= vlast(r); ++t) {
        const int tl = (t & 1) ? ((t - 1) >> 1) : ((t - 2) >> 1);
        if (nB(t)) { tot += nB(t); const int i = KB == 32 ? t + RB : tl + RB; posB[i] = tot; cntB[i] += nB(t); }
        if (nA(t)) { tot += nA(t); posA[tl + RA] = tot; cntA[tl + RA] += nA(t); }
      }
    }
    int nb = s + 1;
    if (nb >= NS) return 63;
    const int ib = KB == 32 ? nb : nb / 2;
    if (cntB[ib] != NB || cntA[nb / 2] != NA) return -1;
    int need = posB[ib] > posA[nb / 2] ? posB[ib] : posA[nb / 2];
    int v = tot - need;
    return v > 63 ? 63 : v;
  }
  static constexpr int NMAIN = nmain();
  static constexpr int vm_main(int u) {
    int v = 63;
    for (int s = H + u; s < NMAIN; s += P) { int x = vm_at(s); v = x < v ? x : v; }
    return v;
  }
};

template <class C> struct Loop {
  unsigned lbase;
  const char* abase;
  unsigned ga[C::NA];
  unsigned gbo[C::NB];
  const char* bbase;
  unsigned bdst;
  long bstep;
  const char *a0, *b0;
  unsigned aoff, boff;
  int sp;
  int wid, lane, wm, wn;
  unsigned ra[2][C::PERM ? C::MT : 1];
  unsigned rb[C::NT];
  bf16x8 FA[2][C::MT];
  s16x4 FBh[2][C::NT][2];
  f32x4 acc[C::MT][C::NT];
  f32x4 rs, rq;
  f32x4 cs[3];
  bf16x8 ones;

  template <int SET, int I> DEV void readOp(int aslot, int ks, int boff) {
    if constexpr (I < C::MT) {
      unsigned a = C::PERM ? ra[ks][C::PERM ? I : 0] : ra[ks][0] + I * (C::AFRAG ? 256 : 2048);
      FA[SET][I] = *(const LAS bf16x8*)(size_t)(a + aslot * C::ATILE);
    } else {
      constexpr int j = (I - C::MT) / 2, hf = (I - C::MT) & 1;
      FBh[SET][j][hf] = rdtr(rb[j] + boff + hf * C::BHF);
    }
  }
  static constexpr int rord(int i) {
    if (i == 0) return 0;
    if (i <= 2 * C::NT) return C::MT + i - 1;
    return i - 2 * C::NT;
  }
  template <int SET> DEV bf16x8 FB(int j) const { return cat(FBh[SET][j][0], FBh[SET][j][1]); }
  static constexpr int NMM = C::MT * C::NT, NR = C::MT + 2 * C::NT;
  template <int SET, int Q, bool Z> DEV void mmaOp() {
    if constexpr (Q < NMM) {
      constexpr int m = Q / C::NT, j = Q % C::NT;
      if constexpr ((C::TRANS >> j) & 1) mfma_a<Z>(acc[m][j], FB<SET>(j), FA[SET][m]);
      else mfma_a<Z>(acc[m][j], FA[SET][m], FB<SET>(j));
    } else if constexpr (Q == NMM) {
      mfma_a<Z>(rs, ones, FA[SET][0]);
    } else if constexpr (Q == NMM + 1) {
      mfma_a<Z>(rq, FA[SET][0], FA[SET][0]);
    } else {
      mfma_a<Z>(cs[Q - NMM - 2], ones, FB<SET>(Q - NMM - 2));
    }
  }
  template <int PAR, bool IB, int I> DEV void gldsOp(int bslot, int aslot) {
    constexpr int nb = !IB ? 0 : C::KB == 32 ? C::NB : PAR ? C::NB1 : C::NB - C::NB1;
    if constexpr (I < nb) {
      constexpr int i = (C::KB == 64 && !PAR) ? C::NB1 + I : I;
      constexpr int rem = C::CPW - 64 * i;
      const unsigned dst = bdst + bslot * C::BSLOT + i * 1024;
      if constexpr (rem >= 64) glds_s(gbo[i], bbase, dst);
      else glds_sm(gbo[i], bbase, dst, (1ull << (rem & 63)) - 1ull);
      if constexpr (i == C::NB - 1) {
        if constexpr (C::STATS) {
          boff += (unsigned)bstep; boff = boff == 16u * (unsigned)bstep ? 0u : boff; bbase = b0 + boff;
          asm volatile("" : "+s"(boff));
        } else bbase += bstep;
        asm volatile("" : "+s"(bbase));
      }
    } else {
      constexpr int i = (PAR ? 0 : C::NA1) + (I - nb);
      glds_s(ga[i], abase, lbase + aslot * C::ATILE + (wid * C::NA + i) * 1024);
      if constexpr (i == C::NA - 1) {
        if constexpr (C::STATS) { aoff = (aoff + 128u) & 2047u; abase = a0 + aoff; asm volatile("" : "+s"(aoff)); }
        else abase += C::AFRAG ? C::ATILE : 128;
        asm volatile("" : "+s"(abase));
      }
    }
  }
  static constexpr int spread(int N, int Q0, int Q1, int Q) { return Q <= Q0 ? 0 : Q >= Q1 ? N : (N * (Q - Q0) + (Q1 - Q0) - 1) / (Q1 - Q0); }
  template <int SET, int LO, int HI> DEV void readRange(int aslot, int ks, int boff) {
    if constexpr (LO < HI) { readOp<SET, rord(LO)>(aslot, ks, boff); readRange<SET, LO + 1, HI>(aslot, ks, boff); }
  }
  static constexpr int wslotA(int U) { return (((U & 1) ? (U - 1) / 2 : (U - 2 + 2 * C::P) / 2)) % C::RA; }
  static constexpr int wslotB(int U) { return C::KB == 32 ? U % C::RB : (((U & 1) ? (U - 1) / 2 : (U - 2 + 2 * C::P) / 2)) % C::RB; }
  static constexpr int rslotA(int U1) { return (U1 / 2) % C::RA; }
  static constexpr int roffB(int U1) { return C::KB == 32 ? (U1 % C::RB) * C::BSLOT : ((U1 / 2) % C::RB) * C::BSLOT + (U1 & 1) * 32 * C::NC * 16; }
  static constexpr int nops(int V, int VHI, bool MAIN) {
    int n = 0;
    for (int v = V; v <= VHI; ++v)
      n += MAIN ? ((C::KB == 32 ? C::NB : (v & 1) ? C::NB1 : C::NB - C::NB1) + ((v & 1) ? C::NA1 : C::NA - C::NA1)) : C::nB(v) + C::nA(v);
    return n;
  }
  template <int V, int VHI, bool MAIN, int I> DEV void vop() {
    if constexpr (V <= VHI) {
      constexpr int n = nops(V, V, MAIN), U = ((V % C::P) + C::P) % C::P;
      if constexpr (I < n) gldsOp<U & 1, MAIN || C::hasB(V), I>(wslotB(U), wslotA(U));
      else vop<V + 1, VHI, MAIN, I - n>();
    }
  }
  template <int V, int VHI, bool MAIN, int LO, int HI> DEV void vopRange() {
    if constexpr (LO < HI) { vop<V, VHI, MAIN, LO>(); vopRange<V, VHI, MAIN, LO + 1, HI>(); }
  }
  template <int CP, int U, int V, int VHI, bool MAIN, bool RN, int Q, int HS, class H> DEV void body(H& hook) {
    constexpr int NM = NMM + CP;
    if constexpr (Q < NM) {
      constexpr int PAR = U & 1;
      constexpr int NG = nops(V, VHI, MAIN);
      constexpr int RQ = C::RQ1 + (NM - NMM);
      mmaOp<PAR, Q, !MAIN && V == C::TPRE + 1>();
      vopRange<V, VHI, MAIN, spread(NG, 0, NM, Q), spread(NG, 0, NM, Q + 1)>();
      if constexpr (RN) readRange<PAR ^ 1, spread(NR, 0, RQ, Q), spread(NR, 0, RQ, Q + 1)>(rslotA(U + 1), PAR ^ 1, roffB(U + 1));
      if constexpr (HS >= 0) hook(IC<HS + Q>{});
      sched_fence();
      body<CP, U, V, VHI, MAIN, RN, Q + 1, HS>(hook);
    }
  }
  template <int CP, int S, int VM, bool MAIN, class H> DEV void step(H& hook) {
    constexpr int U = S % C::P;
    constexpr bool RN = MAIN || S + 1 < C::NS;
    if constexpr (RN && (C::KB == 32 || (U & 1))) {
      static_assert(VM >= 0, "schedule"); wait_vm<VM>(); bar();
    }
    sched_fence();
    constexpr int HM = MAIN ? 0 : C::hk(S);
    if constexpr (HM == 1) { hook(IC<S>{}); sched_fence(); }
    if constexpr (HM == 3) { if (wid >= C::NW / 2) hook(IC<S>{}); sched_fence(); }
    body<0, U, C::vlast(S - 1) + 1, C::vlast(S), MAIN, RN, 0, HM == 2 ? S * 100 : -1>(hook);
    if constexpr (C::STATS && !MAIN && S < 8) {
      if (sp == S / 2) {
        mmaOp<U & 1, NMM, false>(); mmaOp<U & 1, NMM + 1, false>(); mmaOp<U & 1, NMM + 2, false>();
        mmaOp<U & 1, NMM + 3, false>(); mmaOp<U & 1, NMM + 4, false>();
      }
      sched_fence();
    }
    if constexpr (HM == 3) { if (wid < C::NW / 2) hook(IC<S>{}); sched_fence(); }
    if constexpr (C::KB == 32 || !(U & 1)) wait_lgkm0();
    sched_fence();
  }
  template <int CP, int U, class H> DEV void mainSteps(H& hook) {
    if constexpr (U < C::P) { step<CP, C::H + U, C::vm_main(U), true>(hook); mainSteps<CP, U + 1>(hook); }
  }
  template <int CP, int Sx, int SE, class H> DEV void seqSteps(H& hook) {
    if constexpr (Sx < SE) { step<CP, Sx, C::vm_at(Sx), false>(hook); seqSteps<CP, Sx + 1, SE>(hook); }
  }
  template <int KID, int CP, class H> DEV void allSteps(H& hook) {
    sched_fence();
    ts();
    wait_vm<C::vm_at(-1)>();
    ts();
    bar();
    sched_fence();
    ts();
    readRange<0, 0, NR>(0, 0, 0);
    wait_lgkm0();
    sched_fence();
    STAMP(KID, 1);
    ts();
    asm volatile("s_nop 7" ::: "memory");
    sched_fence();
    seqSteps<CP, 0, C::H>(hook);
    for (int it = 0; it < (C::NMAIN - C::H) / C::P; ++it) mainSteps<CP, 0>(hook);
    seqSteps<CP, C::NMAIN, C::NS>(hook);
    asm volatile("s_nop 7\n\ts_nop 7\n\ts_nop 7" ::: "memory");
    sched_fence();
  }
  DEV void ts() {
  }
  template <int KID> DEV void tsdump() {
  }
  template <class BF> DEV void init0(LAS char* l, int lda, BF bsrc, int mperm) {
    lbase = (unsigned)(size_t)l;
    const int tid = threadIdx.x;
    wid = __builtin_amdgcn_readfirstlane(tid >> 6); lane = tid & 63;
    wm = wid / C::WN; wn = wid % C::WN;
    if (mperm < 0) mperm = wn;
#pragma unroll
    for (int i = 0; i < C::NA; ++i) {
      int q = (wid * C::NA + i) * 64 + lane, row = q >> 3, p = q & 7, c = p ^ ((row >> 1) & 7);
      ga[i] = C::AFRAG ? q * 16 : row * lda + c * 16;
      asm volatile("" : "+v"(ga[i]));
    }
#pragma unroll
    for (int i = 0; i < C::NB; ++i) {
      if constexpr (C::KIND == 2) {
        int q = i * 64 + lane;
        if (q >= 112) q = 111;
        const int k = q / 14, c = q - k * 14;
        gbo[i] = bsrc((wid & 3) * 8 + k, c);
      } else {
        const int q = wid * C::CPW + i * 64 + lane;
        const int k = q / C::NC, p = q % C::NC, idx = p ^ C::bswz(k);
        gbo[i] = bsrc(k, idx);
      }
      asm volatile("" : "+v"(gbo[i]));
    }
    bdst = lbase + C::LDS_B0 + (C::KIND == 2 ? wid * 1920 + (wid >> 2) * 16 : wid * C::CPW * 16);
    mp = mperm;
    sched_fence();
  }
  int mp;
  DEV void init2() {
    const int mperm = mp;
    const int fr = lane & 15, g = lane >> 4;
#pragma unroll
    for (int ks = 0; ks < 2; ++ks)
#pragma unroll
      for (int m = 0; m < (C::PERM ? C::MT : 1); ++m) {
        ra[ks][m] = C::AFRAG ? lbase + (4 * ks + g) * (C::BM * 16) + ((wm * C::MT + (C::PERM ? (m ^ mperm) : m)) * 16 + fr) * 16
                             : lbase + ((wm * C::MT + (C::PERM ? (m ^ mperm) : m)) * 16 + fr) * 128 + (((4 * ks + g) ^ (fr >> 1)) * 16);
        asm volatile("" : "+v"(ra[ks][m]));
      }
    const int q4 = fr >> 2, p4 = fr & 3, k = 8 * g + q4;
#pragma unroll
    for (int j = 0; j < C::NT; ++j) {
      if constexpr (C::KIND == 2) {
        const int mat = j < 3 ? 0 : j < 6 ? 1 : (p4 >> 1);
        const int ch = wn * 7 + (j < 6 ? 2 * (j % 3) + (p4 >> 1) : 6);
        rb[j] = lbase + C::LDS_B0 + (mat * 4 + g) * 1920 + mat * 16 + q4 * 224 + ch * 16 + 8 * (p4 & 1);
      } else {
        int idx;
        if (C::KIND == 1) idx = j * 8 + wn * 2 + (p4 >> 1);
        else idx = 2 * (wn * C::NT + j) + (p4 >> 1);
        rb[j] = lbase + C::LDS_B0 + k * C::NC * 16 + ((idx ^ C::bswz(k)) * 16) + 8 * (p4 & 1);
      }
      asm volatile("" : "+v"(rb[j]));
    }
    sched_fence();
  }
  DEV void init1(const char* Abase, const char* Bbase, long bstep_, int t0 = 0) {
    bstep = bstep_;
    abase = Abase; bbase = Bbase;
    if constexpr (C::STATS) {
      a0 = Abase; b0 = Bbase; aoff = t0 * 128; boff = t0 * (unsigned)bstep_;
      abase = a0 + aoff; bbase = b0 + boff;
    }
    vopRange<C::T0, C::TPRE, false, 0, C::nfirst()>();
    sched_fence();
    bar();
    sched_fence();
    vopRange<C::T0, C::TPRE, false, C::nfirst(), C::npro()>();
    sched_fence();
    init2();
    if constexpr (C::STATS) {
      u32x4 o4 = {0x3F803F80u, 0x3F803F80u, 0x3F803F80u, 0x3F803F80u};
      asm volatile("" : "+v"(o4));
      ones = __builtin_bit_cast(bf16x8, o4);
      rs = rq = cs[0] = cs[1] = cs[2] = (f32x4){0.f, 0.f, 0.f, 0.f};
      asm volatile("" : "+v"(rs), "+v"(rq), "+v"(cs[0]), "+v"(cs[1]), "+v"(cs[2]));
    }
  }
  template <int KID, class H> DEV void run(H hook) {
    allSteps<KID, 0>(hook);
    ts();
    STAMP(KID, 2);
  }
};

DEV u32x4 xpose2(u32x2 a, u32x2 b) {
  auto x = __builtin_amdgcn_permlane16_swap(a.x, b.x, false, false);
  auto y = __builtin_amdgcn_permlane16_swap(a.y, b.y, false, false);
  return (u32x4){x[0], y[0], x[1], y[1]};
}
#define STAUX 0
#define SC1M 29
#define AUXK(k) (((SC1M >> (k)) & 1) ? 16 : 0)
template <int AUX> DEV void st16(void* base, long byte_off, u32x4 v) {
  if constexpr (AUX == 0) *(u32x4*)((char*)base + byte_off) = v;
  else {
    const __amdgpu_buffer_rsrc_t r = __builtin_amdgcn_make_buffer_rsrc(base, 0, 0x7fffffff, 0x00020000);
    __builtin_amdgcn_raw_buffer_store_b128(v, r, (int)byte_off, 0, AUX);
  }
}
DEV u32x2 phi4(f32x4 u) {
  const f32x4 a = u * 1.4426950408889634f;
  f32x4 e, m;
#pragma unroll
  for (int r = 0; r < 4; ++r) { e[r] = __builtin_amdgcn_fmed3f(__builtin_amdgcn_exp2f(a[r]), 0.f, 1.f); m[r] = __builtin_fmaxf(u[r], 1e-6f); }
  const f32x4 p = m + e;
  u32x2 w; w.x = pk2(p[0], p[1]); w.y = pk2(p[2], p[3]);
  return w;
}

using C1 = Cfg<128, 24, 2, 4, 4, 3, 4, 4, 64, 32, 8, 1, 0x1u>;
static_assert(C1::XS == 25 && C1::LDS_BYTES == 163840, "K1 schedule");
constexpr int IB2 = 16 * 528, IBX = IB2 + 4224 + 128;
constexpr int AUX_I = 0, AUX_T = AUX_I + 256 * IBX, AUX_F = AUX_T + 4096, AUX_R = AUX_F + 65536, AUX_C = AUX_R + 262144,
              AUX_G = AUX_C + 262144, WS_O = AUX_G + 65536, WS_HS = WS_O + S_ * D_ * 2, WS_CS = WS_HS + S_ * 128,
              AUX_END = WS_CS + 4 * 2 * DF_ * 4, WS_H = AUX_END, WS_ACT = WS_H + S_ * D_ * 2, WS_X = WS_ACT + S_ * DF_ * 2;
static_assert(WS_O % 4096 == 0 && WS_H % 4096 == 0, "alignment");
constexpr int OOB = 0x7FFFFFF0;
DEV void ld2c(u32x2& d, unsigned voff, const void* sbase) {
  asm volatile("global_load_dwordx2 %0, %1, %2 sc1" : "=v"(d) : "v"(voff), "s"(sbase) : "memory");
}
template <int OFF> DEV void ld4c(f32x4& d, unsigned voff, const void* sbase) {
  asm volatile("global_load_dwordx4 %0, %1, %2 offset:%3 sc1" : "=v"(d) : "v"(voff), "s"(sbase), "n"(OFF) : "memory");
}
DEV void touchd(unsigned& d, unsigned voff, const void* sbase) {
  asm volatile("global_load_dword %0, %1, %2" : "+v"(d) : "v"(voff), "s"(sbase) : "memory");
}
DEV bool pend(u32x4 v) { return __any(max(max(v[0], v[1]), max(v[2], v[3])) == SENT); }
#define SL0 8
#define SL1 8
#define SL2 2
#define BK2 16
#define BK1 16
struct Svc {
  f32x4 q[28], c[4], sr, sc;
  u32x4 sel;
  unsigned wo_, ro, co;
  const char *cbase, *aux;
  template <int N> DEV void issue() {
    if constexpr (N < 32) {
      constexpr int p = N >> 1;
      ld4c<(p & 3) * 1024>(q[N % 28], (N & 1) ? co : ro, aux + (p >> 2) * 4096);
    } else {
      ld4<0>(q[N % 28], wo_, cbase + (long)(N - 32) * (4 * DF_ * 2));
    }
  }
  template <int N> DEV void use() {
    asm volatile("" : "+v"(q[N % 28]));
    if constexpr (N < 32) {
      f32x4& t = (N & 1) ? sc : sr;
#pragma unroll
      for (int r = 0; r < 4; ++r) t[r] = fadd1(t[r], q[N % 28][r]);
    } else {
      mfma_a<(N < 36)>(c[N & 3], __builtin_bit_cast(bf16x8, sel), __builtin_bit_cast(bf16x8, q[N % 28]));
    }
  }
  template <int A, int B> DEV void issues() { if constexpr (A < B) { issue<A>(); issues<A + 1, B>(); } }
  template <int A, int B> DEV void uses() { if constexpr (A < B) { use<A>(); uses<A + 1, B>(); } }
  template <int A, int B, int ISS> DEV void take() { if constexpr (A < B) { wait_vm<ISS - B>(); uses<A, B>(); sched_fence(); } }
  template <int A, int E> DEV void roll() {
    if constexpr (A < E) { take<A, A + 4, A + 28>(); issues<A + 28, A + 32>(); sched_fence(); roll<A + 4, E>(); }
  }
};
DEV void bars(int n) { for (int i = 0; i < n; ++i) bar(); sched_fence(); }
__global__ __launch_bounds__(C1::NTHR + 64) void k1_qkv(const u16* __restrict__ x, const u16* __restrict__ wqkv,
                                                        char* __restrict__ ws, const u16* __restrict__ wo,
                                                        const u16* __restrict__ wg, const u16* __restrict__ wu) {
  extern __shared__ __attribute__((aligned(16))) char smem[];
  LAS char* lds = (LAS char*)smem;
  const int bid = blockIdx.x, xcd = bid & 7, idx = bid >> 3;
  const int rb = (xcd & 1) * 8 + (idx & 7), hp = (xcd >> 1) * 4 + (idx >> 3);
  STAMP(0, 0);
  asm volatile("k1_code_begin:");
  LAS char* ep = lds;
  LAS float* sRa = (LAS float*)ep;
  LAS float* sRb = (LAS float*)(ep + 512);
  LAS float* sC = (LAS float*)(ep + 1024);
  LAS char* sF = ep + 4096;
  LAS char* sQ = lds + C1::LDS_B0;
  LAS char* sK = sQ + 16384;
  const int ib = AUX_I + (rb * 16 + hp) * IBX;
  if (__builtin_expect(__builtin_amdgcn_readfirstlane(threadIdx.x) >= C1::NTHR, 0)) {
#define STS()
    bar();
    const int lane = threadIdx.x & 63, fr = lane & 15, g = lane >> 4;
    const char* aux = ws;
    const __amdgpu_buffer_rsrc_t rx = __builtin_amdgcn_make_buffer_rsrc((void*)aux, 0, AUX_END, 0x00020000);
    const int pj = lane & 31, pc = pj < 16 ? rb * 16 + pj : (pj - 16) * 16 + hp;
    u32x2 tv, fl;
    unsigned td = 0;
    ld2c(tv, AUX_T + pc * 8, aux);
    const u32x4 s4 = {SENT, SENT, SENT, SENT};
    __builtin_amdgcn_raw_buffer_store_b128(s4, rx, AUX_R + (rb * 16 + hp) * 1024 + lane * 16, 0, 16);
    __builtin_amdgcn_raw_buffer_store_b128(s4, rx, AUX_C + (hp * 16 + rb) * 1024 + lane * 16, 0, 16);
#pragma unroll
    for (int i = 0; i < 13; ++i) __builtin_amdgcn_raw_buffer_store_b128(s4, rx, (i < 12 || lane < 24) ? ib + lane * 16 : OOB, i * 1024, 16);
    __builtin_amdgcn_raw_buffer_store_b128(s4, rx, lane < 16 ? AUX_G + bid * 256 + lane * 16 : OOB, 0, 16);
    __builtin_amdgcn_raw_buffer_store_b128(s4, rx, WS_HS + bid * 1024 + lane * 16, 0, 16);
    {
      const char* pc0; int back, csz;
      asm volatile("s_getpc_b64 %0\nk1_code_here:\n\ts_mov_b32 %1, k1_code_here-k1_code_begin\n\ts_mov_b32 %2, k4_code_end-k1_code_begin"
                   : "=s"(pc0), "=s"(back), "=s"(csz));
      const int f = (idx * 32 + pj) * 64;
      touchd(td, f < csz ? f : 0, pc0 - back);
    }
    Svc V;
    const int cu = (xcd & 1) * 28 + (idx < 28 ? idx : idx - 4), cg = cu >> 2, ccol = (xcd >> 1) * 896 + (cg < 7 ? cg : cg - 7) * 128;
    V.cbase = (const char*)(cg < 7 ? wg : wu) + ((long)(cu & 3) * 256 * DF_ + ccol) * 2;
    V.aux = aux;
    V.wo_ = g * (DF_ * 2) + fr * 16;
    V.ro = AUX_R + rb * 16384 + lane * 16;
    V.co = AUX_C + hp * 16384 + (lane < 48 ? lane : lane - 16) * 16;
#pragma unroll
    for (int j = 0; j < 4; ++j) V.sel[j] = fr == 2 * j ? 0x3F80u : fr == 2 * j + 1 ? 0x3F800000u : 0u;
    V.sr = V.sc = (f32x4){0.f, 0.f, 0.f, 0.f};
    asm volatile("" : "+v"(V.sel));
    sched_fence();
    bar();
    sched_fence();
    asm volatile("s_waitcnt vmcnt(0)" : "+v"(tv) :: "memory");
    tv.x = tv.x * 0x9E3779B1u + 0x7F4A7C15u;
    tv.y = tv.y * 0x85EBCA6Bu + 0xC2B2AE35u;
    __builtin_amdgcn_raw_buffer_store_b64(tv, rx, lane < 32 ? AUX_F + (pc * 32 + (pj < 16 ? hp : 16 + rb)) * 8 : OOB, 0, 16);
    const unsigned e0 = __builtin_amdgcn_readlane(tv.x, hp), e1 = __builtin_amdgcn_readlane(tv.y, hp);
    const unsigned lo = AUX_F + ((rb * 16 + hp) * 32 + pj) * 8;
    sched_fence();
#define TJ0 2
#define TK 0
#define TJ1 6
    auto trickle = [&](int J) {
      bars(1);
#pragma unroll
      for (int i = 0; i < TK; ++i)
        if (J >= TJ0 && J < TJ1) touchd(td, (lane >> 2) * (DF_ * 2) + (lane & 3) * 64, V.cbase + (long)(15 - (J - TJ0) * TK - i) * (16 * DF_ * 2));
      sched_fence();
    };
    trickle(0); trickle(1); trickle(2); trickle(3); trickle(4);
    ld2c(fl, lo, aux);
    trickle(5); trickle(6);
    __builtin_amdgcn_s_setprio(3);
    asm volatile("s_waitcnt vmcnt(%1)" : "+v"(fl) : "n"(TK * ((TJ0 <= 5 && TJ1 > 5) + (TJ0 <= 6 && TJ1 > 6))) : "memory");
    if (!__all(fl.x == e0 && fl.y == e1)) {
      for (int spin = 0; spin < (1 << 18); ++spin) {
        ld2c(fl, lo, aux);
        asm volatile("s_waitcnt vmcnt(0)" : "+v"(fl) :: "memory");
        if (__all(fl.x == e0 && fl.y == e1)) break;
      }
    }
    sched_fence();
    bars(1); V.issues<0, 8>();
    bars(1); V.issues<8, 16>();
    bars(1); V.issues<16, 24>(); V.take<0, 8, 24>();
    bars(1); V.issues<24, 32>(); V.take<8, 16, 32>();
    bars(1); V.take<16, 24, 32>();
    bars(1); STS(); V.issues<32, 32 + BK1>(); STS();
    bars(1); STS(); V.take<24, 32, 32 + BK1>();
    if (__builtin_expect(__any(__builtin_isunordered(V.sr[0], V.sr[1]) || __builtin_isunordered(V.sr[2], V.sr[3]) ||
                               __builtin_isunordered(V.sc[0], V.sc[1]) || __builtin_isunordered(V.sc[2], V.sc[3])), 0)) {
      V.sr = V.sc = (f32x4){0.f, 0.f, 0.f, 0.f};
#pragma nounroll
      for (int hf = 0; hf < 4; ++hf) {
        u32x4 a[8];
        for (int spin = 0; spin < (1 << 16); ++spin) {
          asm volatile("" ::: "memory");
#pragma unroll
          for (int p = 0; p < 8; ++p) a[p] = __builtin_amdgcn_raw_buffer_load_b128(rx, ((p & 1) ? V.co : V.ro) + hf * 4096, (p >> 1) * 1024, 16);
          u32x4 mx = a[0];
#pragma unroll
          for (int p = 1; p < 8; ++p) mx = __builtin_elementwise_max(mx, a[p]);
          if (!pend(mx)) break;
        }
#pragma unroll
        for (int p = 0; p < 8; p += 2) { V.sr += __builtin_bit_cast(f32x4, a[p]); V.sc += __builtin_bit_cast(f32x4, a[p + 1]); }
      }
    }
    {
      f32x2 ra, rm;
#pragma unroll
      for (int r = 0; r < 2; ++r) {
        const float nm = V.sr[2 * r] * (-1.f / D_);
        ra[r] = __builtin_amdgcn_rsqf(fmaxf(V.sr[2 * r + 1] * (1.f / D_) - nm * nm, 0.f) + 1e-5f);
        rm[r] = nm * ra[r];
      }
      *(LAS f32x2*)(sRa + 2 * lane) = ra;
      *(LAS f32x2*)(sRb + 2 * lane) = rm;
      *(LAS f32x4*)(sC + 4 * lane) = V.sc;
    }
    STS();
    lbar();
    sched_fence();
    STS();
    __builtin_amdgcn_s_setprio(0);
    __builtin_amdgcn_raw_buffer_store_b64((u32x2){e0, e1}, rx, lane == 0 ? AUX_T + (rb * 16 + hp) * 8 : OOB, 0, 16);
    V.issues<32 + BK1, 60>();
    STS();
    V.roll<32, 32 + BK2>();
#pragma unroll
    for (int i = 0; i < 8; ++i) touchd(td, (idx * 32 + i * 4 + g) * 2048 + (xcd >> 2) * 1024 + fr * 64, wo);
    STS();
    bars(1);
    STS();
    V.roll<32 + BK2, 68>();
    STS();
    V.take<68, 96, 96>();
    STS();
    {
      asm volatile("s_nop 7\n\ts_nop 7\n\ts_nop 7" : "+v"(V.c[0]), "+v"(V.c[1]), "+v"(V.c[2]), "+v"(V.c[3]), "+v"(td));
      const f32x4 t = (V.c[0] + V.c[1]) + (V.c[2] + V.c[3]);
      __builtin_amdgcn_raw_buffer_store_b128(__builtin_bit_cast(u32x4, t), rx,
          lane < (idx < 28 ? 32 : 0) ? WS_CS + ((cu & 3) * (2 * DF_) + (cg < 7 ? 0 : DF_) + ccol + fr * 8 + g * 4) * 4 : OOB, 0, AUXK(0));
    }
    return;
  }
  u32x8 ka = karg8();
  sched_fence();
  Loop<C1> L;
  auto bsrc = [&](int k, int c) -> unsigned { return (k * (3 * D_) + (c >> 3) * D_ + (c & 7) * 8) * 2; };
  L.init0(lds, D_ * 2, bsrc, -1);
  karg_wait(ka);
  L.sp = (rb + hp) & 3;
  L.init1(karg_ptr(ka, 0) + (long)rb * 128 * D_ * 2, karg_ptr(ka, 1) + hp * 128, 64L * 3 * D_ * 2, (((rb >> 2) + (hp >> 2)) & 3) * 4);
  const int lane = L.lane, fr = lane & 15, g = lane >> 4, wm = L.wm, wn = L.wn, wid = L.wid, pj = lane & 31;
  const char* aux = karg_ptr(ka, 2);
  const __amdgpu_buffer_rsrc_t rx = __builtin_amdgcn_make_buffer_rsrc((void*)aux, 0, AUX_END, 0x00020000);
  int ro, co;
  {
    ro = AUX_R + ((rb * 16 + hp) * 128 + wm * 64 + wn * 16 + fr) * 8;
    co = AUX_C + ((hp * 16 + rb) * 256 + g * 64 + wn * 16 + fr) * 4;
    asm volatile("" : "+v"(ro), "+v"(co));
    ro = g == (fr >> 2) ? ro : OOB;
    co = g < (wm ? 0 : 3) ? co : OOB;
    asm volatile("" : "+v"(ro), "+v"(co));
  }
  float w0, w1;
  L.run<0>([&](auto sx) {
    constexpr int sv = decltype(sx)::value;
    if constexpr (sv == 804) { w0 = fsel(fr & 2, fsel(fr & 1, L.rq[3], L.rq[2]), fsel(fr & 1, L.rq[1], L.rq[0])); asm volatile("" : "+v"(w0) : "v"(L.rs)); }
    if constexpr (sv == 806) __builtin_amdgcn_raw_buffer_store_b64((u32x2){__float_as_uint(L.rs[0]), __float_as_uint(w0)}, rx, ro, 0, 16);
    if constexpr (sv == 808) { w1 = fsel(g == 0, L.cs[0][0], fsel(g == 1, L.cs[1][0], L.cs[2][0])); asm volatile("" : "+v"(w1) : "v"(L.cs[0]), "v"(L.cs[1]), "v"(L.cs[2])); }
    if constexpr (sv == 810) __builtin_amdgcn_raw_buffer_store_b32(__float_as_uint(w1), rx, co, 0, 16);
  });
  {
    const int ik = 64 + wn * 16 + fr;
    const float ck = sC[ik], cv = sC[64 + ik];
#pragma unroll
    for (int m = 0; m < 4; ++m) {
      const int am = m ^ wn, r0 = wm * 64 + am * 16 + 4 * g;
      const f32x4 sa = *(const LAS f32x4*)(sRa + r0), sb = *(const LAS f32x4*)(sRb + r0);
      const f32x4 k = L.acc[m][1] * sa + (sb * ck + 1e-6f);
      const f32x4 v = L.acc[m][2] * sa + sb * cv;
      u32x2 a = phi4(k), b;
      b.x = pk2(v[0], v[1]); b.y = pk2(v[2], v[3]);
      LAS char* f = sF + (wn * 4 + wm * 2 + (am >> 1)) * 1024 + lane * 16 + (am & 1) * 8;
      *(LAS u32x2*)f = a;
      *(LAS u32x2*)(f + 16384) = b;
    }
  }
  L.ts();
  lbar();
  L.ts();
  {
    f32x4 d = {0.f, 0.f, 0.f, 0.f}, z = d;
#pragma unroll
    for (int G = 0; G < 4; ++G) {
      const bf16x8 fk = *(const LAS bf16x8*)(sF + (wn * 4 + G) * 1024 + lane * 16);
      const bf16x8 fv = *(const LAS bf16x8*)(sF + 16384 + (((wn & 2) + wm) * 4 + G) * 1024 + lane * 16);
      d = mfma16(fk, fv, d);
      z = mfma16(fk, L.ones, z);
    }
    int zo = AUX_I + ((wn * 4 + g) * 16 + hp) * IBX + rb * 528 + 512;
    asm volatile("" : "+v"(zo));
    __builtin_amdgcn_raw_buffer_store_b128(__builtin_bit_cast(u32x4, d), rx, AUX_I + ((2 * wid + (lane >> 5)) * 16 + hp) * IBX + rb * 528 + pj * 16, 0, 16);
    __builtin_amdgcn_raw_buffer_store_b128(__builtin_bit_cast(u32x4, z), rx, fr < (wm ? 0 : 1) ? zo : OOB, 0, 16);
  }
  sched_fence();
  L.ts();
  {
    const int ic = wn * 16 + 4 * g;
    const f32x4 c4 = *(const LAS f32x4*)(sC + ic);
#pragma unroll
    for (int m = 0; m < 4; ++m) {
      const int r = wm * 64 + (m ^ wn) * 16 + fr;
      *(LAS u32x2*)(sQ + ((wn >> 1) * 8 + wm * 4 + (m ^ wn)) * 1024 + lane * 16 + (wn & 1) * 8) = phi4(L.acc[m][0] * sRa[r] + (c4 * sRb[r] + 1e-6f));
    }
  }
  sched_fence();
  L.ts();
  if (wid == 7) {
    if (SL0) __builtin_amdgcn_s_sleep(SL0);
    {
      u32x4 v[9];
      const int io = ib + (lane >> 5) * 528 + pj * 16, zo = ib + fr * 528 + 512;
      unsigned pm = 0x1FF;
      for (int spin = 0; spin < (1 << 18) && pm; ++spin) {
        asm volatile("" ::: "memory");
#pragma unroll
        for (int i = 0; i < 9; ++i) if (pm >> i & 1) v[i] = __builtin_amdgcn_raw_buffer_load_b128(rx, i < 8 ? io : zo, i < 8 ? i * 1056 : 0, 16);
#pragma unroll
        for (int i = 0; i < 9; ++i) if ((pm >> i & 1) && !pend(v[i])) pm &= ~(1u << i);
        if (SL2 && pm) __builtin_amdgcn_s_sleep(SL2);
      }
      L.ts();
#define F4(i) __builtin_bit_cast(f32x4, v[i])
      const f32x4 t = ((F4(0) + F4(1)) + (F4(2) + F4(3))) + ((F4(4) + F4(5)) + (F4(6) + F4(7)));
      auto a = __builtin_amdgcn_permlane32_swap(__float_as_uint(t[0]), __float_as_uint(t[2]), false, false);
      auto b = __builtin_amdgcn_permlane32_swap(__float_as_uint(t[1]), __float_as_uint(t[3]), false, false);
      const unsigned w = pk2(__uint_as_float(a[0]) + __uint_as_float(a[1]), __uint_as_float(b[0]) + __uint_as_float(b[1]));
      auto c = __builtin_amdgcn_permlane32_swap(w, w, false, false);
      const int oo = AUX_I + ((lane >> 5) * 16 + hp) * IBX + IB2 + rb * 256 + pj * 8;
#pragma unroll
      for (int i = 0; i < 8; ++i) __builtin_amdgcn_raw_buffer_store_b64((u32x2){c[0], c[1]}, rx, oo, i * 32 * IBX, 16);
      f32x4 z = F4(8);
#undef F4
#pragma unroll
      for (int r = 0; r < 4; ++r) { z[r] += dpp<0x128>(z[r]); z[r] += dpp<0x124>(z[r]); z[r] += dpp<0x4E>(z[r]); z[r] += dpp<0xB1>(z[r]); }
      __builtin_amdgcn_raw_buffer_store_b64((u32x2){pk2(z[0], z[1]), pk2(z[2], z[3])}, rx,
                                            lane < 16 ? AUX_I + (lane * 16 + hp) * IBX + IB2 + 4096 + rb * 8 : OOB, 0, 16);
    }
    L.ts();
    if (SL1) __builtin_amdgcn_s_sleep(SL1);
    {
      u32x4 v[5];
      unsigned pm = 0x1F;
      for (int spin = 0; spin < (1 << 18) && pm; ++spin) {
        asm volatile("" ::: "memory");
#pragma unroll
        for (int i = 0; i < 5; ++i) if (pm >> i & 1) v[i] = __builtin_amdgcn_raw_buffer_load_b128(rx, ib + IB2 + (i < 4 ? lane : lane & 7) * 16, i * 1024, 16);
#pragma unroll
        for (int i = 0; i < 5; ++i) if ((pm >> i & 1) && !pend(v[i])) pm &= ~(1u << i);
        if (SL2 && pm) __builtin_amdgcn_s_sleep(SL2);
      }
      L.ts();
#pragma unroll
      for (int i = 0; i < 5; ++i) *(LAS u32x4*)(sK + i * 1024 + (i < 4 ? lane : lane & 7) * 16) = v[i];
    }
  }
  {
    lbar();
    L.ts();
    bf16x8 fq[2];
#pragma unroll
    for (int m = 0; m < 2; ++m) fq[m] = *(const LAS bf16x8*)(sQ + (wid * 2 + m) * 1024 + lane * 16);
    u32x4 fa[3];
#pragma unroll
    for (int t = 0; t < 3; ++t) {
      LAS char* p = t < 2 ? sK + (t * 4 + wm * 2) * 512 + lane * 8 : sK + 4096 + (wm * 8 + g) * 8;
      const u32x2 lo = *(const LAS u32x2*)p, hi = *(const LAS u32x2*)(p + (t < 2 ? 512 : 32));
      fa[t] = (u32x4){lo.x, lo.y, hi.x, hi.y};
    }
    const f32x4 z4 = {0.f, 0.f, 0.f, 0.f};
#pragma unroll
    for (int m = 0; m < 2; ++m) {
      const f32x4 den = mfma16(__builtin_bit_cast(bf16x8, fa[2]), fq[m], z4);
      const float inv = __builtin_amdgcn_rcpf(den[0] + 1e-6f);
      u32x2 w[2];
#pragma unroll
      for (int t = 0; t < 2; ++t) {
        const f32x4 n = mfma16(__builtin_bit_cast(bf16x8, fa[t]), fq[m], z4) * inv;
        w[t].x = pk2(n[0], n[1]); w[t].y = pk2(n[2], n[3]);
      }
      __builtin_amdgcn_raw_buffer_store_b128(xpose2(w[0], w[1]), rx,
          WS_O + ((rb * 128 + (hp * 2 + wm) * 4 + (g & 1) * 2 + (g >> 1)) * 128 + (wn * 2 + m) * 16 + fr) * 16, 0, AUXK(0));
    }
  }
  L.ts();
  wait_vm<0>();
  L.ts();
  STAMP(0, 3);
  L.tsdump<0>();
}

using C2 = Cfg<128, 8, 2, 2, 4, 2, 4, 4, 64, 32, 8, 0, 0x3u>;
DEV void k2_wo(const u16* __restrict__ x, u16* h, float* hstat, LAS char* lds) {
  const int bid = blockIdx.x, xcd = bid & 7, idx = bid >> 3;
  const int rb = (xcd & 3) * 4 + (idx & 3), ct = (xcd >> 2) * 8 + (idx >> 2);
  STAMP(2, 0);
  u32x4 ka = karg4();
  sched_fence();
  Loop<C2> L;
  auto bsrc = [&](int k, int c) -> unsigned { return (k * D_ + c * 8) * 2; };
  L.init0(lds, D_ * 2, bsrc, 0);
  karg_wait(ka);
  L.init1(karg_ptr(ka, 0) + (long)rb * 128 * D_ * 2, karg_ptr(ka, 1) + ct * 128, 64L * D_ * 2);
  const int lane = L.lane, fr = lane & 15, g = lane >> 4, wm = L.wm, wn = L.wn;
  const long off0 = ((long)rb * 128 + wm * 64 + fr) * D_ + ct * 64 + wn * 32 + (g & 1) * 16 + (g >> 1) * 8;
  u32x4 xr[4];
  L.run<2>([&](auto) {
#pragma unroll
    for (int m = 0; m < 4; ++m) xr[m] = *(const u32x4*)(x + off0 + m * 16 * D_);
  });
  LAS float* sS = (LAS float*)lds;
  float sq[2][4];
#pragma unroll
  for (int m = 0; m < 4; ++m) {
    float s = 0.f, q = 0.f;
    const u32x4 r = xpose2((u32x2){xr[m][0], xr[m][1]}, (u32x2){xr[m][2], xr[m][3]});
    u32x2 w[2];
#pragma unroll
    for (int j = 0; j < 2; ++j) {
      w[j].x = pk2(bflo(r[2 * j]) + L.acc[m][j][0], bfhi(r[2 * j]) + L.acc[m][j][1]);
      w[j].y = pk2(bflo(r[2 * j + 1]) + L.acc[m][j][2], bfhi(r[2 * j + 1]) + L.acc[m][j][3]);
      float f0 = bflo(w[j].x), f1 = bfhi(w[j].x), f2 = bflo(w[j].y), f3 = bfhi(w[j].y);
      s += (f0 + f1) + (f2 + f3);
      q += (f0 * f0 + f1 * f1) + (f2 * f2 + f3 * f3);
    }
    st16<AUXK(2)>(h, (off0 + m * 16 * D_) * 2, xpose2(w[0], w[1]));
    sq[0][m] = s; sq[1][m] = q;
  }
  {
    f32x2 t;
#pragma unroll
    for (int e = 0; e < 2; ++e) {
      auto a = __builtin_amdgcn_permlane32_swap(__float_as_uint(sq[e][0]), __float_as_uint(sq[e][2]), false, false);
      auto b = __builtin_amdgcn_permlane32_swap(__float_as_uint(sq[e][1]), __float_as_uint(sq[e][3]), false, false);
      auto c = __builtin_amdgcn_permlane16_swap(__float_as_uint(__uint_as_float(a[0]) + __uint_as_float(a[1])),
                                                __float_as_uint(__uint_as_float(b[0]) + __uint_as_float(b[1])), false, false);
      t[e] = __uint_as_float(c[0]) + __uint_as_float(c[1]);
    }
    *(LAS f32x2*)(sS + (wn * 128 + wm * 64 + lane) * 2) = t;
  }
  lbar();
  if (threadIdx.x < 128) {
    const int r = threadIdx.x;
    f32x2 v = *(LAS f32x2*)(sS + r * 2) + *(LAS f32x2*)(sS + (128 + r) * 2);
    const __amdgpu_buffer_rsrc_t rh = __builtin_amdgcn_make_buffer_rsrc((void*)hstat, 0, S_ * 128, 0x00020000);
    __builtin_amdgcn_raw_buffer_store_b64(__builtin_bit_cast(u32x2, v), rh, ((rb * 128 + r) * 16 + ct) * 8, 0, 16);
  }
  wait_vm<0>();
  L.ts();
  STAMP(2, 3);
  L.tsdump<2>();
}

using C3 = Cfg<256, 28, 4, 2, 4, 7, 3, 4, 32, 32, 12, 2, 0x7Fu>;
using C4 = Cfg<128, 16, 2, 4, 4, 2, 4, 4, 64, 56, 8, 3, 0x3u>;
#define INV2 13
#define PW 0
#define SL6 8
#define SL7 2
DEV void k4_down(const u16* act, const u16* __restrict__ wd, const u16* h,
                 u16* __restrict__ out, unsigned* xbuf, const char* flags, LAS char* lds);
constexpr int K3_LDS = C3::LDS_BYTES + 3072;
__global__ __launch_bounds__(C3::NTHR, 2) void k3_glu(const u16* __restrict__ o, const u16* __restrict__ wo, const u16* __restrict__ x,
                                                      u16* h, const u16* __restrict__ wg, const u16* __restrict__ wu,
                                                      float* hstat,
                                                      u16* act, const u16* __restrict__ wd, unsigned* xbuf, u16* __restrict__ out) {
  extern __shared__ __attribute__((aligned(16))) char smem[];
  LAS char* lds = (LAS char*)smem;
  const int bid = blockIdx.x, xcd = bid & 7, idx = bid >> 3;
  const int rb = (xcd & 1) * 4 + (idx & 3), ct = (xcd >> 1) * 8 + (idx >> 2);
  const int tid = threadIdx.x;
  const char* hsp = (const char*)hstat;
  const char* flags = hsp - (WS_HS - AUX_G);
  const __amdgpu_buffer_rsrc_t rf = __builtin_amdgcn_make_buffer_rsrc((void*)flags, 0, 65536, 0x00020000);
  if (__builtin_amdgcn_readfirstlane(tid) < C2::NTHR) k2_wo(x, h, hstat, lds);
  else {
    bars(18);
  }
  sched_fence();
  bar();
  sched_fence();
  if (__builtin_amdgcn_readfirstlane(tid) == 0) {
    const int rb2 = (xcd & 3) * 4 + (idx & 3), ct2 = (xcd >> 2) * 8 + (idx >> 2);
    __builtin_amdgcn_raw_buffer_store_b32(0u, rf, tid < 32 ? ((rb2 >> 1) * 32 + tid) * 256 + 128 + ((rb2 & 1) * 16 + ct2) * 4 : OOB, 0, 16);
  }
  STAMP(3, 0);
  sched_fence();
  auto cmap = [](int c, bool& up) -> int {
    const int w = c >= 14, r = c - 14 * w;
    up = r >= 12 ? (r & 1) : r >= 6;
    return w * 7 + (r >= 12 ? 6 : r >= 6 ? r - 6 : r);
  };
  f32x4 hs[4];
  float cp[2];
  Loop<C3> L;
  L.ts();
  auto bsrc = [&](int k, int c) -> unsigned { return (k * DF_ + c * 8) * 2; };
  L.init0(lds, D_ * 2, bsrc, 0);
  L.ts();
  if (L.wid == PW) {
    const int fo = (rb * 32 + ct) * 256 + 128 + (L.lane & 31) * 4;
    if (SL6) __builtin_amdgcn_s_sleep(SL6);
    for (int spin = 0; spin < (1 << 22); ++spin) {
      asm volatile("" ::: "memory");
      if (!__any(__builtin_amdgcn_raw_buffer_load_b32(rf, fo, 0, 16) == SENT)) break;
      if (SL7) __builtin_amdgcn_s_sleep(SL7);
    }
  }
  sched_fence();
  bar();
  sched_fence();
  L.ts();
  L.init1((const char*)h + (long)rb * 256 * D_ * 2, (const char*)(L.wid >> 2 ? wu : wg) + ct * 224, 32L * DF_ * 2);
  const int lane = L.lane, fr = lane & 15, g = lane >> 4, wm = L.wm, wn = L.wn;
  LAS float* sC = (LAS float*)(lds + C3::LDS_BYTES);
  LAS float* sR = (LAS float*)(lds + C3::LDS_BYTES + 1024);
  unsigned ho = (rb * 256 + L.wid * 32) * 128 + lane * 16, co, to, sra, sca;
  f32x2 pp[4];
  float w8[8];
  {
    const int c = tid < 448 ? tid >> 1 : 0;
    bool up; const int ch = cmap(c >> 3, up);
    co = (S_ * 32 + (up ? DF_ : 0) + ct * 112 + ch * 8 + (c & 7) + (tid & 1) * (2 * 2 * DF_)) * 4;
    to = (((xcd & 1) * 4 + (xcd >> 1)) * 256 + idx * 8 + L.wid) * 3584 + (lane < 28 ? lane : 0) * 64;
    sca = (unsigned)(size_t)(sC + c);
    sra = (unsigned)(size_t)((lane & 4) ? sR + (L.wid * 32 + (lane & 3) * 8 + (lane >> 3)) * 2 : sC + 240);
    asm volatile("" : "+v"(ho), "+v"(co), "+v"(to), "+v"(sra), "+v"(sca));
  }
  L.run<3>([&](auto sx) {
    constexpr int sv = decltype(sx)::value;
    if constexpr (sv == SENTS * 100 + 12 || sv == SENTS * 100 + 26) stsent(bid * 16384 + tid * 16 + (sv == SENTS * 100 + 12 ? 0 : 8192), xbuf);
    constexpr int s0 = sv - SB * 100, su = s0 < 0 || s0 >= 500 ? -1 : s0 < 200 ? s0 + 2600 : s0 < 300 ? -1 : s0 + 2500;
    if constexpr (su >= 0) {
      if constexpr (su == 2602) ld4c<0>(hs[0], ho, hsp);
      if constexpr (su == 2609) ld4c<1024>(hs[1], ho, hsp);
      if constexpr (su == 2616) ld4c<2048>(hs[2], ho, hsp);
      if constexpr (su == 2623) ld4c<3072>(hs[3], ho, hsp);
      if constexpr (su == 2703) ld1(cp[0], co, hsp);
      if constexpr (su == 2717) ld1(cp[1], co, hsp + 1L * (2 * DF_ * 4));
      if constexpr (su == 2800) asm volatile("s_waitcnt vmcnt(8)" : "+v"(hs[0]), "+v"(hs[1]), "+v"(hs[2]), "+v"(hs[3]) :: "memory");
      if constexpr (su >= 2800 && su < 2804) {
        constexpr int i = su - 2800;
        pp[i] = (f32x2){hs[i][0], hs[i][1]} + (f32x2){hs[i][2], hs[i][3]};
        asm volatile("" : "+v"(pp[i]));
      }
      if constexpr (su >= 2804 && su < 2812) {
        constexpr int i = (su - 2804) >> 2, e = (su - 2804) & 1, gv = ((su - 2804) >> 1) & 1;
        w8[su - 2804] = fsel(lane & 1, pp[2 * i + 1 - gv][e], pp[2 * i + gv][e]);
        asm volatile("" : "+v"(w8[su - 2804]));
      }
      if constexpr (su >= 2812 && su < 2816) {
        constexpr int i = (su - 2812) >> 1, e = (su - 2812) & 1;
        pp[i][e] = w8[4 * i + e] + dpp<0xB1>(w8[4 * i + 2 + e]);
        asm volatile("" : "+v"(pp[i]));
      }
      if constexpr (su >= 2816 && su < 2820) {
        constexpr int e = (su - 2816) & 1, gv = (su - 2816) >> 1;
        w8[su - 2816] = fsel(lane & 2, pp[1 - gv][e], pp[gv][e]);
        asm volatile("" : "+v"(w8[su - 2816]));
      }
      if constexpr (su == 2820 || su == 2821) { w8[su - 2816] = w8[su - 2820] + dpp<0x4E>(w8[su - 2818]); asm volatile("" : "+v"(w8[su - 2816])); }
      if constexpr (su == 2823 || su == 2824) {
        w8[su - 2817] = (w8[su - 2819] + dpp<0x124>(w8[su - 2819])) * (1.f / D_); asm volatile("" : "+v"(w8[su - 2817]));
      }
      if constexpr (su == 2826) { w8[7] = fmaxf(w8[7] - w8[6] * w8[6], 0.f); asm volatile("" : "+v"(w8[7])); }
      if constexpr (su == 2900) *(LAS f32x2*)(size_t)sra = (f32x2){-w8[6], w8[7] + 1e-5f};
      if constexpr (su == 2902) {
        asm volatile("s_waitcnt vmcnt(8)" : "+v"(cp[0]), "+v"(cp[1]) :: "memory");
        cp[0] += cp[1]; asm volatile("" : "+v"(cp[0]));
      }
      if constexpr (su == 2906) { cp[0] += dpp<0xB1>(cp[0]); asm volatile("" : "+v"(cp[0])); }
      if constexpr (su == 2908) *(LAS float*)(size_t)sca = cp[0];
    }
    if constexpr (sv == TCH * 100) touchv(to, wd);
    if constexpr (sv == TCH2 * 100 + 1) touchv(to + 1792, wd);
  });
  static_assert(C3::XS == 28, "K3 schedule");
  {
    float nm[4];
#pragma unroll
    for (int m = 0; m < 4; ++m) nm[m] = sR[(wm * 64 + m * 16 + fr) * 2];
#pragma unroll
    for (int j = 0; j < 7; ++j) {
      const f32x4 c4 = *(const LAS f32x4*)(sC + (wn * 14 + 2 * j) * 8 + 4 * g);
#pragma unroll
      for (int m = 0; m < 4; ++m) L.acc[m][j] += c4 * nm[m];
    }
  }
  const int hi = lane >> 5;
  const int apo = ((rb * 2 + (wm >> 1)) * 448 + ct * 14 + wn * 7) * 2048 + ((wm & 1) * 64 + fr) * 16;
  constexpr float K1c = -2.f * 0.7978845608028654f * 1.4426950408889634f, K2c = K1c * 0.044715f;
  auto glu = [&](f32x4 a, f32x4 b, float q, float r1, float r3) -> u32x2 {
    const f32x4 z = a * ((a * a) * r3 + r1);
    f32x4 e;
#pragma unroll
    for (int r = 0; r < 4; ++r) e[r] = __builtin_amdgcn_exp2f(z[r]);
    e = e * q + q;
#pragma unroll
    for (int r = 0; r < 4; ++r) e[r] = __builtin_amdgcn_rcpf(e[r]);
    const f32x4 o = (a * b) * e;
    u32x2 w; w.x = pk2(o[0], o[1]); w.y = pk2(o[2], o[3]);
    return w;
  };
  u32x2 w6[2];
#pragma unroll
  for (int mp = 0; mp < 2; ++mp) {
    float q[2], r1[2], r3[2];
#pragma unroll
    for (int mm = 0; mm < 2; ++mm) {
      q[mm] = sR[(wm * 64 + (2 * mp + mm) * 16 + fr) * 2 + 1];
      const float rstd = __builtin_amdgcn_rsqf(q[mm]);
      r1[mm] = K1c * rstd; r3[mm] = K2c * rstd * rstd * rstd;
    }
#pragma unroll
    for (int j = 0; j < 3; ++j) {
      const u32x2 wa = glu(L.acc[2 * mp][j], L.acc[2 * mp][3 + j], q[0], r1[0], r3[0]);
      const u32x2 wb = glu(L.acc[2 * mp + 1][j], L.acc[2 * mp + 1][3 + j], q[1], r1[1], r3[1]);
      st16<AUXK(3)>(act, apo + (2 * j + (g >> 1)) * 2048 + (2 * mp + (g & 1)) * 256, xpose2(wa, wb));
    }
    f32x4 G, U;
#pragma unroll
    for (int r = 0; r < 4; ++r) {
      auto sw = __builtin_amdgcn_permlane32_swap(__float_as_uint(L.acc[2 * mp][6][r]), __float_as_uint(L.acc[2 * mp + 1][6][r]), false, false);
      G[r] = __uint_as_float(sw[0]); U[r] = __uint_as_float(sw[1]);
    }
    w6[mp] = glu(G, U, hi ? q[1] : q[0], hi ? r1[1] : r1[0], hi ? r3[1] : r3[0]);
  }
  st16<AUXK(3)>(act, apo + 6 * 2048 + (2 * (g & 1) + (g >> 1)) * 256, xpose2(w6[0], w6[1]));
  wait_vm<0>();
  L.ts();
  STAMP(3, 3);
  L.tsdump<3>();
  sched_fence();
  bar();
  sched_fence();
  if (L.wid == 0) {
    const int fc = lane < 16 ? (((rb * 2 + (lane >> 3)) * 2 + (ct >> 4)) * 8 + (lane & 7)) * 256 + (ct & 15) * 4
                             : ((((xcd >> 1) * 4 + (idx & 3)) * 2 + ((xcd & 1) ^ 1)) * 8 + (idx >> 2)) * 256 + 64;
    __builtin_amdgcn_raw_buffer_store_b32(0u, rf, lane <= 16 ? fc : OOB, 0, 16);
  }
  k4_down(act, wd, h, out, xbuf, flags, lds);
}

DEV void k4_down(const u16* act, const u16* __restrict__ wd, const u16* h,
                 u16* __restrict__ out, unsigned* xbuf, const char* flags, LAS char* lds) {
  const int bid = blockIdx.x, xcd = bid & 7, idx = bid >> 3;
  const int kh = xcd & 1, rb = (xcd >> 1) * 4 + (idx & 3), cb = idx >> 2;
  STAMP(4, 0);
  sched_fence();
  Loop<C4> L;
  auto bsrc = [&](int k, int c) -> unsigned { return (k * D_ + c * 8) * 2; };
  L.init0(lds, DF_ * 2, bsrc, 2 * kh);
  L.ts();
  if (L.wid == PW) {
    const __amdgpu_buffer_rsrc_t rf = __builtin_amdgcn_make_buffer_rsrc((void*)flags, 0, 65536, 0x00020000);
    const int fo = ((rb * 2 + kh) * 8 + cb) * 256 + (L.lane < 16 ? L.lane : 16) * 4;
    if (SL6) __builtin_amdgcn_s_sleep(SL6);
    for (int spin = 0; spin < (1 << 22); ++spin) {
      asm volatile("" ::: "memory");
      if (!__any(__builtin_amdgcn_raw_buffer_load_b32(rf, fo, 0, 16) == SENT)) break;
      if (SL7) __builtin_amdgcn_s_sleep(SL7);
    }
    L.ts();
  }
  sched_fence();
  bar();
  sched_fence();
  L.init1((const char*)act + (long)(rb * 448 + kh * 224) * 2048, (const char*)wd + ((long)kh * 1792 * D_ + cb * 128) * 2, 64L * D_ * 2);
  const int lane = L.lane, fr = lane & 15, g = lane >> 4, wm = L.wm, wn = L.wn;
  const long off0 = ((long)rb * 128 + wm * 64 + kh * 32 + fr) * D_ + cb * 128 + wn * 32 + (g & 1) * 16 + (g >> 1) * 8;
  u32x4 xr[2];
  L.run<4>([&](auto) {
#pragma unroll
    for (int m = 0; m < 2; ++m) xr[m] = *(const u32x4*)(h + off0 + m * 16 * D_);
  });
  const __amdgpu_buffer_rsrc_t rx = __builtin_amdgcn_make_buffer_rsrc((void*)xbuf, 0, XBUF_BYTES, 0x00020000);
  const int xo = L.wid * 2048 + lane * 16;
#pragma unroll
  for (int i = 0; i < 2; ++i) {
    const f32x4 a = L.acc[2 + i][0], b = L.acc[2 + i][1];
    const u32x4 v = {pk2(a[0], a[1]), pk2(a[2], a[3]), pk2(b[0], b[1]), pk2(b[2], b[3])};
    __builtin_amdgcn_raw_buffer_store_b128(v, rx, bid * 16384 + xo + i * 1024, 0, 16);
  }
  u32x4 rv[2];
#define SL4 0
#define SL5 0
  if (SL4) __builtin_amdgcn_s_sleep(SL4);
  for (int spin = 0; spin < (1 << 22); ++spin) {
    asm volatile("" ::: "memory");
    bool ok = true;
#pragma unroll
    for (int i = 0; i < 2; ++i) rv[i] = __builtin_amdgcn_raw_buffer_load_b128(rx, (bid ^ 1) * 16384 + xo + i * 1024, 0, 16);
#pragma unroll
    for (int i = 0; i < 2; ++i) ok = ok && rv[i][0] != SENT && rv[i][1] != SENT && rv[i][2] != SENT && rv[i][3] != SENT;
    if (__all(ok)) break;
    if (SL5) __builtin_amdgcn_s_sleep(SL5);
  }
#pragma unroll
  for (int m = 0; m < 2; ++m) {
    const u32x4 r = xpose2((u32x2){xr[m][0], xr[m][1]}, (u32x2){xr[m][2], xr[m][3]});
    u32x2 w[2];
#pragma unroll
    for (int j = 0; j < 2; ++j) {
      const f32x4 a = L.acc[m][j];
      w[j].x = pk2(bflo(r[2 * j]) + (a[0] + bflo(rv[m][2 * j])), bfhi(r[2 * j]) + (a[1] + bfhi(rv[m][2 * j])));
      w[j].y = pk2(bflo(r[2 * j + 1]) + (a[2] + bflo(rv[m][2 * j + 1])), bfhi(r[2 * j + 1]) + (a[3] + bfhi(rv[m][2 * j + 1])));
    }
    st16<AUXK(4)>(out, (off0 + m * 16 * D_) * 2, xpose2(w[0], w[1]));
  }
  L.ts();
  STAMP(4, 3);
  L.tsdump<4>();
  asm volatile("k4_code_end:");
}

extern "C" void kernel_launch(void* const* d_in, const int* in_sizes, int n_in, void* d_out, int out_size, void* d_ws,
                              size_t ws_size, hipStream_t stream) {
  const u16* x = (const u16*)d_in[0];
  const u16* wqkv = (const u16*)d_in[1];
  const u16* wo = (const u16*)d_in[2];
  const u16* wg = (const u16*)d_in[4];
  const u16* wu = (const u16*)d_in[5];
  const u16* wd = (const u16*)d_in[6];
  u16* out = (u16*)d_out;
  char* ws = (char*)d_ws;
  u16* o = (u16*)(ws + WS_O);
  u16* h = (u16*)(ws + WS_H);
  u16* act = (u16*)(ws + WS_ACT);
  float* hstat = (float*)(ws + WS_HS);
  unsigned* xbuf = (unsigned*)(ws + WS_X);
  (void)hipFuncSetAttribute((const void*)k1_qkv, hipFuncAttributeMaxDynamicSharedMemorySize, C1::LDS_BYTES);
  (void)hipFuncSetAttribute((const void*)k3_glu, hipFuncAttributeMaxDynamicSharedMemorySize, K3_LDS);
  k1_qkv<<<256, C1::NTHR + 64, C1::LDS_BYTES, stream>>>(x, wqkv, ws, wo, wg, wu);
  k3_glu<<<256, C3::NTHR, K3_LDS, stream>>>(o, wo, x, h, wg, wu, hstat, act, wd, xbuf, out);
}
```
